```python
import torch

def _softmax(x, axis=-1):
    return torch.softmax(x, dim=axis)
def _gelu(x):
    return torch.nn.functional.gelu(x, approximate="tanh")
def _silu(x):
    return torch.nn.functional.silu(x)
def _sigmoid(x):
    return torch.sigmoid(x)
def _layernorm(x, axis=-1, eps=1e-5):
    m = x.mean(dim=axis, keepdim=True)
    v = x.var(dim=axis, keepdim=True, unbiased=False)
    return (x - m) / torch.sqrt(v + eps)
def _scatter_add(tgt, idx, src, axis=0):
    return tgt.clone().index_add_(axis, idx, src)
def _segment_sum(src, seg, n):
    out = torch.zeros((n,) + src.shape[1:], dtype=src.dtype)
    return out.index_add_(0, seg, src)
def _pad(x, pw):
    flat = tuple(v for pair in reversed(pw) for v in pair)
    return torch.nn.functional.pad(x, flat)

kernel_name = 'sequential_preLN__linear_kernel_hedgehog__dense_activation_relu__prompt'

def setup_inputs():

    _w = (lambda a: (lambda t: t.to(torch.bfloat16) if t.is_floating_point() else t)(torch.as_tensor(a)))
    def _A_setup():
        B, S, D, H = 1, 2048, 1024, 32
        Dh = D // H
        return {
            "x": _w((lambda *s: torch.randn(*s, dtype=torch.bfloat16) * 0.05)(B, S, D)),
            "wqkv": _w((lambda *s: torch.randn(*s, dtype=torch.bfloat16) * 0.2)(D, 3 * D)),
            "w_phi": _w((lambda *s: torch.randn(*s, dtype=torch.bfloat16) * 0.2)(H, Dh, Dh) * (Dh ** -0.5)),
            "wo": _w((lambda *s: torch.randn(*s, dtype=torch.bfloat16) * 0.05)(D, D)),
        }
    _A_in = _A_setup()

    def _B_setup():
        B, S, D, Dff = 1, 2048, 1024, 3584
        return {
            "x": _w((lambda *s: torch.randn(*s, dtype=torch.bfloat16) * 0.05)(B, S, D)),
            "w1": _w((lambda *s: torch.randn(*s, dtype=torch.bfloat16) * 0.05)(D, Dff)),
            "w2": _w((lambda *s: torch.randn(*s, dtype=torch.bfloat16) * 0.05)(Dff, D)),
        }
    _B_in = _B_setup()

    return {
        "A_x": _A_in["x"],
        "A_wqkv": _A_in["wqkv"],
        "A_w_phi": _A_in["w_phi"],
        "A_wo": _A_in["wo"],
        "B_x": _B_in["x"],
        "B_w1": _B_in["w1"],
        "B_w2": _B_in["w2"],
    }

def reference(A_x, A_wqkv, A_w_phi, A_wo, B_x, B_w1, B_w2):
    def _A_ref(x, wqkv, w_phi, wo):
        B, S, D = x.shape
        H = 32; Dh = D // H
        qkv = (lambda x, *d: x.permute(*d))((x @ wqkv).reshape(B, S, 3, H, Dh), 2, 0, 3, 1, 4)
        q, k, v = qkv[0], qkv[1], qkv[2]
        pq = torch.einsum("bhsd,hdf->bhsf", q, w_phi)
        pk = torch.einsum("bhsd,hdf->bhsf", k, w_phi)
        phi_q = torch.concatenate([_softmax(pq, axis=-1), _softmax(-pq, axis=-1)], axis=-1)
        phi_k = torch.concatenate([_softmax(pk, axis=-1), _softmax(-pk, axis=-1)], axis=-1)
        kv = torch.einsum("bhsf,bhsd->bhfd", phi_k, v)
        z  = torch.einsum("bhsf->bhf", phi_k)
        num = torch.einsum("bhqf,bhfd->bhqd", phi_q, kv)
        den = torch.einsum("bhqf,bhf->bhq", phi_q, z)[..., None] + 1e-6
        o = num / den
        return (lambda x, *d: x.permute(*d))(o, 0, 2, 1, 3).reshape(B, S, D) @ wo

    def _B_ref(x, w1, w2):
        h = torch.einsum("bsd,df->bsf", x, w1)
        zero = torch.zeros_like(h)
        h = torch.maximum(h, zero)
        return torch.einsum("bsf,fd->bsd", h, w2)

    x = A_x
    h = x + _A_ref(_layernorm(x, axis=-1), A_wqkv, A_w_phi, A_wo)
    h2 = h + _B_ref(_layernorm(h, axis=-1), B_w1, B_w2)
    return h2
```

```cpp
#include <hip/hip_runtime.h>
#include <hip/hip_bf16.h>

#define LAS __attribute__((address_space(3)))
#define DI __device__ __forceinline__
typedef __bf16 bf16x8 __attribute__((ext_vector_type(8)));
typedef __bf16 bf16x2 __attribute__((ext_vector_type(2)));
typedef short s16x4 __attribute__((ext_vector_type(4)));
typedef short s16x8 __attribute__((ext_vector_type(8)));
typedef float f32x2 __attribute__((ext_vector_type(2)));
typedef float f32x4 __attribute__((ext_vector_type(4)));
typedef float f32x16 __attribute__((ext_vector_type(16)));
typedef unsigned u32x2 __attribute__((ext_vector_type(2)));
typedef unsigned u32x4 __attribute__((ext_vector_type(4)));
typedef unsigned short u16;

constexpr int S_ = 2048, D_ = 1024, H_ = 32, FF_ = 3584;
constexpr float LN_EPS = 1e-5f;

constexpr size_t WS_CNT = 0;
constexpr size_t WS_XN = 8192;
constexpr size_t WS_O = WS_XN + (size_t)S_ * D_ * 2;
constexpr size_t WS_H = WS_O + (size_t)S_ * D_ * 2;
constexpr size_t WS_HN = WS_H + (size_t)S_ * D_ * 2;
constexpr size_t WS_H1 = WS_HN + (size_t)S_ * D_ * 2;
constexpr size_t WS_KVP = WS_H1 + (size_t)S_ * FF_ * 2;
constexpr int KVP_HEAD = 2112;
constexpr int KVP_BLK = 2 * KVP_HEAD;

#if defined(__HIP_DEVICE_COMPILE__)
typedef __amdgpu_buffer_rsrc_t rsrc_t;
template <int N> DI void wait_vm() { asm volatile("s_waitcnt vmcnt(%0)" ::"n"(N) : "memory"); }
DI void wait_lgkm0() { asm volatile("s_waitcnt lgkmcnt(0)" ::: "memory"); }
DI void bar() { asm volatile("" ::: "memory"); __builtin_amdgcn_s_barrier(); asm volatile("" ::: "memory"); }
DI unsigned pk2(float a, float b) { f32x2 v = {a, b}; return __builtin_bit_cast(unsigned, __builtin_convertvector(v, bf16x2)); }
DI float bflo(unsigned u) { return __uint_as_float(u << 16); }
DI float bfhi(unsigned u) { return __uint_as_float(u & 0xffff0000u); }
DI bf16x8 ld_b128(LAS const char* p) { return *(LAS const bf16x8*)p; }
DI bf16x8 ld_tr2(LAS const char* p0, LAS const char* p1) {
  s16x4 a = __builtin_amdgcn_ds_read_tr16_b64_v4i16((LAS s16x4*)p0);
  s16x4 b = __builtin_amdgcn_ds_read_tr16_b64_v4i16((LAS s16x4*)p1);
  return __builtin_bit_cast(bf16x8, __builtin_shufflevector(a, b, 0, 1, 2, 3, 4, 5, 6, 7));
}
DI bf16x8 pack8(const f32x16& x, int s) {
  u32x4 p = {pk2(x[8 * s], x[8 * s + 1]), pk2(x[8 * s + 2], x[8 * s + 3]), pk2(x[8 * s + 4], x[8 * s + 5]), pk2(x[8 * s + 6], x[8 * s + 7])};
  return __builtin_bit_cast(bf16x8, p);
}
DI float xhalf_max(float v) { auto r = __builtin_amdgcn_permlane32_swap(__float_as_uint(v), __float_as_uint(v), false, false); return fmaxf(__uint_as_float(r[0]), __uint_as_float(r[1])); }
DI float xhalf_min(float v) { auto r = __builtin_amdgcn_permlane32_swap(__float_as_uint(v), __float_as_uint(v), false, false); return fminf(__uint_as_float(r[0]), __uint_as_float(r[1])); }
DI float xhalf_sum(float v) { auto r = __builtin_amdgcn_permlane32_swap(__float_as_uint(v), __float_as_uint(v), false, false); return __uint_as_float(r[0]) + __uint_as_float(r[1]); }
#define MFMA32(a, b, c) __builtin_amdgcn_mfma_f32_32x32x16_bf16((a), (b), (c), 0, 0, 0)
#define MFMA16(a, b, c) __builtin_amdgcn_mfma_f32_16x16x32_bf16((a), (b), (c), 0, 0, 0)

template <int BN> DI constexpr int bswz(int k) {
  return BN == 224 ? (((k >> 3) & 1) << 1)
       : BN == 64  ? ((((k >> 1) & 1) | (((k >> 3) & 1) << 1)) << 1)
       : BN == 128 ? (((k & 3) | (((k >> 3) & 1) << 2)) << 1)
                   : (((k >> 1) & 1) << 2);
}

template <int BN, int NS, int NT, bool QKV>
DI void loader_loop(LAS char* smem, rsrc_t ra, rsrc_t rb, int a_off, int lda_b, int b_off, int ldb_b, int lw, int lane) {
  constexpr int AI = 16, BI = BN / 8, TI = AI + BI, PW = TI / 4, STAGE = TI * 1024, CPR = BN / 8;
  static_assert(TI % 4 == 0 && AI % 4 == 0, "");
  int voff[PW];
#pragma unroll
  for (int i = 0; i < PW; ++i) {
    const int ins = lw + i * 4;
    if (i < AI / 4) {
      const int r = ins * 8 + (lane >> 3), c = (lane & 7) ^ ((r >> 1) & 7);
      voff[i] = a_off + r * lda_b + c * 16;
    } else {
      const int ch = (ins - AI) * 64 + lane, k = ch / CPR, c = (ch % CPR) ^ bswz<BN>(k);
      voff[i] = b_off + k * ldb_b + (QKV ? ((c >> 3) * 2048 + (c & 7) * 16) : c * 16);
    }
  }
  auto issue = [&](int t, int slot) {
#pragma unroll
    for (int i = 0; i < PW; ++i) {
      LAS char* dst = smem + slot * STAGE + (lw + i * 4) * 1024;
      if (i < AI / 4) __builtin_amdgcn_raw_ptr_buffer_load_lds(ra, dst, 16, voff[i], t * 128, 0, 0);
      else __builtin_amdgcn_raw_ptr_buffer_load_lds(rb, dst, 16, voff[i], t * 64 * ldb_b, 0, 0);
    }
  };
#pragma unroll
  for (int t = 0; t < NS - 1; ++t) issue(t, t);
  int slot = NS - 1;
  for (int t = 0; t < NT - (NS - 1); ++t) {
    wait_vm<(NS - 2) * PW>();
    bar();
    issue(t + NS - 1, slot);
    slot = (slot == NS - 1) ? 0 : slot + 1;
  }
  if constexpr (NS >= 7) { wait_vm<5 * PW>(); bar(); }
  if constexpr (NS >= 6) { wait_vm<4 * PW>(); bar(); }
  if constexpr (NS >= 5) { wait_vm<3 * PW>(); bar(); }
  if constexpr (NS >= 4) { wait_vm<2 * PW>(); bar(); }
  if constexpr (NS >= 3) { wait_vm<1 * PW>(); bar(); }
  wait_vm<0>(); bar();
}
#endif

__global__ __launch_bounds__(512) void ln_kernel(const u16* __restrict__ x, u16* __restrict__ y, unsigned* cnt) {
#if defined(__HIP_DEVICE_COMPILE__)
  const int lane = threadIdx.x & 63, row = blockIdx.x * 8 + (threadIdx.x >> 6);
  if (cnt != nullptr && blockIdx.x == 0 && threadIdx.x < 16) cnt[threadIdx.x * 64] = 0u;
  const u32x4* p = (const u32x4*)(x + (size_t)row * D_);
  u32x4 v0 = p[lane], v1 = p[lane + 64];
  float f[16];
#pragma unroll
  for (int i = 0; i < 4; ++i) { f[2 * i] = bflo(v0[i]); f[2 * i + 1] = bfhi(v0[i]); f[8 + 2 * i] = bflo(v1[i]); f[8 + 2 * i + 1] = bfhi(v1[i]); }
  float s = 0.f;
#pragma unroll
  for (int i = 0; i < 16; ++i) s += f[i];
#pragma unroll
  for (int m = 1; m < 64; m <<= 1) s += __shfl_xor(s, m);
  const float mean = s * (1.0f / D_);
  float q = 0.f;
#pragma unroll
  for (int i = 0; i < 16; ++i) { f[i] -= mean; q += f[i] * f[i]; }
#pragma unroll
  for (int m = 1; m < 64; m <<= 1) q += __shfl_xor(q, m);
  const float rstd = 1.0f / sqrtf(q * (1.0f / D_) + LN_EPS);
  u32x4 o0, o1;
#pragma unroll
  for (int i = 0; i < 4; ++i) { o0[i] = pk2(f[2 * i] * rstd, f[2 * i + 1] * rstd); o1[i] = pk2(f[8 + 2 * i] * rstd, f[8 + 2 * i + 1] * rstd); }
  u32x4* op = (u32x4*)(y + (size_t)row * D_);
  op[lane] = o0; op[lane + 64] = o1;
#endif
}

template <int BN, int NS, int K, int N, int EPI, int XA  >
__global__ __launch_bounds__(512, 2) void gemm_kernel(const u16* __restrict__ A, const u16* __restrict__ B, const u16* __restrict__ R, u16* __restrict__ C) {
#if defined(__HIP_DEVICE_COMPILE__)
  extern __shared__ __attribute__((aligned(16))) char smem_[];
  LAS char* smem = (LAS char*)smem_;
  constexpr int NT = K / 64, TN = BN / 32, RS = BN * 2, STAGE = (16 + BN / 8) * 1024;
  const int tid = threadIdx.x, lane = tid & 63, wid = __builtin_amdgcn_readfirstlane(tid >> 6);
  const int xcd = blockIdx.x & 7, loc = blockIdx.x >> 3;
  constexpr int GA = 16 / XA;
  const int tm = (xcd % GA) * XA + (loc % XA), tn = (xcd / GA) * (32 / XA) + (loc / XA);
  if (wid >= 4) {
    rsrc_t ra = __builtin_amdgcn_make_buffer_rsrc((void*)A, 0, S_ * K * 2, 0x00020000);
    rsrc_t rb = __builtin_amdgcn_make_buffer_rsrc((void*)B, 0, K * N * 2, 0x00020000);
    loader_loop<BN, NS, NT, false>(smem, ra, rb, tm * 128 * K * 2, K * 2, tn * BN * 2, N * 2, wid - 4, lane);
    return;
  }
  const int wm = wid >> 1, wn = wid & 1;
  const int r16 = lane & 15, g = lane >> 4, q = (lane & 15) >> 2, p = lane & 3;
  const int orow = tm * 128 + wm * 64 + r16;
  const int ocol = tn * BN + wn * (BN / 2) + (g & 1) * 16 + (g >> 1) * 8;
  u32x4 res[4][(TN + 1) / 2];
  if constexpr (EPI == 0) {
#pragma unroll
    for (int i = 0; i < 4; ++i)
#pragma unroll
      for (int j = 0; j < TN / 2; ++j) res[i][j] = *(const u32x4*)(R + (size_t)(orow + i * 16) * N + ocol + j * 32);
  }
  int aoff[2], boff[TN];
#pragma unroll
  for (int s = 0; s < 2; ++s) aoff[s] = (wm * 64 + r16) * 128 + (((4 * s + g) ^ (r16 >> 1)) * 16);
#pragma unroll
  for (int j = 0; j < TN; ++j) boff[j] = 16384 + (8 * g + q) * RS + (((2 * (wn * TN + j) + (p >> 1)) ^ bswz<BN>(8 * g + q)) * 16) + 8 * (p & 1);
  f32x4 acc[4][TN];
#pragma unroll
  for (int i = 0; i < 4; ++i)
#pragma unroll
    for (int j = 0; j < TN; ++j) acc[i][j] = (f32x4){0.f, 0.f, 0.f, 0.f};
  bf16x8 a0[4], b0[TN], a1[4], b1[TN];
  auto ldf = [&](bf16x8(&a)[4], bf16x8(&b)[TN], int sb, int s) {
#pragma unroll
    for (int j = 0; j < TN; ++j) b[j] = ld_tr2(smem + sb + boff[j] + (32 * s) * RS, smem + sb + boff[j] + (32 * s + 4) * RS);
#pragma unroll
    for (int i = 0; i < 4; ++i) a[i] = ld_b128(smem + sb + aoff[s] + i * 2048);
  };
  auto mma = [&](bf16x8(&a)[4], bf16x8(&b)[TN]) {
#pragma unroll
    for (int i = 0; i < 4; ++i)
#pragma unroll
      for (int j = 0; j < TN; ++j) acc[i][j] = MFMA16(b[j], a[i], acc[i][j]);
  };
  auto sched = [&]() {
    constexpr int NL = 4 + 2 * TN, NM = 4 * TN;
    constexpr int PER = NL <= NM ? 1 : 2;
#pragma unroll
    for (int i = 0; i < NM; ++i) {
      __builtin_amdgcn_sched_group_barrier(0x8, 1, 0);
      if (i * PER < NL) __builtin_amdgcn_sched_group_barrier(0x100, PER, 0);
    }
  };
  bar();
  int sb = 0;
  ldf(a0, b0, 0, 0);
  for (int t = 0; t < NT - 1; ++t) {
    ldf(a1, b1, sb, 1);
    mma(a0, b0);
    sched();
    __builtin_amdgcn_sched_barrier(0);
    wait_lgkm0();
    bar();
    sb = (sb == (NS - 1) * STAGE) ? 0 : sb + STAGE;
    ldf(a0, b0, sb, 0);
    mma(a1, b1);
    sched();
    __builtin_amdgcn_sched_barrier(0);
  }
  ldf(a1, b1, sb, 1);
  mma(a0, b0);
  sched();
  __builtin_amdgcn_sched_barrier(0);
  mma(a1, b1);
#pragma unroll
  for (int i = 0; i < 4; ++i) {
    u16* crow = C + (size_t)(orow + i * 16) * N;
#pragma unroll
    for (int j = 0; j < TN / 2; ++j) {
      f32x4 x = acc[i][2 * j], y = acc[i][2 * j + 1];
#pragma unroll
      for (int e = 0; e < 4; ++e) {
        auto r = __builtin_amdgcn_permlane16_swap(__float_as_uint(x[e]), __float_as_uint(y[e]), false, false);
        x[e] = __uint_as_float(r[0]); y[e] = __uint_as_float(r[1]);
      }
      if constexpr (EPI == 0) {
        const u32x4 rv = res[i][j];
        x[0] += bflo(rv[0]); x[1] += bfhi(rv[0]); x[2] += bflo(rv[1]); x[3] += bfhi(rv[1]);
        y[0] += bflo(rv[2]); y[1] += bfhi(rv[2]); y[2] += bflo(rv[3]); y[3] += bfhi(rv[3]);
      } else {
#pragma unroll
        for (int e = 0; e < 4; ++e) { x[e] = fmaxf(x[e], 0.f); y[e] = fmaxf(y[e], 0.f); }
      }
      u32x4 o = {pk2(x[0], x[1]), pk2(x[2], x[3]), pk2(y[0], y[1]), pk2(y[2], y[3])};
      *(u32x4*)(crow + ocol + j * 32) = o;
    }
    if constexpr (TN & 1) {
      static_assert(EPI == 1 || !(TN & 1), "");
      f32x4 x = acc[i][TN - 1];
      u32x2 o = {pk2(fmaxf(x[0], 0.f), fmaxf(x[1], 0.f)), pk2(fmaxf(x[2], 0.f), fmaxf(x[3], 0.f))};
      *(u32x2*)(crow + tn * BN + wn * (BN / 2) + (TN - 1) * 16 + g * 4) = o;
    }
  }
#endif
}

#if defined(__HIP_DEVICE_COMPILE__)
DI void dual_softmax(const f32x16& pv, f32x16& e1, f32x16& e2) {
  constexpr float L2E = 1.4426950408889634f;
  float mx = pv[0], mn = pv[0];
#pragma unroll
  for (int r = 1; r < 16; ++r) { mx = fmaxf(mx, pv[r]); mn = fminf(mn, pv[r]); }
  mx = xhalf_max(mx); mn = xhalf_min(mn);
  const float a = -mx * L2E, b = mn * L2E;
  float s1 = 0.f, s2 = 0.f;
#pragma unroll
  for (int r = 0; r < 16; ++r) {
    e1[r] = __builtin_amdgcn_exp2f(fmaf(pv[r], L2E, a));
    e2[r] = __builtin_amdgcn_exp2f(fmaf(pv[r], -L2E, b));
    s1 += e1[r]; s2 += e2[r];
  }
  s1 = xhalf_sum(s1); s2 = xhalf_sum(s2);
  const float r1 = 1.0f / s1, r2 = 1.0f / s2;
#pragma unroll
  for (int r = 0; r < 16; ++r) { e1[r] *= r1; e2[r] *= r2; }
}
#endif

constexpr int QKV_NS = 3, QKV_STAGE = 40 * 1024;
__global__ __launch_bounds__(512, 2) void qkv_attn_kernel(const u16* __restrict__ XN, const u16* __restrict__ W, const u16* __restrict__ WPHI,
                                                           float* KVP, unsigned* cnt, u16* __restrict__ O) {
#if defined(__HIP_DEVICE_COMPILE__)
  extern __shared__ __attribute__((aligned(16))) char smem_[];
  LAS char* smem = (LAS char*)smem_;
  constexpr int NT = 16, NS = QKV_NS, STAGE = QKV_STAGE, RS = 384;
  const int tid = threadIdx.x, lane = tid & 63, wid = __builtin_amdgcn_readfirstlane(tid >> 6);
  const int xcd = blockIdx.x & 7, loc = blockIdx.x >> 3;
  const int tm = (xcd & 1) * 8 + (loc & 7), gp = (xcd >> 1) * 4 + (loc >> 3);
  constexpr int L_PHI = 0;
  constexpr int L_RED = 4 * 64 * 136;
  constexpr int L_SUM = L_RED + KVP_BLK * 4;
  const int wm = (wid >> 1) & 1, hh = wid & 1;
  const int r32 = lane & 31, h = lane >> 5, blk = (lane >> 4) & 1, q = (lane & 15) >> 2, p = lane & 3;
  bf16x8 pf[2][2][2];
  float* kvp_blk = KVP + (size_t)(gp * 16 + tm) * KVP_BLK;
  if (wid >= 4) {
    rsrc_t ra = __builtin_amdgcn_make_buffer_rsrc((void*)XN, 0, S_ * D_ * 2, 0x00020000);
    rsrc_t rb = __builtin_amdgcn_make_buffer_rsrc((void*)W, 0, D_ * 3 * D_ * 2, 0x00020000);
    loader_loop<192, NS, NT, true>(smem, ra, rb, tm * 128 * D_ * 2, D_ * 2, gp * 128, 3 * D_ * 2, wid - 4, lane);
    bar();
    bar();
  } else {
    const int head = gp * 2 + hh;
    bf16x8 wf[2];
#pragma unroll
    for (int s = 0; s < 2; ++s) {
      s16x8 w;
#pragma unroll
      for (int j = 0; j < 8; ++j) w[j] = (short)WPHI[head * 1024 + (16 * s + 8 * (j >> 2) + 4 * h + (j & 3)) * 32 + r32];
      wf[s] = __builtin_bit_cast(bf16x8, w);
    }
    int aoff[4];
#pragma unroll
    for (int s = 0; s < 4; ++s) aoff[s] = (wm * 64 + r32) * 128 + (((2 * s + h) ^ ((r32 >> 1) & 7)) * 16);
    const int boff = 16384 + (8 * h + q) * RS + (((hh * 4 + blk * 2 + (p >> 1)) ^ bswz<192>(q)) * 16) + 8 * (p & 1);
    f32x16 aq[2], ak[2], av[2];
#pragma unroll
    for (int i = 0; i < 2; ++i)
#pragma unroll
      for (int r = 0; r < 16; ++r) { aq[i][r] = 0.f; ak[i][r] = 0.f; av[i][r] = 0.f; }
    bf16x8 a0[2], b0[3], a1[2], b1[3];
    auto ldf = [&](bf16x8(&a)[2], bf16x8(&b)[3], int sb, int s) {
#pragma unroll
      for (int j = 0; j < 3; ++j) b[j] = ld_tr2(smem + sb + boff + (16 * s) * RS + j * 128, smem + sb + boff + (16 * s + 4) * RS + j * 128);
#pragma unroll
      for (int i = 0; i < 2; ++i) a[i] = ld_b128(smem + sb + aoff[s] + i * 4096);
    };
    auto mma = [&](bf16x8(&a)[2], bf16x8(&b)[3]) {
#pragma unroll
      for (int i = 0; i < 2; ++i) {
        aq[i] = MFMA32(b[0], a[i], aq[i]);
        ak[i] = MFMA32(b[1], a[i], ak[i]);
        av[i] = MFMA32(a[i], b[2], av[i]);
      }
    };
    auto sched = [&]() {
#pragma unroll
      for (int i = 0; i < 6; ++i) {
        __builtin_amdgcn_sched_group_barrier(0x8, 1, 0);
        if (i < 4) __builtin_amdgcn_sched_group_barrier(0x100, 2, 0);
      }
      __builtin_amdgcn_sched_barrier(0);
    };
    bar();
    int sb = 0;
    ldf(a0, b0, 0, 0);
    for (int t = 0; t < NT; ++t) {
      ldf(a1, b1, sb, 1); mma(a0, b0); sched();
      ldf(a0, b0, sb, 2); mma(a1, b1); sched();
      ldf(a1, b1, sb, 3); mma(a0, b0); sched();
      wait_lgkm0();
      bar();
      sb = (sb == (NS - 1) * STAGE) ? 0 : sb + STAGE;
      if (t + 1 < NT) ldf(a0, b0, sb, 0);
      mma(a1, b1); sched();
    }
    LAS char* phis = smem + L_PHI + wid * (64 * 136);
    f32x16 kv[2], zz[2];
#pragma unroll
    for (int r = 0; r < 16; ++r) { kv[0][r] = 0.f; kv[1][r] = 0.f; zz[0][r] = 0.f; zz[1][r] = 0.f; }
    const s16x8 one_ = {0x3f80, 0x3f80, 0x3f80, 0x3f80, 0x3f80, 0x3f80, 0x3f80, 0x3f80};
    const bf16x8 ones = __builtin_bit_cast(bf16x8, one_);
#pragma unroll
    for (int i = 0; i < 2; ++i) {
      f32x16 pk;
#pragma unroll
      for (int r = 0; r < 16; ++r) pk[r] = 0.f;
      pk = MFMA32(wf[0], pack8(ak[i], 0), pk);
      pk = MFMA32(wf[1], pack8(ak[i], 1), pk);
      f32x16 e1, e2;
      dual_softmax(pk, e1, e2);
      LAS char* wr = phis + (i * 32 + r32) * 136 + h * 8;
#pragma unroll
      for (int gq = 0; gq < 4; ++gq) {
        *(LAS u32x2*)(wr + gq * 16) = (u32x2){pk2(e1[4 * gq], e1[4 * gq + 1]), pk2(e1[4 * gq + 2], e1[4 * gq + 3])};
        *(LAS u32x2*)(wr + 64 + gq * 16) = (u32x2){pk2(e2[4 * gq], e2[4 * gq + 1]), pk2(e2[4 * gq + 2], e2[4 * gq + 3])};
      }
    }
#pragma unroll
    for (int i = 0; i < 2; ++i)
#pragma unroll
      for (int st = 0; st < 2; ++st) {
        const bf16x8 vf = pack8(av[i], st);
#pragma unroll
        for (int ft = 0; ft < 2; ++ft) {
          LAS const char* rd = phis + (i * 32 + 16 * st + 4 * h + q) * 136 + ft * 64 + blk * 32 + p * 8;
          const bf16x8 pa = ld_tr2(rd, rd + 8 * 136);
          kv[ft] = MFMA32(pa, vf, kv[ft]);
          zz[ft] = MFMA32(pa, ones, zz[ft]);
        }
      }
    LAS float* red = (LAS float*)(smem + L_RED) + hh * KVP_HEAD;
    if (wm == 1) {
#pragma unroll
      for (int ft = 0; ft < 2; ++ft)
#pragma unroll
        for (int gq = 0; gq < 4; ++gq) {
          *(LAS f32x4*)(red + ((ft * 4 + gq) * 64 + lane) * 4) = (f32x4){kv[ft][4 * gq], kv[ft][4 * gq + 1], kv[ft][4 * gq + 2], kv[ft][4 * gq + 3]};
          if (r32 == 0) *(LAS f32x4*)(red + 2048 + (h * 2 + ft) * 16 + gq * 4) = (f32x4){zz[ft][4 * gq], zz[ft][4 * gq + 1], zz[ft][4 * gq + 2], zz[ft][4 * gq + 3]};
        }
    }
    wait_lgkm0();
    bar();
    if (wm == 0) {
      rsrc_t rk = __builtin_amdgcn_make_buffer_rsrc((void*)(kvp_blk + hh * KVP_HEAD), 0, KVP_HEAD * 4, 0x00020000);
#pragma unroll
      for (int ft = 0; ft < 2; ++ft)
#pragma unroll
        for (int gq = 0; gq < 4; ++gq) {
          f32x4 o = *(LAS f32x4*)(red + ((ft * 4 + gq) * 64 + lane) * 4);
          o += (f32x4){kv[ft][4 * gq], kv[ft][4 * gq + 1], kv[ft][4 * gq + 2], kv[ft][4 * gq + 3]};
          __builtin_amdgcn_raw_buffer_store_b128(__builtin_bit_cast(u32x4, o), rk, ((ft * 4 + gq) * 64 + lane) * 16, 0, 16);
          if (r32 == 0) {
            f32x4 z = *(LAS f32x4*)(red + 2048 + (h * 2 + ft) * 16 + gq * 4);
            z += (f32x4){zz[ft][4 * gq], zz[ft][4 * gq + 1], zz[ft][4 * gq + 2], zz[ft][4 * gq + 3]};
            __builtin_amdgcn_raw_buffer_store_b128(__builtin_bit_cast(u32x4, z), rk, (2048 + (h * 2 + ft) * 16 + gq * 4) * 4, 0, 16);
          }
        }
      wait_vm<0>();
      if (lane == 0) __hip_atomic_fetch_add(cnt + gp * 64, 1u, __ATOMIC_RELAXED, __HIP_MEMORY_SCOPE_AGENT);
    }
#pragma unroll
    for (int i = 0; i < 2; ++i) {
      f32x16 pq;
#pragma unroll
      for (int r = 0; r < 16; ++r) pq[r] = 0.f;
      pq = MFMA32(wf[0], pack8(aq[i], 0), pq);
      pq = MFMA32(wf[1], pack8(aq[i], 1), pq);
      f32x16 e1, e2;
      dual_softmax(pq, e1, e2);
      pf[i][0][0] = pack8(e1, 0); pf[i][0][1] = pack8(e1, 1);
      pf[i][1][0] = pack8(e2, 0); pf[i][1][1] = pack8(e2, 1);
    }
  }
  if (wid == 4) {
    unsigned spins = 0;
    while (__builtin_amdgcn_readfirstlane(__hip_atomic_load(cnt + gp * 64, __ATOMIC_RELAXED, __HIP_MEMORY_SCOPE_AGENT)) < 32u) {
      __builtin_amdgcn_s_sleep(1);
      if (++spins > (1u << 22)) break;
    }
    __builtin_amdgcn_fence(__ATOMIC_ACQUIRE, "agent");
    wait_vm<0>();
  }
  bar();
  {
    rsrc_t rk = __builtin_amdgcn_make_buffer_rsrc((void*)(KVP + (size_t)gp * 16 * KVP_BLK), 0, 16 * KVP_BLK * 4, 0x00020000);
#pragma unroll
    for (int it = 0; it < 3; ++it) {
      const int v4 = it * 512 + tid;
      if (it < 2 || v4 < KVP_BLK / 4) {
        u32x4 t[16];
#pragma unroll
        for (int j = 0; j < 16; ++j) t[j] = __builtin_amdgcn_raw_buffer_load_b128(rk, v4 * 16, j * KVP_BLK * 4, 16);
        f32x4 s = __builtin_bit_cast(f32x4, t[0]);
#pragma unroll
        for (int j = 1; j < 16; ++j) s += __builtin_bit_cast(f32x4, t[j]);
        *(LAS f32x4*)(smem + L_SUM + v4 * 16) = s;
      }
    }
  }
  wait_lgkm0();
  bar();
  if (wid < 4) {
    LAS const float* tot = (LAS const float*)(smem + L_SUM) + hh * KVP_HEAD;
    bf16x8 ka[2][2], za[2][2];
#pragma unroll
    for (int ft = 0; ft < 2; ++ft)
#pragma unroll
      for (int s = 0; s < 2; ++s) {
        const f32x4 x0 = *(LAS const f32x4*)(tot + ((ft * 4 + 2 * s) * 64 + lane) * 4), x1 = *(LAS const f32x4*)(tot + ((ft * 4 + 2 * s + 1) * 64 + lane) * 4);
        ka[ft][s] = __builtin_bit_cast(bf16x8, (u32x4){pk2(x0[0], x0[1]), pk2(x0[2], x0[3]), pk2(x1[0], x1[1]), pk2(x1[2], x1[3])});
        const f32x4 z0 = *(LAS const f32x4*)(tot + 2048 + (h * 2 + ft) * 16 + 8 * s), z1 = *(LAS const f32x4*)(tot + 2048 + (h * 2 + ft) * 16 + 8 * s + 4);
        za[ft][s] = __builtin_bit_cast(bf16x8, (u32x4){pk2(z0[0], z0[1]), pk2(z0[2], z0[3]), pk2(z1[0], z1[1]), pk2(z1[2], z1[3])});
      }
#pragma unroll
    for (int i = 0; i < 2; ++i) {
      f32x16 o, dn;
#pragma unroll
      for (int r = 0; r < 16; ++r) { o[r] = 0.f; dn[r] = 0.f; }
#pragma unroll
      for (int ft = 0; ft < 2; ++ft)
#pragma unroll
        for (int s = 0; s < 2; ++s) { o = MFMA32(ka[ft][s], pf[i][ft][s], o); dn = MFMA32(za[ft][s], pf[i][ft][s], dn); }
      const float rd = 1.0f / (dn[0] + 1e-6f);
      u16* orow = O + (size_t)(tm * 128 + wm * 64 + i * 32 + r32) * D_ + (gp * 2 + hh) * 32 + h * 8;
#pragma unroll
      for (int gq = 0; gq < 4; gq += 2) {
        unsigned a0_ = pk2(o[4 * gq] * rd, o[4 * gq + 1] * rd), a1_ = pk2(o[4 * gq + 2] * rd, o[4 * gq + 3] * rd);
        unsigned b0_ = pk2(o[4 * gq + 4] * rd, o[4 * gq + 5] * rd), b1_ = pk2(o[4 * gq + 6] * rd, o[4 * gq + 7] * rd);
        auto r0 = __builtin_amdgcn_permlane32_swap(a0_, b0_, false, false);
        auto r1 = __builtin_amdgcn_permlane32_swap(a1_, b1_, false, false);
        *(u32x4*)(orow + gq * 8) = (u32x4){r0[0], r1[0], r0[1], r1[1]};
      }
    }
  }
#endif
}

extern "C" void kernel_launch(void* const* d_in, const int* in_sizes, int n_in, void* d_out, int out_size, void* d_ws, size_t ws_size,
                              hipStream_t stream) {
  const u16* x = (const u16*)d_in[0];
  const u16* wqkv = (const u16*)d_in[1];
  const u16* wphi = (const u16*)d_in[2];
  const u16* wo = (const u16*)d_in[3];
  const u16* w1 = (const u16*)d_in[5];
  const u16* w2 = (const u16*)d_in[6];
  char* ws = (char*)d_ws;
  unsigned* cnt = (unsigned*)(ws + WS_CNT);
  u16* xn = (u16*)(ws + WS_XN);
  u16* o = (u16*)(ws + WS_O);
  u16* hbuf = (u16*)(ws + WS_H);
  u16* hn = (u16*)(ws + WS_HN);
  u16* h1 = (u16*)(ws + WS_H1);
  float* kvp = (float*)(ws + WS_KVP);
  u16* out = (u16*)d_out;

  auto g2 = gemm_kernel<64, 6, 1024, 1024, 0, 4>;
  auto g3 = gemm_kernel<224, 3, 1024, 3584, 1, 8>;
  auto g4 = gemm_kernel<64, 6, 3584, 1024, 0, 4>;
  constexpr int L1 = QKV_NS * QKV_STAGE, L2 = 6 * 24 * 1024, L3 = 3 * 44 * 1024;
  hipFuncSetAttribute((const void*)qkv_attn_kernel, hipFuncAttributeMaxDynamicSharedMemorySize, L1);
  hipFuncSetAttribute((const void*)g2, hipFuncAttributeMaxDynamicSharedMemorySize, L2);
  hipFuncSetAttribute((const void*)g3, hipFuncAttributeMaxDynamicSharedMemorySize, L3);
  hipFuncSetAttribute((const void*)g4, hipFuncAttributeMaxDynamicSharedMemorySize, L2);

  ln_kernel<<<256, 512, 0, stream>>>(x, xn, cnt);
  qkv_attn_kernel<<<256, 512, L1, stream>>>(xn, wqkv, wphi, kvp, cnt, o);
  g2<<<256, 512, L2, stream>>>(o, wo, x, hbuf);
  ln_kernel<<<256, 512, 0, stream>>>(hbuf, hn, nullptr);
  g3<<<256, 512, L3, stream>>>(hn, w1, nullptr, h1);
  g4<<<256, 512, L2, stream>>>(h1, w2, hbuf, out);
}
```

```cpp
#include <hip/hip_runtime.h>
#include <hip/hip_bf16.h>

#define LAS __attribute__((address_space(3)))
#define GAS __attribute__((address_space(1)))
#define DI __device__ __forceinline__
typedef __bf16 bf16x8 __attribute__((ext_vector_type(8)));
typedef __bf16 bf16x2 __attribute__((ext_vector_type(2)));
typedef short s16x4 __attribute__((ext_vector_type(4)));
typedef short s16x8 __attribute__((ext_vector_type(8)));
typedef float f32x2 __attribute__((ext_vector_type(2)));
typedef float f32x4 __attribute__((ext_vector_type(4)));
typedef float f32x16 __attribute__((ext_vector_type(16)));
typedef unsigned u32x2 __attribute__((ext_vector_type(2)));
typedef unsigned u32x4 __attribute__((ext_vector_type(4)));
typedef unsigned short u16;

constexpr int S_ = 2048, D_ = 1024, FF_ = 3584;
constexpr float LN_EPS = 1e-5f;

constexpr int C_EPOCH = 0, C_TMO = 1, C_ST = 2, C_CS1 = 18, C_CS3 = 34, C_KV = 50, C_O = 66, C_H = 82, C_H1 = 98, C_P4 = 114, C_LINES = 130;
constexpr unsigned WS_XST = 36864;
constexpr unsigned WS_CS1 = WS_XST + S_ * 8;
constexpr unsigned WS_CS3 = WS_CS1 + 16 * 16 * 192 * 4;
constexpr unsigned WS_HST = WS_CS3 + 16 * 16 * 224 * 4;
constexpr unsigned KVP_BYTES = 8192 + 512;
constexpr unsigned WS_KVP = WS_HST + S_ * 16 * 8;
#define BLOCKED 1
constexpr unsigned WS_O = WS_KVP + 256 * KVP_BYTES;
constexpr unsigned WS_H = WS_O + S_ * D_ * 2;
constexpr unsigned WS_H1 = WS_H + S_ * D_ * 2;
constexpr unsigned WS_P4 = WS_H1 + S_ * FF_ * 2;
constexpr unsigned WS_END = WS_P4 + 256 * 32768;
static_assert(C_LINES * 256 <= (int)WS_XST && WS_O % 256 == 0 && WS_KVP % 256 == 0, "");

constexpr int L_W1T = 120 * 1024;
constexpr int L_TAB = 148 * 1024;
constexpr int L_DUMMY = L_TAB;
constexpr int L_RST = L_TAB + 256;
constexpr int L_CS = L_RST + 1024;
constexpr int L_CSS = L_CS + 1024;
constexpr int L_XST = L_CSS + 8 * 224 * 4;
constexpr int LDS_BYTES = L_XST + 64;

#define STAMP(k, i) do {} while (0)

#if defined(__HIP_DEVICE_COMPILE__)
typedef __amdgpu_buffer_rsrc_t rsrc_t;
#define AAUX 16
#define ROT2 0
#define ROT3 0
#define ROT4 0
constexpr int SC1 = 16;
template <int N> DI void wait_vm() { asm volatile("s_waitcnt vmcnt(%0)" ::"n"(N) : "memory"); }
DI void wait_lgkm0() { asm volatile("s_waitcnt lgkmcnt(0)" ::: "memory"); }
DI void bar() { asm volatile("" ::: "memory"); __builtin_amdgcn_s_barrier(); asm volatile("" ::: "memory"); }
DI void lbar() { wait_lgkm0(); bar(); }
DI unsigned pk2(float a, float b) { f32x2 v = {a, b}; return __builtin_bit_cast(unsigned, __builtin_convertvector(v, bf16x2)); }
DI float bflo(unsigned u) { return __uint_as_float(u << 16); }
DI float bfhi(unsigned u) { return __uint_as_float(u & 0xffff0000u); }
DI bf16x8 ld_b128(LAS const char* p) { return *(LAS const bf16x8*)p; }
DI bf16x8 ld_tr2(LAS const char* p0, LAS const char* p1) {
  s16x4 a = __builtin_amdgcn_ds_read_tr16_b64_v4i16((LAS s16x4*)p0);
  s16x4 b = __builtin_amdgcn_ds_read_tr16_b64_v4i16((LAS s16x4*)p1);
  return __builtin_bit_cast(bf16x8, __builtin_shufflevector(a, b, 0, 1, 2, 3, 4, 5, 6, 7));
}
DI bf16x8 pack8(const f32x16& x, int s) {
  u32x4 p = {pk2(x[8 * s], x[8 * s + 1]), pk2(x[8 * s + 2], x[8 * s + 3]), pk2(x[8 * s + 4], x[8 * s + 5]), pk2(x[8 * s + 6], x[8 * s + 7])};
  return __builtin_bit_cast(bf16x8, p);
}
DI float x32_max(float v) { auto r = __builtin_amdgcn_permlane32_swap(__float_as_uint(v), __float_as_uint(v), false, false); return fmaxf(__uint_as_float(r[0]), __uint_as_float(r[1])); }
DI float x32_min(float v) { auto r = __builtin_amdgcn_permlane32_swap(__float_as_uint(v), __float_as_uint(v), false, false); return fminf(__uint_as_float(r[0]), __uint_as_float(r[1])); }
DI float x32_sum(float v) { auto r = __builtin_amdgcn_permlane32_swap(__float_as_uint(v), __float_as_uint(v), false, false); return __uint_as_float(r[0]) + __uint_as_float(r[1]); }
DI float x16_sum(float v) { auto r = __builtin_amdgcn_permlane16_swap(__float_as_uint(v), __float_as_uint(v), false, false); return __uint_as_float(r[0]) + __uint_as_float(r[1]); }
template <int CTRL> DI float dpp_add(float v) { return v + __int_as_float(__builtin_amdgcn_update_dpp(0, __float_as_int(v), CTRL, 0xf, 0xf, true)); }
DI float wave_sum(float v) {
  v = dpp_add<0xB1>(v);
  v = dpp_add<0x4E>(v);
  v = dpp_add<0x141>(v);
  v = dpp_add<0x140>(v);
  return x32_sum(x16_sum(v));
}
#define MFMA32(a, b, c) __builtin_amdgcn_mfma_f32_32x32x16_bf16((a), (b), (c), 0, 0, 0)
#define MFMA16(a, b, c) __builtin_amdgcn_mfma_f32_16x16x32_bf16((a), (b), (c), 0, 0, 0)
DI rsrc_t mkrsrc(const void* p, unsigned bytes) { return __builtin_amdgcn_make_buffer_rsrc((void*)p, 0, bytes, 0x00020000); }
DI void st128(u32x4 v, rsrc_t r, int off) { __builtin_amdgcn_raw_buffer_store_b128(v, r, off, 0, SC1); }
DI u32x4 ld128(rsrc_t r, int off, int soff) { return __builtin_amdgcn_raw_buffer_load_b128(r, off, soff, SC1); }

DI unsigned read_tag(const char* ws) {
  unsigned e;
  asm volatile("s_load_dword %0, %1, 0x0\n\ts_waitcnt lgkmcnt(0)" : "=s"(e) : "s"(ws) : "memory");
  return e + 1u;
}
DI void set_flag(GAS unsigned* ctl, int line, int word, unsigned tag) { __hip_atomic_store(ctl + line * 64 + word, tag, __ATOMIC_RELAXED, __HIP_MEMORY_SCOPE_AGENT); }
DI void acquire() {
  asm volatile("" ::: "memory");
}
DI void poll(GAS unsigned* ctl, int line, int lo, int n, unsigned tag, int lane) {
  GAS unsigned* p = ctl + line * 64 + lo + (lane < n ? lane : 0);
  unsigned spins = 0;
  while (!__all(__hip_atomic_load(p, __ATOMIC_RELAXED, __HIP_MEMORY_SCOPE_AGENT) == tag)) {
    __builtin_amdgcn_s_sleep(1);
    if (++spins > (1u << 17)) { if (lane == 0) __hip_atomic_store(ctl + C_TMO * 64, (unsigned)line, __ATOMIC_RELAXED, __HIP_MEMORY_SCOPE_AGENT); break; }
  }
  acquire();
}

template <int BN> DI constexpr int bswz(int k) {
  return BN == 224 ? (((k >> 3) & 1) << 1)
       : BN == 64  ? ((((k >> 1) & 1) | (((k >> 3) & 1) << 1)) << 1)
       : BN == 128 ? (((k & 3) | (((k >> 3) & 1) << 2)) << 1)
                   : (((k >> 1) & 1) << 2);
}

DI constexpr int aswz(int r) { return (-(r >> 2)) & 3; }

template <int BN, int NS, int NT, bool AROW>
struct Loader {
  static constexpr int AI = 16, TI = AI + BN / 8, PW = TI / 4, STAGE = TI * 1024, CPR = BN / 8, NSTEP = NT - (NS - 1);
  static_assert(TI % 4 == 0 && NS * STAGE <= L_TAB, "");
  LAS char* dst0; rsrc_t ra, rb; int voff[PW]; int ldb64, kk, slot;
  DI void issue() {
#pragma unroll
    for (int i = 0; i < PW; ++i) {
      LAS char* dst = dst0 + slot * STAGE + i * 4096;
      if (i < AI / 4) __builtin_amdgcn_raw_ptr_buffer_load_lds(ra, dst, 16, voff[i], kk * (AROW ? 128 : 16384), 0, AROW ? 0 : AAUX);
      else __builtin_amdgcn_raw_ptr_buffer_load_lds(rb, dst, 16, voff[i], kk * ldb64, 0, 0);
    }
    kk = (kk == NT - 1) ? 0 : kk + 1;
    slot = (slot == NS - 1) ? 0 : slot + 1;
  }
  DI void start(LAS char* smem, rsrc_t ra_, rsrc_t rb_, int a_off, int lda_b, int b_off, int ldb_b, int segb, int lw, int lane, int rot) {
    dst0 = smem + lw * 1024; ra = ra_; rb = rb_; ldb64 = 64 * ldb_b; kk = rot; slot = 0;
#pragma unroll
    for (int i = 0; i < PW; ++i) {
      const int ins = lw + i * 4;
      if (i < AI / 4) {
        const int r = ins * 8 + (lane >> 3), c = (lane & 7) ^ ((r >> 1) & 7);
        voff[i] = AROW ? a_off + r * lda_b + c * 16 : a_off + ins * 1024 + lane * 16;
      } else {
        const int ch = (ins - AI) * 64 + lane, k = ch / CPR, c = (ch % CPR) ^ bswz<BN>(k);
        voff[i] = b_off + k * ldb_b + (c >> 3) * segb + (c & 7) * 16;
      }
    }
#pragma unroll
    for (int t = 0; t < NS - 1; ++t) issue();
  }
  DI void step() { wait_vm<(NS - 2) * PW>(); lbar(); issue(); }
  DI void steps(int n) {
#pragma unroll 1
    for (int t = 0; t < n; ++t) step();
  }
  DI void finish() {
    if constexpr (NS >= 6) { wait_vm<4 * PW>(); lbar(); }
    if constexpr (NS >= 5) { wait_vm<3 * PW>(); lbar(); }
    if constexpr (NS >= 4) { wait_vm<2 * PW>(); lbar(); }
    if constexpr (NS >= 3) { wait_vm<1 * PW>(); lbar(); }
    wait_vm<0>(); lbar();
  }
};
DI int opaque(int v) { asm volatile("" : "+v"(v)); return v; }

DI void touch_lines(LAS char* smem, rsrc_t r, int base, int line, int nlines) {
  __builtin_amdgcn_raw_ptr_buffer_load_lds(r, smem + L_DUMMY, 4, line < nlines ? base + line * 128 : (int)0x80000000, 0, 0, 0);
}

template <int BN> DI void colsum_a(LAS char* smem, int img, int lw, int lane) {
  constexpr int CPR = BN / 8, RS = BN * 2;
  const int rg = lane / CPR, c = lane % CPR;
  if (lane < 2 * CPR) {
    float s[8] = {0.f, 0.f, 0.f, 0.f, 0.f, 0.f, 0.f, 0.f};
#pragma unroll
    for (int r = 0; r < 8; ++r) {
      const int k = lw * 16 + rg * 8 + r;
      const u32x4 v = *(LAS const u32x4*)(smem + img + k * RS + ((c ^ bswz<BN>(k)) * 16));
#pragma unroll
      for (int e = 0; e < 4; ++e) { s[2 * e] += bflo(v[e]); s[2 * e + 1] += bfhi(v[e]); }
    }
    LAS f32x4* d = (LAS f32x4*)(smem + L_CSS + ((lw * 2 + rg) * BN + c * 8) * 4);
    d[0] = (f32x4){s[0], s[1], s[2], s[3]};
    d[1] = (f32x4){s[4], s[5], s[6], s[7]};
  }
}
template <int BN> DI void colsum_b(LAS char* smem, rsrc_t rws, unsigned off, int lane) {
  if (lane < BN / 4) {
    f32x4 s = {0.f, 0.f, 0.f, 0.f};
#pragma unroll
    for (int r = 0; r < 8; ++r) s += *(LAS const f32x4*)(smem + L_CSS + (r * BN + lane * 4) * 4);
    st128(__builtin_bit_cast(u32x4, s), rws, off + lane * 16);
  }
}
DI unsigned peek(GAS unsigned* ctl, int line, int lane) { return __hip_atomic_load(ctl + line * 64 + (lane & 15), __ATOMIC_RELAXED, __HIP_MEMORY_SCOPE_AGENT); }
DI void confirm(unsigned seen, GAS unsigned* ctl, int line, unsigned tag, int lane) {
  if (!__all(seen == tag)) poll(ctl, line, 0, 16, tag, lane);
  else acquire();
}

template <int BN, int NS, int NT, int TM, int TN, class NMap>
DI void mma_loop(LAS char* smem, f32x4 (&acc)[TM][TN], int row0, int lane, NMap&& nt) {
  constexpr int RS = BN * 2, STAGE = (16 + BN / 8) * 1024;
  const int r16 = lane & 15, g = lane >> 4, q = (lane & 15) >> 2, p = lane & 3;
  const int aoff = (row0 + r16) * 64 + ((g ^ aswz(r16)) * 16);
  int boff[TN];
#pragma unroll
  for (int j = 0; j < TN; ++j) boff[j] = 16384 + (8 * g + q) * RS + (((2 * nt(j) + (p >> 1)) ^ bswz<BN>(8 * g + q)) * 16) + 8 * (p & 1);
#pragma unroll
  for (int i = 0; i < TM; ++i)
#pragma unroll
    for (int j = 0; j < TN; ++j) acc[i][j] = (f32x4){0.f, 0.f, 0.f, 0.f};
  bf16x8 a0[TM], b0[TN], a1[TM], b1[TN];
  auto ldf = [&](bf16x8(&a)[TM], bf16x8(&b)[TN], int sb, int s) {
    a[0] = ld_b128(smem + sb + aoff + s * 8192);
#pragma unroll
    for (int j = 0; j < TN; ++j) b[j] = ld_tr2(smem + sb + boff[j] + (32 * s) * RS, smem + sb + boff[j] + (32 * s + 4) * RS);
#pragma unroll
    for (int i = 1; i < TM; ++i) a[i] = ld_b128(smem + sb + aoff + s * 8192 + i * 1024);
  };
  auto mma = [&](bf16x8(&a)[TM], bf16x8(&b)[TN]) {
#pragma unroll
    for (int i = 0; i < TM; ++i)
#pragma unroll
      for (int j = 0; j < TN; ++j) acc[i][j] = MFMA16(b[j], a[i], acc[i][j]);
  };
  auto sched = [&]() {
    constexpr int NL = TM + 2 * TN, NM = TM * TN;
    constexpr int PER = NL <= NM ? 1 : 2;
#pragma unroll
    for (int i = 0; i < NM; ++i) {
      __builtin_amdgcn_sched_group_barrier(0x8, 1, 0);
      if (i * PER < NL) __builtin_amdgcn_sched_group_barrier(0x100, PER, 0);
    }
    __builtin_amdgcn_sched_barrier(0);
  };
  bar();
  if (threadIdx.x < 64) STAMP(BN == 64 ? 1 : BN == 224 ? 2 : 3, 1);
  int sb = 0;
  ldf(a0, b0, 0, 0);
#pragma unroll 1
  for (int t = 0; t < NT - 1; ++t) {
    ldf(a1, b1, sb, 1); mma(a0, b0); sched();
    lbar();
    sb = (sb == (NS - 1) * STAGE) ? 0 : sb + STAGE;
    ldf(a0, b0, sb, 0); mma(a1, b1); sched();
  }
  ldf(a1, b1, sb, 1); mma(a0, b0); sched();
  mma(a1, b1);
}
DI void swap_pair(f32x4& x, f32x4& y) {
#pragma unroll
  for (int e = 0; e < 4; ++e) {
    auto r = __builtin_amdgcn_permlane16_swap(__float_as_uint(x[e]), __float_as_uint(y[e]), false, false);
    x[e] = __uint_as_float(r[0]); y[e] = __uint_as_float(r[1]);
  }
}
DI void add_bf8(f32x4& x, f32x4& y, const u32x4& rv) {
  x[0] += bflo(rv[0]); x[1] += bfhi(rv[0]); x[2] += bflo(rv[1]); x[3] += bfhi(rv[1]);
  y[0] += bflo(rv[2]); y[1] += bfhi(rv[2]); y[2] += bflo(rv[3]); y[3] += bfhi(rv[3]);
}
DI u32x4 pack_xy(const f32x4& x, const f32x4& y) { return (u32x4){pk2(x[0], x[1]), pk2(x[2], x[3]), pk2(y[0], y[1]), pk2(y[2], y[3])}; }

DI void dual_softmax(const f32x16& pv, f32x16& e1, f32x16& e2) {
  constexpr float L2E = 1.4426950408889634f;
  float mx = pv[0], mn = pv[0];
#pragma unroll
  for (int r = 1; r < 16; ++r) { mx = fmaxf(mx, pv[r]); mn = fminf(mn, pv[r]); }
  mx = x32_max(mx); mn = x32_min(mn);
  const float a = -mx * L2E, b = mn * L2E;
  float s1 = 0.f, s2 = 0.f;
#pragma unroll
  for (int r = 0; r < 16; ++r) {
    e1[r] = __builtin_amdgcn_exp2f(fmaf(pv[r], L2E, a));
    e2[r] = __builtin_amdgcn_exp2f(fmaf(pv[r], -L2E, b));
    s1 += e1[r]; s2 += e2[r];
  }
  s1 = x32_sum(s1); s2 = x32_sum(s2);
  const float r1 = __builtin_amdgcn_rcpf(s1), r2 = __builtin_amdgcn_rcpf(s2);
#pragma unroll
  for (int r = 0; r < 16; ++r) { e1[r] *= r1; e2[r] *= r2; }
}
DI void st16(LAS char* p, const f32x16& v) {
#pragma unroll
  for (int gq = 0; gq < 4; ++gq) *(LAS f32x4*)(p + gq * 1024) = (f32x4){v[4 * gq], v[4 * gq + 1], v[4 * gq + 2], v[4 * gq + 3]};
}
DI void ld16(LAS const char* p, f32x16& v) {
#pragma unroll
  for (int gq = 0; gq < 4; ++gq) { const f32x4 t = *(LAS const f32x4*)(p + gq * 1024); v[4 * gq] = t[0]; v[4 * gq + 1] = t[1]; v[4 * gq + 2] = t[2]; v[4 * gq + 3] = t[3]; }
}
#endif

__global__ __launch_bounds__(512, 2) void block_kernel(const u16* __restrict__ X, const u16* __restrict__ WQKV, const u16* __restrict__ WPHI,
                                                        const u16* __restrict__ WO, const u16* __restrict__ W1, const u16* __restrict__ W2,
                                                        u16* __restrict__ OUT, char* WS) {
#if defined(__HIP_DEVICE_COMPILE__)
  extern __shared__ __attribute__((aligned(16))) char smem_[];
  LAS char* smem = (LAS char*)smem_;
  const int tid = threadIdx.x, wid = __builtin_amdgcn_readfirstlane(tid >> 6);
  int lane = tid & 63;
  const int bid = blockIdx.x, xcd = bid & 7, loc = bid >> 3;
  const int tm = (xcd & 1) * 8 + (loc & 7), gp = (xcd >> 1) * 4 + (loc >> 3);
  const int rot = tm + 4 * (loc >> 3);
  const bool loader = wid >= 4;
  const int lw = wid & 3;
  GAS unsigned* ctl = (GAS unsigned*)WS;
  const rsrc_t rws = mkrsrc(WS, WS_END);
  if (wid == 0) STAMP(0, 0);
  unsigned tag = 0;

  {
    constexpr int NT = 16, NS = 3, STAGE = 40 * 1024, RS = 384;
    constexpr int L_XF = 0, XF_B = 14336;
    constexpr int L_PHI = 4 * XF_B;
    const int wm = (wid >> 1) & 1, hh = wid & 1, mi = wid >> 2;
    const int r32 = lane & 31, h = lane >> 5, blk = (lane >> 4) & 1, q = (lane & 15) >> 2, p = lane & 3;
    const int head = gp * 2 + hh;
    bf16x8 wf[2];
    f32x16 fq, fk, fv;
    if (loader) {
      const rsrc_t ra = mkrsrc(X, S_ * D_ * 2), rb = mkrsrc(WQKV, D_ * 3 * D_ * 2);
      const rsrc_t r1 = mkrsrc(W1, D_ * FF_ * 2), ro = mkrsrc(WO, D_ * D_ * 2);
      Loader<192, NS, NT, true> L;
      L.start(smem, ra, rb, tm * 128 * D_ * 2, D_ * 2, gp * 128, 3 * D_ * 2, 2048, lw, lane, rot & 15);
      tag = read_tag(WS);
      L.step();
      colsum_a<192>(smem, 16384, lw, lane);
      L.step();
      if (lw == 0) colsum_b<192>(smem, rws, WS_CS1 + (gp * 16 + tm) * 768, lane);
      if (lw == 1 && lane < 4) st128(*(LAS const u32x4*)(smem + L_XST + lane * 16), rws, WS_XST + (tm * 128 + gp * 8) * 8 + lane * 16);
      L.steps(2);
      if (lane == 0 && lw < 2) set_flag(ctl, lw == 0 ? C_CS1 + gp : C_ST + tm, lw == 0 ? tm : gp, tag);
      L.steps(1 + (loc & 3));
#pragma unroll
      for (int i = 0; i < 7; ++i) {
        const int ch = (lw + i * 4) * 64 + lane, k = ch / 28, c = (ch % 28) ^ bswz<224>(k);
        __builtin_amdgcn_raw_ptr_buffer_load_lds(r1, smem + L_W1T + (lw + i * 4) * 1024, 16, (tm * 64 + k) * (FF_ * 2) + gp * 448 + c * 16, 0, 0, 0);
      }
      touch_lines(smem, ro, bid * 8192, lane < 16 ? lw * 16 + lane : 4096, 64);
      L.steps(4 - (loc & 3));
      const unsigned seen = peek(ctl, lw == 0 ? C_CS1 + gp : C_ST + tm, lane);
      L.steps(2);
      confirm(seen, ctl, lw == 0 ? C_CS1 + gp : C_ST + tm, tag, lane);
      u32x4 gt[16];
      if (lw == 0) {
#pragma unroll
        for (int j = 0; j < 16; ++j) gt[j] = ld128(rws, WS_CS1 + gp * 16 * 768 + lane * 16, j * 768);
      } else {
        gt[0] = ld128(rws, WS_XST + tm * 1024 + lane * 16, 0);
      }
      colsum_a<224>(smem, L_W1T, lw, lane);
      L.step();
      if (lw == 2) colsum_b<224>(smem, rws, WS_CS3 + (gp * 16 + tm) * 896, lane);
      L.step();
      if (lw == 0) {
        f32x4 sum = __builtin_bit_cast(f32x4, gt[0]);
#pragma unroll
        for (int j = 1; j < 16; ++j) sum += __builtin_bit_cast(f32x4, gt[j]);
        if (lane < 48) *(LAS f32x4*)(smem + L_CS + lane * 16) = sum;
      }
      if (lw == 1) *(LAS u32x4*)(smem + L_RST + lane * 16) = gt[0];
      L.step();
      if (lane == 0 && lw == 2) set_flag(ctl, C_CS3 + gp, tm, tag);
      L.finish();
      bar();
      bar();
      ld16(smem + L_XF + lw * XF_B + lane * 16, fk);
      wf[0] = ld_b128(smem + L_XF + lw * XF_B + 12288 + lane * 16);
      wf[1] = ld_b128(smem + L_XF + lw * XF_B + 13312 + lane * 16);
      ld16(smem + L_XF + lw * XF_B + 4096 + lane * 16, fv);
      ld16(smem + L_XF + lw * XF_B + 8192 + lane * 16, fq);
    } else {
#pragma unroll
      for (int s = 0; s < 2; ++s) {
        s16x8 w;
#pragma unroll
        for (int j = 0; j < 8; ++j) w[j] = (short)WPHI[head * 1024 + (16 * s + 8 * (j >> 2) + 4 * h + (j & 3)) * 32 + r32];
        wf[s] = __builtin_bit_cast(bf16x8, w);
      }
      {
        const int row = tm * 128 + gp * 8 + wid * 2;
        const u32x4* px = (const u32x4*)(X + (size_t)row * D_);
        u32x4 v[4] = {px[lane], px[lane + 64], px[lane + 128], px[lane + 192]};
        tag = read_tag(WS);
        f32x2 st[2];
#pragma unroll
        for (int r = 0; r < 2; ++r) {
          float f[16];
#pragma unroll
          for (int i = 0; i < 4; ++i) { f[2 * i] = bflo(v[2 * r][i]); f[2 * i + 1] = bfhi(v[2 * r][i]); f[8 + 2 * i] = bflo(v[2 * r + 1][i]); f[9 + 2 * i] = bfhi(v[2 * r + 1][i]); }
          float s = 0.f;
#pragma unroll
          for (int i = 0; i < 16; ++i) s += f[i];
          const float mean = wave_sum(s) * (1.0f / D_);
          float qq = 0.f;
#pragma unroll
          for (int i = 0; i < 16; ++i) { const float d = f[i] - mean; qq += d * d; }
          st[r] = (f32x2){mean, 1.0f / sqrtf(wave_sum(qq) * (1.0f / D_) + LN_EPS)};
        }
        if (lane == 0) *(LAS f32x4*)(smem + L_XST + wid * 16) = (f32x4){st[0][0], st[0][1], st[1][0], st[1][1]};
      }
      int aoff[4];
#pragma unroll
      for (int s = 0; s < 4; ++s) aoff[s] = (wm * 64 + r32) * 128 + (((2 * s + h) ^ ((r32 >> 1) & 7)) * 16);
      const int boff = 16384 + (8 * h + q) * RS + (((hh * 4 + blk * 2 + (p >> 1)) ^ bswz<192>(q)) * 16) + 8 * (p & 1);
      f32x16 aq[2], ak[2], av[2];
#pragma unroll
      for (int i = 0; i < 2; ++i)
#pragma unroll
        for (int r = 0; r < 16; ++r) { aq[i][r] = 0.f; ak[i][r] = 0.f; av[i][r] = 0.f; }
      bf16x8 a0[2], b0[3], a1[2], b1[3];
      auto ldf = [&](bf16x8(&a)[2], bf16x8(&b)[3], int sb, int s) {
        a[0] = ld_b128(smem + sb + aoff[s]);
#pragma unroll
        for (int j = 0; j < 3; ++j) b[j] = ld_tr2(smem + sb + boff + (16 * s) * RS + j * 128, smem + sb + boff + (16 * s + 4) * RS + j * 128);
        a[1] = ld_b128(smem + sb + aoff[s] + 4096);
      };
      auto mma = [&](bf16x8(&a)[2], bf16x8(&b)[3]) {
#pragma unroll
        for (int i = 0; i < 2; ++i) {
          aq[i] = MFMA32(b[0], a[i], aq[i]);
          ak[i] = MFMA32(b[1], a[i], ak[i]);
          av[i] = MFMA32(a[i], b[2], av[i]);
        }
      };
      auto sched = [&]() {
#pragma unroll
        for (int i = 0; i < 6; ++i) {
          __builtin_amdgcn_sched_group_barrier(0x8, 1, 0);
          if (i < 4) __builtin_amdgcn_sched_group_barrier(0x100, 2, 0);
        }
        __builtin_amdgcn_sched_barrier(0);
      };
      if (wid == 0) STAMP(4, 0);
      lbar();
      if (wid == 0) STAMP(0, 1);
      int sb = 0;
      ldf(a0, b0, 0, 0);
#pragma unroll 1
      for (int t = 0; t < NT - 1; ++t) {
        ldf(a1, b1, sb, 1); mma(a0, b0); sched();
        ldf(a0, b0, sb, 2); mma(a1, b1); sched();
        ldf(a1, b1, sb, 3); mma(a0, b0); sched();
        lbar();
        sb = (sb == (NS - 1) * STAGE) ? 0 : sb + STAGE;
        ldf(a0, b0, sb, 0); mma(a1, b1); sched();
      }
      ldf(a1, b1, sb, 1); mma(a0, b0); sched();
      ldf(a0, b0, sb, 2); mma(a1, b1); sched();
      ldf(a1, b1, sb, 3); mma(a0, b0); sched();
      lbar();
      mma(a1, b1);
      if (wid == 0) STAMP(0, 2);
      st16(smem + L_XF + wid * XF_B + lane * 16, ak[1]);
      *(LAS bf16x8*)(smem + L_XF + wid * XF_B + 12288 + lane * 16) = wf[0];
      *(LAS bf16x8*)(smem + L_XF + wid * XF_B + 13312 + lane * 16) = wf[1];
      st16(smem + L_XF + wid * XF_B + 4096 + lane * 16, av[1]);
      st16(smem + L_XF + wid * XF_B + 8192 + lane * 16, aq[1]);
      lbar();
      if (wid == 0) STAMP(4, 4);
      fq = aq[0]; fk = ak[0]; fv = av[0];
    }
    const int rbase = wm * 64 + mi * 32;
    LAS const float* cs = (LAS const float*)(smem + L_CS) + hh * 32;
    LAS const f32x2* rst = (LAS const f32x2*)(smem + L_RST) + rbase;
    {
      const f32x2 ms = rst[r32];
      const float a = ms[1], b = -ms[0] * ms[1];
      const float csv = cs[128 + r32];
#pragma unroll
      for (int gq = 0; gq < 4; ++gq) {
        const f32x4 cq = *(LAS const f32x4*)(cs + 8 * gq + 4 * h), ck = *(LAS const f32x4*)(cs + 64 + 8 * gq + 4 * h);
        const f32x4 s01 = *(LAS const f32x4*)(rst + 8 * gq + 4 * h), s23 = *(LAS const f32x4*)(rst + 8 * gq + 4 * h + 2);
        const float mv[4] = {s01[0], s01[2], s23[0], s23[2]}, rv[4] = {s01[1], s01[3], s23[1], s23[3]};
#pragma unroll
        for (int e = 0; e < 4; ++e) {
          fq[4 * gq + e] = fmaf(fq[4 * gq + e], a, b * cq[e]);
          fk[4 * gq + e] = fmaf(fk[4 * gq + e], a, b * ck[e]);
          fv[4 * gq + e] = fmaf(-mv[e], csv, fv[4 * gq + e]) * rv[e];
        }
      }
    }
    if (wid == 0) STAMP(4, 5);
    f32x16 kv[2], zz[2];
    {
      LAS char* phis = smem + L_PHI + wid * 4352;
      f32x16 pk;
#pragma unroll
      for (int r = 0; r < 16; ++r) { pk[r] = 0.f; kv[0][r] = 0.f; kv[1][r] = 0.f; zz[0][r] = 0.f; zz[1][r] = 0.f; }
      pk = MFMA32(wf[0], pack8(fk, 0), pk);
      pk = MFMA32(wf[1], pack8(fk, 1), pk);
      f32x16 e1, e2;
      dual_softmax(pk, e1, e2);
      if (wid == 0) STAMP(4, 6);
      LAS char* wr = phis + r32 * 136 + h * 8;
#pragma unroll
      for (int gq = 0; gq < 4; ++gq) {
        *(LAS u32x2*)(wr + gq * 16) = (u32x2){pk2(e1[4 * gq], e1[4 * gq + 1]), pk2(e1[4 * gq + 2], e1[4 * gq + 3])};
        *(LAS u32x2*)(wr + 64 + gq * 16) = (u32x2){pk2(e2[4 * gq], e2[4 * gq + 1]), pk2(e2[4 * gq + 2], e2[4 * gq + 3])};
      }
      const s16x8 one_ = {0x3f80, 0x3f80, 0x3f80, 0x3f80, 0x3f80, 0x3f80, 0x3f80, 0x3f80};
      const bf16x8 ones = __builtin_bit_cast(bf16x8, one_);
#pragma unroll
      for (int st = 0; st < 2; ++st) {
        const bf16x8 vf = pack8(fv, st);
#pragma unroll
        for (int ft = 0; ft < 2; ++ft) {
          LAS const char* rd = phis + (16 * st + 4 * h + q) * 136 + ft * 64 + blk * 32 + p * 8;
          const bf16x8 pa = ld_tr2(rd, rd + 8 * 136);
          kv[ft] = MFMA32(pa, vf, kv[ft]);
          zz[ft] = MFMA32(pa, ones, zz[ft]);
        }
      }
    }
    if (wid == 0) STAMP(4, 7);
    if (wid == 4) STAMP(4, 9);
    lbar();
    {
      if (wid == 0) STAMP(4, 8);
      LAS char* kw = smem + wid * 8448;
      st16(kw + lane * 16, kv[0]);
      st16(kw + 4096 + lane * 16, kv[1]);
      if (r32 == 0) {
#pragma unroll
        for (int ft = 0; ft < 2; ++ft)
#pragma unroll
          for (int gq = 0; gq < 4; ++gq)
            *(LAS f32x4*)(kw + 8192 + ((h * 2 + ft) * 16 + gq * 4) * 4) = (f32x4){zz[ft][4 * gq], zz[ft][4 * gq + 1], zz[ft][4 * gq + 2], zz[ft][4 * gq + 3]};
      }
    }
    lbar();
    if (wid == 0) STAMP(0, 3);
    const unsigned kvp_grp = WS_KVP + gp * 16 * KVP_BYTES;
    {
      const int th = wid >> 2, fr = wid & 3;
      f32x4 x = {0.f, 0.f, 0.f, 0.f}, y = x;
#pragma unroll
      for (int w = 0; w < 4; ++w) {
        LAS const char* src = smem + (2 * w + th) * 8448 + fr * 2048 + lane * 16;
        x += *(LAS const f32x4*)src; y += *(LAS const f32x4*)(src + 1024);
      }
      st128(pack_xy(x, y), rws, kvp_grp + tm * KVP_BYTES + tid * 16);
      if (tid < 32) {
        f32x4 z = {0.f, 0.f, 0.f, 0.f};
#pragma unroll
        for (int w = 0; w < 4; ++w) z += *(LAS const f32x4*)(smem + (2 * w + (tid >> 4)) * 8448 + 8192 + (tid & 15) * 16);
        st128(__builtin_bit_cast(u32x4, z), rws, kvp_grp + tm * KVP_BYTES + 8192 + tid * 16);
      }
    }
    wait_vm<0>();
    lbar();
    if (tid == 0) set_flag(ctl, C_KV + gp, tm, tag);
    if (wid == 0) STAMP(0, 4);
    bf16x8 pf[2][2];
    {
      f32x16 pq;
#pragma unroll
      for (int r = 0; r < 16; ++r) pq[r] = 0.f;
      pq = MFMA32(wf[0], pack8(fq, 0), pq);
      pq = MFMA32(wf[1], pack8(fq, 1), pq);
      f32x16 e1, e2;
      dual_softmax(pq, e1, e2);
      pf[0][0] = pack8(e1, 0); pf[0][1] = pack8(e1, 1);
      pf[1][0] = pack8(e2, 0); pf[1][1] = pack8(e2, 1);
    }
    if (wid == 0) STAMP(0, 5);
    poll(ctl, C_KV + gp, 0, 16, tag, lane);
    if (wid == 0) STAMP(0, 6);
    {
      u32x4 t[16];
#pragma unroll
      for (int j = 0; j < 16; ++j) t[j] = ld128(rws, kvp_grp + tid * 16, j * KVP_BYTES);
      u32x4 tz[16];
      if (tid < 32) {
#pragma unroll
        for (int j = 0; j < 16; ++j) tz[j] = ld128(rws, kvp_grp + 8192 + tid * 16, j * KVP_BYTES);
      }
      f32x4 x = {0.f, 0.f, 0.f, 0.f}, y = x;
#pragma unroll
      for (int j = 0; j < 16; ++j) add_bf8(x, y, t[j]);
      *(LAS u32x4*)(smem + tid * 16) = pack_xy(x, y);
      if (tid < 32) {
        f32x4 z = __builtin_bit_cast(f32x4, tz[0]);
#pragma unroll
        for (int j = 1; j < 16; ++j) z += __builtin_bit_cast(f32x4, tz[j]);
        *(LAS u32x2*)(smem + 8192 + tid * 8) = (u32x2){pk2(z[0], z[1]), pk2(z[2], z[3])};
      }
    }
    lbar();
    if (wid == 0) STAMP(0, 7);
    {
      f32x16 o, dn;
#pragma unroll
      for (int r = 0; r < 16; ++r) { o[r] = 0.f; dn[r] = 0.f; }
#pragma unroll
      for (int ft = 0; ft < 2; ++ft)
#pragma unroll
        for (int s = 0; s < 2; ++s) {
          const bf16x8 ka = ld_b128(smem + hh * 4096 + (ft * 2 + s) * 1024 + lane * 16);
          const bf16x8 za = ld_b128(smem + 8192 + hh * 128 + (h * 2 + ft) * 32 + s * 16);
          o = MFMA32(ka, pf[ft][s], o);
          dn = MFMA32(za, pf[ft][s], dn);
        }
      const float rd = 1.0f / (dn[0] + 1e-6f);
      u32x4 c01, c23;
      {
        auto r0 = __builtin_amdgcn_permlane32_swap(pk2(o[0] * rd, o[1] * rd), pk2(o[4] * rd, o[5] * rd), false, false);
        auto r1 = __builtin_amdgcn_permlane32_swap(pk2(o[2] * rd, o[3] * rd), pk2(o[6] * rd, o[7] * rd), false, false);
        auto r2 = __builtin_amdgcn_permlane32_swap(pk2(o[8] * rd, o[9] * rd), pk2(o[12] * rd, o[13] * rd), false, false);
        auto r3 = __builtin_amdgcn_permlane32_swap(pk2(o[10] * rd, o[11] * rd), pk2(o[14] * rd, o[15] * rd), false, false);
        c01 = (u32x4){r0[0], r1[0], r0[1], r1[1]}; c23 = (u32x4){r2[0], r3[0], r2[1], r3[1]};
      }
#pragma unroll
      for (int e = 0; e < 4; ++e) { auto r = __builtin_amdgcn_permlane16_swap(c01[e], c23[e], false, false); c01[e] = r[0]; c23[e] = r[1]; }
      const int ob = WS_O + (tm * 32 + head) * 8192 + (rbase + (lane & 15)) * 64 + (((2 * blk + h) ^ aswz(lane & 15)) * 16);
      st128(c01, rws, ob);
      st128(c23, rws, ob + 1024);
    }
    wait_vm<0>();
    lbar();
    if (tid == 0) set_flag(ctl, C_O + tm, gp, tag);
    if (wid == 0) STAMP(0, 8);
  }

  int r16, g, cpair, cswz;
  auto relane = [&]() {
    lane = opaque(lane);
    r16 = lane & 15; g = lane >> 4;
    cpair = (g & 1) * 16 + (g >> 1) * 8;
    cswz = ((cpair >> 3) ^ aswz(r16)) * 16;
  };
  relane();
  if (loader) {
    poll(ctl, C_O + tm, 0, 16, tag, lane);
    if (wid == 4) STAMP(1, 0);
    if (bid == 0 && tid == 256) __hip_atomic_store(ctl + C_EPOCH * 64, tag, __ATOMIC_RELAXED, __HIP_MEMORY_SCOPE_AGENT);
    Loader<64, 5, 16, false> L;
    L.start(smem, rws, mkrsrc(WO, D_ * D_ * 2), WS_O + tm * 128 * D_ * 2, 0, gp * 128, D_ * 2, 128, lw, lane, ROT2);
    L.steps(L.NSTEP);
    L.finish();
  } else {
    const int row = tm * 128 + wid * 32 + r16, col = gp * 64 + cpair;
    u32x4 res[2][2];
#pragma unroll
    for (int i = 0; i < 2; ++i)
#pragma unroll
      for (int j = 0; j < 2; ++j) res[i][j] = *(const u32x4*)(X + (size_t)(row + i * 16) * D_ + col + j * 32);
    f32x4 acc[2][4];
    mma_loop<64, 5, 16, 2, 4>(smem, acc, wid * 32, lane, [](int j) { return j; });
    if (wid == 0) STAMP(1, 2);
#pragma unroll
    for (int i = 0; i < 2; ++i) {
      float s = 0.f, qq = 0.f;
#pragma unroll
      for (int j = 0; j < 2; ++j) {
        f32x4 x = acc[i][2 * j], y = acc[i][2 * j + 1];
        swap_pair(x, y);
        add_bf8(x, y, res[i][j]);
        const u32x4 o = pack_xy(x, y);
        st128(o, rws, WS_H + (tm * 32 + gp * 2 + j) * 8192 + (wid * 32 + i * 16 + r16) * 64 + cswz);
#pragma unroll
        for (int e = 0; e < 4; ++e) { const float lo = bflo(o[e]), hi = bfhi(o[e]); s += lo + hi; qq = fmaf(lo, lo, fmaf(hi, hi, qq)); }
      }
      s = x32_sum(x16_sum(s)); qq = x32_sum(x16_sum(qq));
      if (g == 0) __builtin_amdgcn_raw_buffer_store_b64(__builtin_bit_cast(u32x2, (f32x2){s, qq}), rws, WS_HST + ((tm * 16 + gp) * 128 + wid * 32 + i * 16 + r16) * 8, 0, SC1);
    }
    wait_vm<0>();
  }
  bar();
  if (tid == 0) set_flag(ctl, C_H + tm, gp, tag);
  if (wid == 0) STAMP(1, 3);

  relane();
  if (loader) {
    poll(ctl, C_H + tm, 0, 16, tag, lane);
    if (wid == 4) STAMP(2, 0);
    const rsrc_t r2 = mkrsrc(W2, FF_ * D_ * 2);
    Loader<224, 3, 16, false> L;
    L.start(smem, rws, mkrsrc(W1, D_ * FF_ * 2), WS_H + tm * 128 * D_ * 2, 0, gp * 448, FF_ * 2, 128, lw, lane, ROT3);
    L.steps(1 + (loc & 7));
    touch_lines(smem, r2, bid * 28672, lane < 56 ? lw * 56 + lane : 4096, 224);
    L.steps(8 - (loc & 7));
    const unsigned seen = peek(ctl, C_CS3 + gp, lane);
    L.steps(2);
    confirm(seen, ctl, C_CS3 + gp, tag, lane);
    u32x4 gt[16];
    if (lw < 2) {
      const int off = lw == 0 ? WS_CS3 + gp * 16 * 896 : WS_HST + tm * 16384, st = lw == 0 ? 896 : 1024;
#pragma unroll
      for (int j = 0; j < 16; ++j) gt[j] = ld128(rws, off + lane * 16, j * st);
    }
    L.steps(2);
    if (lw < 2) {
      f32x4 sum = __builtin_bit_cast(f32x4, gt[0]);
#pragma unroll
      for (int j = 1; j < 16; ++j) sum += __builtin_bit_cast(f32x4, gt[j]);
      if (lw == 0) {
        if (lane < 56) *(LAS f32x4*)(smem + L_CS + lane * 16) = sum;
      } else {
        const float m0 = sum[0] * (1.0f / D_), m1 = sum[2] * (1.0f / D_);
        const float v0 = fmaxf(sum[1] * (1.0f / D_) - m0 * m0, 0.f), v1 = fmaxf(sum[3] * (1.0f / D_) - m1 * m1, 0.f);
        *(LAS f32x4*)(smem + L_RST + lane * 16) = (f32x4){m0, 1.0f / sqrtf(v0 + LN_EPS), m1, 1.0f / sqrtf(v1 + LN_EPS)};
      }
    }
    L.step();
    L.finish();
  } else {
    const int wm = wid >> 1, wn = wid & 1;
    f32x4 acc[4][7];
    mma_loop<224, 3, 16, 4, 7>(smem, acc, wm * 64, lane, [&](int j) { return j < 6 ? wn * 8 + j : 6 + wn; });
    if (wid == 0) STAMP(2, 2);
    relane();
    f32x4 cs[7];
#pragma unroll
    for (int j = 0; j < 7; ++j) cs[j] = *(LAS const f32x4*)(smem + L_CS + ((j < 6 ? wn * 8 + j : 6 + wn) * 16 + g * 4) * 4);
#pragma unroll
    for (int i = 0; i < 4; ++i) {
      const f32x2 ms = *(LAS const f32x2*)(smem + L_RST + (wm * 64 + i * 16 + r16) * 8);
      const float a = ms[1], b = -ms[0] * ms[1];
#pragma unroll
      for (int j = 0; j < 7; ++j)
#pragma unroll
        for (int e = 0; e < 4; ++e) acc[i][j][e] = fmaxf(fmaf(acc[i][j][e], a, b * cs[j][e]), 0.f);
      const int ob = WS_H1 + (tm * 112 + gp * 7 + wn * 4) * 8192 + (wm * 64 + i * 16 + r16) * 64 + cswz;
#pragma unroll
      for (int j = 0; j < 3; ++j) {
        f32x4 x = acc[i][2 * j], y = acc[i][2 * j + 1];
        swap_pair(x, y);
        st128(pack_xy(x, y), rws, ob + j * 8192);
      }
      if (i & 1) {
        f32x4 x = acc[i - 1][6], y = acc[i][6];
        swap_pair(x, y);
        st128(pack_xy(x, y), rws, WS_H1 + (tm * 112 + gp * 7 + 3) * 8192 + (wm * 64 + (i - 1 + (g & 1)) * 16 + r16) * 64 + (((2 * wn + (g >> 1)) ^ aswz(r16)) * 16));
      }
    }
    if (wid == 0) STAMP(2, 3);
    wait_vm<0>();
  }
  bar();
  if (tid == 0) set_flag(ctl, C_H1 + tm, gp, tag);
  if (wid == 0) STAMP(2, 4);

  relane();
  const int kh = gp >> 3;
  if (loader) {
    poll(ctl, C_H1 + tm, kh * 8, 8, tag, lane);
    if (wid == 4) STAMP(3, 0);
    Loader<128, 4, 28, false> L;
    L.start(smem, rws, mkrsrc(W2, FF_ * D_ * 2), WS_H1 + (tm * 112 + kh * 56) * 8192, 0, kh * 1792 * D_ * 2 + gp * 128, D_ * 2,
            kh ? -1024 : 1024, lw, lane, ROT4);
    L.steps(L.NSTEP);
    L.finish();
  } else {
    const int wm = wid >> 1, wn = wid & 1;
    const int row = tm * 128 + wm * 64 + r16, col = gp * 64 + wn * 32 + cpair;
    u32x4 res[4];
#pragma unroll
    for (int i = 0; i < 4; ++i) res[i] = ld128(rws, WS_H + (tm * 32 + gp * 2 + wn) * 8192 + (wm * 64 + i * 16 + r16) * 64 + cswz, 0);
    f32x4 acc[4][4];
    mma_loop<128, 4, 28, 4, 4>(smem, acc, wm * 64, lane, [&](int j) { return (j >> 1) * 4 + wn * 2 + (j & 1); });
    if (wid == 0) STAMP(3, 2);
    const int pb = WS_P4 + (tm * 16 + gp) * 32768 + wid * 8192 + lane * 16;
#pragma unroll
    for (int i = 0; i < 4; ++i)
#pragma unroll
      for (int j = 0; j < 2; ++j) st128(__builtin_bit_cast(u32x4, acc[i][2 + j]), rws, pb + (i * 2 + j) * 1024);
    wait_vm<0>();
    if (lane == 0) set_flag(ctl, C_P4 + tm, gp * 4 + wid, tag);
    if (wid == 0) STAMP(3, 3);
    poll(ctl, C_P4 + tm, (gp ^ 8) * 4 + wid, 1, tag, lane);
    if (wid == 0) STAMP(3, 4);
    const int qb = WS_P4 + (tm * 16 + (gp ^ 8)) * 32768 + wid * 8192 + lane * 16;
    u32x4 t[4][2];
#pragma unroll
    for (int i = 0; i < 4; ++i)
#pragma unroll
      for (int j = 0; j < 2; ++j) t[i][j] = ld128(rws, qb + (i * 2 + j) * 1024, 0);
#pragma unroll
    for (int i = 0; i < 4; ++i) {
      f32x4 x = acc[i][0] + __builtin_bit_cast(f32x4, t[i][0]), y = acc[i][1] + __builtin_bit_cast(f32x4, t[i][1]);
      swap_pair(x, y);
      add_bf8(x, y, res[i]);
      *(u32x4*)(OUT + (size_t)(row + i * 16) * D_ + col) = pack_xy(x, y);
    }
    if (wid == 0) STAMP(3, 5);
  }
#endif
}

extern "C" void kernel_launch(void* const* d_in, const int* in_sizes, int n_in, void* d_out, int out_size, void* d_ws, size_t ws_size,
                              hipStream_t stream) {
  (void)hipFuncSetAttribute((const void*)block_kernel, hipFuncAttributeMaxDynamicSharedMemorySize, LDS_BYTES);
  block_kernel<<<256, 512, LDS_BYTES, stream>>>((const u16*)d_in[0], (const u16*)d_in[1], (const u16*)d_in[2], (const u16*)d_in[3],
                                                (const u16*)d_in[5], (const u16*)d_in[6], (u16*)d_out, (char*)d_ws + (128u  __H0__ ));
}
```

```cpp
#include <hip/hip_runtime.h>
#include <hip/hip_bf16.h>

#define LAS __attribute__((address_space(3)))
#define GAS __attribute__((address_space(1)))
#define DI __device__ __forceinline__
typedef __bf16 bf16x8 __attribute__((ext_vector_type(8)));
typedef __bf16 bf16x2 __attribute__((ext_vector_type(2)));
typedef short s16x4 __attribute__((ext_vector_type(4)));
typedef short s16x8 __attribute__((ext_vector_type(8)));
typedef float f32x2 __attribute__((ext_vector_type(2)));
typedef float f32x4 __attribute__((ext_vector_type(4)));
typedef float f32x16 __attribute__((ext_vector_type(16)));
typedef unsigned u32x2 __attribute__((ext_vector_type(2)));
typedef unsigned u32x4 __attribute__((ext_vector_type(4)));
typedef unsigned short u16;

constexpr int S_ = 2048, D_ = 1024, FF_ = 3584;
constexpr float LN_EPS = 1e-5f;

constexpr int C_EPOCH = 0, C_TMO = 1, C_ST = 2, C_CS1 = 18, C_CS3 = 34, C_KV = 50, C_O = 66, C_H = 82, C_H1 = 98, C_P4 = 114, C_LINES = 130;
constexpr unsigned WS_XST = 36864;
constexpr unsigned WS_CS1 = WS_XST + S_ * 8;
constexpr unsigned WS_CS3 = WS_CS1 + 16 * 16 * 192 * 4;
constexpr unsigned WS_HST = WS_CS3 + 16 * 16 * 224 * 4;
constexpr unsigned KVP_BYTES = 8192 + 512;
constexpr unsigned WS_KVP = WS_HST + S_ * 16 * 8;
#define BLOCKED 1
constexpr unsigned WS_O = WS_KVP + 256 * KVP_BYTES;
constexpr unsigned WS_H = WS_O + S_ * D_ * 2;
constexpr unsigned WS_H1 = WS_H + S_ * D_ * 2;
constexpr unsigned WS_P4 = WS_O, WS_END = WS_H1 + S_ * FF_ * 2;
static_assert(C_LINES * 256 <= (int)WS_XST && WS_O % 256 == 0 && WS_KVP % 256 == 0, "");

constexpr int L_WT = 120 * 1024;
constexpr int L_TAB = 149 * 1024;
constexpr int L_DUMMY = L_TAB;
constexpr int L_RST = L_TAB + 256;
constexpr int L_CS = L_RST + 1024;
constexpr int L_CSS = L_CS + 1024;
constexpr int L_XST = L_CSS + 8192;
constexpr int LDS_BYTES = L_XST + 64;
static_assert(LDS_BYTES <= 160 * 1024, "");

#define STAMP(k, i) do {} while (0)
#define TS_DECL
#define TS(t)
#define TS_OUT(k)

#if defined(__HIP_DEVICE_COMPILE__)
#pragma clang attribute push(__attribute__((target("no-packed-fp32-ops"))), apply_to = function)
typedef __amdgpu_buffer_rsrc_t rsrc_t;
DI float u2f(unsigned u) { return __builtin_bit_cast(float, u); }
DI unsigned f2u(float f) { return __builtin_bit_cast(unsigned, f); }
DI bool wave_all(bool p) { return __builtin_amdgcn_ballot_w64(p) == ~0ull; }
#define ROT1 tm
constexpr int SC1 = 16;
template <int N> DI void wait_vm() { asm volatile("s_waitcnt vmcnt(%0)" ::"n"(N) : "memory"); }
DI void wait_lgkm0() { asm volatile("s_waitcnt lgkmcnt(0)" ::: "memory"); }
DI void bar() { asm volatile("" ::: "memory"); __builtin_amdgcn_s_barrier(); asm volatile("" ::: "memory"); }
DI void lbar() { wait_lgkm0(); bar(); }
DI unsigned pk2(float a, float b) { f32x2 v = {a, b}; return __builtin_bit_cast(unsigned, __builtin_convertvector(v, bf16x2)); }
DI float bflo(unsigned u) { return u2f(u << 16); }
DI float bfhi(unsigned u) { return u2f(u & 0xffff0000u); }
DI bf16x8 ld_b128(LAS const char* p) { return *(LAS const bf16x8*)p; }
DI bf16x8 ld_tr2(LAS const char* p0, LAS const char* p1) {
  s16x4 a = __builtin_amdgcn_ds_read_tr16_b64_v4i16((LAS s16x4*)p0);
  s16x4 b = __builtin_amdgcn_ds_read_tr16_b64_v4i16((LAS s16x4*)p1);
  return __builtin_bit_cast(bf16x8, __builtin_shufflevector(a, b, 0, 1, 2, 3, 4, 5, 6, 7));
}
DI bf16x8 pack8(const f32x16& x, int s) {
  u32x4 p = {pk2(x[8 * s], x[8 * s + 1]), pk2(x[8 * s + 2], x[8 * s + 3]), pk2(x[8 * s + 4], x[8 * s + 5]), pk2(x[8 * s + 6], x[8 * s + 7])};
  return __builtin_bit_cast(bf16x8, p);
}
DI float x32_max(float v) { auto r = __builtin_amdgcn_permlane32_swap(f2u(v), f2u(v), false, false); return __builtin_fmaxf(u2f(r[0]), u2f(r[1])); }
DI float x32_min(float v) { auto r = __builtin_amdgcn_permlane32_swap(f2u(v), f2u(v), false, false); return __builtin_fminf(u2f(r[0]), u2f(r[1])); }
DI float x32_sum(float v) { auto r = __builtin_amdgcn_permlane32_swap(f2u(v), f2u(v), false, false); return u2f(r[0]) + u2f(r[1]); }
DI float x16_sum(float v) { auto r = __builtin_amdgcn_permlane16_swap(f2u(v), f2u(v), false, false); return u2f(r[0]) + u2f(r[1]); }
template <int CTRL> DI float dpp_add(float v) { return v + u2f(__builtin_amdgcn_update_dpp(0, (int)f2u(v), CTRL, 0xf, 0xf, true)); }
DI float wave_sum(float v) {
  v = dpp_add<0xB1>(v);
  v = dpp_add<0x4E>(v);
  v = dpp_add<0x141>(v);
  v = dpp_add<0x140>(v);
  return x32_sum(x16_sum(v));
}
#define MFMA32(a, b, c) __builtin_amdgcn_mfma_f32_32x32x16_bf16((a), (b), (c), 0, 0, 0)
#define MFMA16(a, b, c) __builtin_amdgcn_mfma_f32_16x16x32_bf16((a), (b), (c), 0, 0, 0)
DI rsrc_t mkrsrc(const void* p, unsigned bytes) { return __builtin_amdgcn_make_buffer_rsrc((void*)p, 0, bytes, 0x00020000); }
DI void st128(u32x4 v, rsrc_t r, int off) { __builtin_amdgcn_raw_buffer_store_b128(v, r, off, 0, SC1); }
DI u32x4 ld128(rsrc_t r, int off, int soff) { return __builtin_amdgcn_raw_buffer_load_b128(r, off, soff, SC1); }

DI unsigned read_tag(const char* ws) {
  unsigned e;
  asm volatile("s_load_dword %0, %1, 0x0\n\ts_waitcnt lgkmcnt(0)" : "=s"(e) : "s"(ws) : "memory");
  return e + 1u;
}
DI void set_flag(GAS unsigned* ctl, int line, int word, unsigned tag) { __hip_atomic_store(ctl + line * 64 + word, tag, __ATOMIC_RELAXED, __HIP_MEMORY_SCOPE_AGENT); }
DI void acquire() {
  asm volatile("" ::: "memory");
}
DI void poll(GAS unsigned* ctl, int line, int lo, int n, unsigned tag, int lane) {
  GAS unsigned* p = ctl + line * 64 + lo + (lane < n ? lane : 0);
  unsigned spins = 0;
  while (!wave_all(__hip_atomic_load(p, __ATOMIC_RELAXED, __HIP_MEMORY_SCOPE_AGENT) == tag)) {
    __builtin_amdgcn_s_sleep(1);
    if (++spins > (1u << 17)) { if (lane == 0) __hip_atomic_store(ctl + C_TMO * 64, (unsigned)line, __ATOMIC_RELAXED, __HIP_MEMORY_SCOPE_AGENT); break; }
  }
  acquire();
}

template <int BN> DI constexpr int bswz(int k) {
  return BN == 224 ? (((k >> 3) & 1) << 1)
       : BN == 64  ? ((((k >> 1) & 1) | (((k >> 3) & 1) << 1)) << 1)
       : BN == 128 ? (((k & 3) | (((k >> 3) & 1) << 2)) << 1)
                   : (((k >> 1) & 1) << 2);
}

DI constexpr int aswz(int r) { return (-(r >> 2)) & 3; }

template <int BN, int NS, int NT, bool AROW, int LAG = 0>
struct Loader {
  static constexpr int PA = 4, PB = BN / 32, PW = PA + PB, BST = BN * 128, CPR = BN / 8;
  static_assert(NS * (16384 + BST) <= L_TAB, "");
  LAS char* dst0; rsrc_t ra, rb; int va[PA], vb[PB]; int ldb64, own0, tn;
  DI int ktile(int t) const { return AROW ? ((t + own0) & (NT - 1)) : own0 < 0 ? t : (t < 3 ? own0 + t : (t - 3 < own0 ? t - 3 : t)); }
  DI void issueA(int t) {
    const int kk = ktile(t), slot = t % NS;
#pragma unroll
    for (int i = 0; i < PA; ++i)
      __builtin_amdgcn_raw_ptr_buffer_load_lds(ra, dst0 + slot * 16384 + i * 4096, 16, va[i], kk * (AROW ? 128 : 16384), 0, AROW ? 0 : SC1);
  }
  DI void issueB(int t) {
    const int kk = ktile(t), slot = t % NS;
#pragma unroll
    for (int i = 0; i < PB; ++i) __builtin_amdgcn_raw_ptr_buffer_load_lds(rb, dst0 + NS * 16384 + slot * BST + i * 4096, 16, vb[i], kk * ldb64, 0, 0);
  }
  DI void init(LAS char* smem, rsrc_t ra_, rsrc_t rb_, int a_off, int lda_b, int b_off, int ldb_b, int segb, int lw, int lane, int own) {
    dst0 = smem + lw * 1024; ra = ra_; rb = rb_; ldb64 = 64 * ldb_b; own0 = own; tn = NS - 1;
#pragma unroll
    for (int i = 0; i < PA; ++i) {
      const int ins = lw + i * 4, r = ins * 8 + (lane >> 3), c = (lane & 7) ^ ((r >> 1) & 7);
      va[i] = AROW ? a_off + r * lda_b + c * 16 : a_off + ins * 1024 + lane * 16;
    }
#pragma unroll
    for (int i = 0; i < PB; ++i) {
      const int ch = (lw + i * 4) * 64 + lane, k = ch / CPR, c = (ch % CPR) ^ bswz<BN>(k);
      vb[i] = b_off + k * ldb_b + (c >> 3) * segb + (c & 7) * 16;
    }
  }
  template <int ALLOW = (NS - 2 - LAG) * PW> DI void step() { wait_vm<ALLOW>(); lbar(); issueA(tn); issueB(tn); ++tn; }
  DI void steps(int n) {
#pragma unroll 1
    for (int t = 0; t < n; ++t) step();
  }
  DI void finish() {
    if constexpr (NS - LAG >= 5) { wait_vm<3 * PW>(); lbar(); }
    if constexpr (NS - LAG >= 4) { wait_vm<2 * PW>(); lbar(); }
    if constexpr (NS - LAG >= 3) { wait_vm<1 * PW>(); lbar(); }
    wait_vm<0>(); lbar();
  }
};
template <int BN> DI void load_wtile(LAS char* dst, rsrc_t r, int off, int ldb_b, int segb, int lw, int lane) {
#pragma unroll
  for (int i = 0; i < BN / 32; ++i) {
    const int ch = (lw + i * 4) * 64 + lane, k = ch / (BN / 8), c = (ch % (BN / 8)) ^ bswz<BN>(k);
    __builtin_amdgcn_raw_ptr_buffer_load_lds(r, dst + (lw + i * 4) * 1024, 16, off + k * ldb_b + (c >> 3) * segb + (c & 7) * 16, 0, 0, 0);
  }
}
DI int opaque(int v) { asm volatile("" : "+v"(v)); return v; }

DI void touch_lines(LAS char* smem, rsrc_t r, int base, int line, int nlines) {
  __builtin_amdgcn_raw_ptr_buffer_load_lds(r, smem + L_DUMMY, 4, line < nlines ? base + line * 128 : (int)0x80000000, 0, 0, 0);
}

template <int BN> DI void colsum_tile(LAS char* img, rsrc_t rws, unsigned off, int lw, int lane) {
  constexpr int RS = BN * 2;
  const int r16 = lane & 15, g = lane >> 4, q = (lane & 15) >> 2, p = lane & 3;
  const s16x8 one_ = {0x3f80, 0x3f80, 0x3f80, 0x3f80, 0x3f80, 0x3f80, 0x3f80, 0x3f80};
  LAS const char* rd[4];
#pragma unroll
  for (int i = 0; i < 4; ++i) {
    const int nt = lw + 4 * i < BN / 16 ? lw + 4 * i : lw;
    rd[i] = img + (8 * g + q) * RS + (((2 * nt + (p >> 1)) ^ bswz<BN>(8 * g + q)) * 16) + 8 * (p & 1);
  }
  s16x4 v[4][4];
#define TR4(a, b, c, d, r) "ds_read_b64_tr_b16 %" #a ", %" #r "\n\tds_read_b64_tr_b16 %" #b ", %" #r " offset:%c20\n\t" \
                           "ds_read_b64_tr_b16 %" #c ", %" #r " offset:%c21\n\tds_read_b64_tr_b16 %" #d ", %" #r " offset:%c22\n\t"
  asm volatile(TR4(0, 1, 2, 3, 16) TR4(4, 5, 6, 7, 17) TR4(8, 9, 10, 11, 18) TR4(12, 13, 14, 15, 19) "s_waitcnt lgkmcnt(0)"
               : "=&v"(v[0][0]), "=&v"(v[0][1]), "=&v"(v[0][2]), "=&v"(v[0][3]), "=&v"(v[1][0]), "=&v"(v[1][1]), "=&v"(v[1][2]), "=&v"(v[1][3]),
                 "=&v"(v[2][0]), "=&v"(v[2][1]), "=&v"(v[2][2]), "=&v"(v[2][3]), "=&v"(v[3][0]), "=&v"(v[3][1]), "=&v"(v[3][2]), "=&v"(v[3][3])
               : "v"(rd[0]), "v"(rd[1]), "v"(rd[2]), "v"(rd[3]), "i"(4 * RS), "i"(32 * RS), "i"(36 * RS) : "memory");
#undef TR4
  f32x4 c[4];
#pragma unroll
  for (int i = 0; i < 4; ++i) {
    c[i] = (f32x4){0.f, 0.f, 0.f, 0.f};
    c[i] = MFMA16(__builtin_bit_cast(bf16x8, __builtin_shufflevector(v[i][0], v[i][1], 0, 1, 2, 3, 4, 5, 6, 7)), __builtin_bit_cast(bf16x8, one_), c[i]);
    c[i] = MFMA16(__builtin_bit_cast(bf16x8, __builtin_shufflevector(v[i][2], v[i][3], 0, 1, 2, 3, 4, 5, 6, 7)), __builtin_bit_cast(bf16x8, one_), c[i]);
  }
  const f32x4 out = r16 == 0 ? c[0] : r16 == 1 ? c[1] : r16 == 2 ? c[2] : c[3];
  st128(__builtin_bit_cast(u32x4, out), rws, lw + 4 * r16 < BN / 16 ? off + (lw + 4 * r16) * 64 + g * 16 : 0x80000000u);
}
DI f32x4 sum4(u32x4 (&t)[4]) {
  asm volatile("" : "+v"(t[0]), "+v"(t[1]), "+v"(t[2]), "+v"(t[3]));
  return (__builtin_bit_cast(f32x4, t[0]) + __builtin_bit_cast(f32x4, t[1])) + (__builtin_bit_cast(f32x4, t[2]) + __builtin_bit_cast(f32x4, t[3]));
}
DI f32x4 lds_sum4(LAS const char* p) {
  return (*(LAS const f32x4*)p + *(LAS const f32x4*)(p + 1024)) + (*(LAS const f32x4*)(p + 2048) + *(LAS const f32x4*)(p + 3072));
}
DI unsigned peek(GAS unsigned* ctl, int line, int lane) { return __hip_atomic_load(ctl + line * 64 + lane, __ATOMIC_RELAXED, __HIP_MEMORY_SCOPE_AGENT); }
DI void confirm(unsigned seen, GAS unsigned* ctl, int line, unsigned tag, int lane) {
  asm volatile("" : "+v"(seen));
  if (!wave_all(seen == tag)) poll(ctl, line, 0, 64, tag, lane);
  else acquire();
}

DI void mfma16(f32x4& c, const bf16x8& a, const bf16x8& b) { asm volatile("v_mfma_f32_16x16x32_bf16 %0, %1, %2, %0" : "+v"(c) : "v"(a), "v"(b)); }
DI void mfma_drain() { asm volatile("s_nop 7\n\ts_nop 7" ::: "memory"); }
template <int TM, int TN> struct Frag { s16x4 b[TN][2]; bf16x8 a[TM]; };
template <int BN, int NS, int NT, int TM, int TN, bool DEEP, bool OWN, class NMap, class Mid>
DI void mma_loop(LAS char* smem, f32x4 (&acc)[TM][TN], int row0, int lane, NMap&& nt, Mid&& mid) {
  constexpr int RS = BN * 2, BST = BN * 128, NL = TM + 2 * TN, NM = TM * TN;
  typedef Frag<TM, TN> F;
  const int r16 = lane & 15, g = lane >> 4, q = (lane & 15) >> 2, p = lane & 3;
  const int aoff = (row0 + r16) * 64 + ((g ^ aswz(r16)) * 16);
  int boff[TN];
#pragma unroll
  for (int j = 0; j < TN; ++j) boff[j] = NS * 16384 + (8 * g + q) * RS + (((2 * nt(j) + (p >> 1)) ^ bswz<BN>(8 * g + q)) * 16) + 8 * (p & 1);
#pragma unroll
  for (int i = 0; i < TM; ++i)
#pragma unroll
    for (int j = 0; j < TN; ++j) { acc[i][j] = (f32x4){0.f, 0.f, 0.f, 0.f}; asm volatile("" : "+v"(acc[i][j])); }
  auto ld1 = [&](F& f, int slot, int s, int k) {
    if (k == 0 || k > 2 * TN) { const int i = k == 0 ? 0 : k - 2 * TN; f.a[i] = ld_b128(smem + slot * 16384 + aoff + s * 8192 + i * 1024); }
    else { const int j = (k - 1) >> 1, u = (k - 1) & 1; f.b[j][u] = __builtin_amdgcn_ds_read_tr16_b64_v4i16((LAS s16x4*)(smem + slot * BST + boff[j] + (32 * s + 4 * u) * RS)); }
  };
  auto mm1 = [&](const F& f, int m) {
    const int i = m / TN, j = m % TN;
    mfma16(acc[i][j], __builtin_bit_cast(bf16x8, __builtin_shufflevector(f.b[j][0], f.b[j][1], 0, 1, 2, 3, 4, 5, 6, 7)), f.a[i]);
  };
  auto ldall = [&](F& f, int slot, int s) {
#pragma unroll
    for (int k = 0; k < NL; ++k) ld1(f, slot, s, k);
  };
  auto mmall = [&](const F& f) {
#pragma unroll
    for (int m = 0; m < NM; ++m) mm1(f, m);
  };
  auto sub = [&](const F& x, F& u, int slot, int s) {
    constexpr int SP = DEEP ? NM : (NL < NM ? NL : NM - 2);
#pragma unroll
    for (int m = 0; m < NM; ++m) {
      mm1(x, m);
#pragma unroll
      for (int k = (m * NL + SP - 1) / SP; k < NL && k < ((m + 1) * NL + SP - 1) / SP; ++k) ld1(u, slot, s, k);
      __builtin_amdgcn_sched_barrier(0);
    }
  };
  auto nxt = [](int s) { return s == NS - 1 ? 0 : s + 1; };
  lbar();
  if (threadIdx.x < 64) STAMP(BN == 64 ? 1 : BN == 224 ? 2 : 3, 1);
  TS_DECL; TS(0);
  int slot = 0;
  if constexpr (!DEEP) {
    F f0, f1;
    ldall(f0, 0, 0);
#pragma unroll 1
    for (int t = 0; t < NT - 1; ++t) {
      sub(f0, f1, slot, 1);
      TS(32 + t);
      lbar();
      TS(t + 1);
      slot = nxt(slot);
      sub(f1, f0, slot, 0);
    }
    sub(f0, f1, slot, 1);
    lbar();
    TS(NT); TS_OUT(BN == 64 ? 1 : BN == 224 ? 2 : 3);
    mmall(f1);
  } else {
    F f0, f1, g0, g1;
    auto tile = [&](const F& x0, const F& x1, F& u0, F& u1, int t) {
      TS(32 + t);
      bar();
      TS(t + 1);
      slot = nxt(slot);
      sub(x0, u0, slot, 0);
      sub(x1, u1, slot, 1);
    };
    ldall(f0, 0, 0); ldall(f1, 0, 1);
    int t0 = 0;
    if constexpr (OWN) {
      tile(f0, f1, g0, g1, 0);
      mid();
      tile(g0, g1, f0, f1, 1);
      mmall(f0); mmall(f1);
      __builtin_amdgcn_sched_barrier(0);
      TS(34);
      lbar();
      TS(3);
      slot = 3;
      ldall(g0, 3, 0); ldall(g1, 3, 1);
      tile(g0, g1, f0, f1, 3);
      t0 = 4;
    }
#pragma unroll 1
    for (int t = t0; t < NT - 2; t += 2) {
      tile(f0, f1, g0, g1, t);
      tile(g0, g1, f0, f1, t + 1);
    }
    tile(f0, f1, g0, g1, NT - 2);
    lbar();
    TS(NT); TS_OUT(BN == 64 ? 1 : BN == 224 ? 2 : 3);
    mmall(g0); mmall(g1);
  }
  mfma_drain();
#pragma unroll
  for (int i = 0; i < TM; ++i)
#pragma unroll
    for (int j = 0; j < TN; ++j) asm volatile("" : "+v"(acc[i][j]));
}
DI void swap_pair(f32x4& x, f32x4& y) {
#pragma unroll
  for (int e = 0; e < 4; ++e) {
    auto r = __builtin_amdgcn_permlane16_swap(f2u(x[e]), f2u(y[e]), false, false);
    x[e] = u2f(r[0]); y[e] = u2f(r[1]);
  }
}
DI void add_bf8(f32x4& x, f32x4& y, const u32x4& rv) {
  x[0] += bflo(rv[0]); x[1] += bfhi(rv[0]); x[2] += bflo(rv[1]); x[3] += bfhi(rv[1]);
  y[0] += bflo(rv[2]); y[1] += bfhi(rv[2]); y[2] += bflo(rv[3]); y[3] += bfhi(rv[3]);
}
DI u32x4 pack_xy(const f32x4& x, const f32x4& y) { return (u32x4){pk2(x[0], x[1]), pk2(x[2], x[3]), pk2(y[0], y[1]), pk2(y[2], y[3])}; }

DI void dual_softmax(const f32x16& pv, f32x16& e1, f32x16& e2) {
  constexpr float L2E = 1.4426950408889634f;
  float mx = pv[0], mn = pv[0];
#pragma unroll
  for (int r = 1; r < 16; ++r) { mx = __builtin_fmaxf(mx, pv[r]); mn = __builtin_fminf(mn, pv[r]); }
  mx = x32_max(mx); mn = x32_min(mn);
  const float a = -mx * L2E, b = mn * L2E;
  float s1 = 0.f, s2 = 0.f;
#pragma unroll
  for (int r = 0; r < 16; ++r) {
    e1[r] = __builtin_amdgcn_exp2f(__builtin_fmaf(pv[r], L2E, a));
    e2[r] = __builtin_amdgcn_exp2f(__builtin_fmaf(pv[r], -L2E, b));
    s1 += e1[r]; s2 += e2[r];
  }
  s1 = x32_sum(s1); s2 = x32_sum(s2);
  const float r1 = __builtin_amdgcn_rcpf(s1), r2 = __builtin_amdgcn_rcpf(s2);
#pragma unroll
  for (int r = 0; r < 16; ++r) { e1[r] *= r1; e2[r] *= r2; }
}
DI void st16(LAS char* p, const f32x16& v) {
#pragma unroll
  for (int gq = 0; gq < 4; ++gq) *(LAS f32x4*)(p + gq * 1024) = (f32x4){v[4 * gq], v[4 * gq + 1], v[4 * gq + 2], v[4 * gq + 3]};
}
DI void ld16(LAS const char* p, f32x16& v) {
#pragma unroll
  for (int gq = 0; gq < 4; ++gq) { const f32x4 t = *(LAS const f32x4*)(p + gq * 1024); v[4 * gq] = t[0]; v[4 * gq + 1] = t[1]; v[4 * gq + 2] = t[2]; v[4 * gq + 3] = t[3]; }
}
#endif

__global__ __launch_bounds__(512, 2) void block_kernel(const u16* __restrict__ X, const u16* __restrict__ WQKV, const u16* __restrict__ WPHI,
                                                        const u16* __restrict__ WO, const u16* __restrict__ W1, const u16* __restrict__ W2,
                                                        u16* __restrict__ OUT, char* WS) {
#if defined(__HIP_DEVICE_COMPILE__)
  extern __shared__ __attribute__((aligned(16))) char smem_[];
  LAS char* smem = (LAS char*)smem_;
  const int tid = threadIdx.x, wid = __builtin_amdgcn_readfirstlane(tid >> 6);
  int lane = tid & 63;
  const int bid = blockIdx.x, xcd = bid & 7, loc = bid >> 3;
  const int tm = (xcd & 1) * 8 + (loc & 7), gp = (xcd >> 1) * 4 + (loc >> 3);
  const bool loader = wid >= 4;
  const int lw = wid & 3;
  GAS unsigned* ctl = (GAS unsigned*)WS;
  const rsrc_t rws = mkrsrc(WS, WS_END);
  if (wid == 0) STAMP(0, 0);
  unsigned tag = 0;

  int r16, g, cpair, cswz;
  auto relane = [&]() {
    lane = opaque(lane);
    r16 = lane & 15; g = lane >> 4;
    cpair = (g & 1) * 16 + (g >> 1) * 8;
    cswz = ((cpair >> 3) ^ aswz(r16)) * 16;
  };
  {
    constexpr int NT = 16, NS = 3, RS = 384, BST = 192 * 128;
    constexpr int L_PHI = 0;
    constexpr int L_XV = 2 * 17408;
    constexpr int L_VF = L_XV + 16384;
    constexpr int L_XQ = L_VF + 8192;
    constexpr int L_KVT = L_XQ;
    constexpr int L_WF = L_XQ + 16384;
    static_assert(L_WF + 4096 <= 5 * 16384, "phase 2's B ring is filled during this epilogue");
    const int wm = (wid >> 1) & 1, hh = wid & 1, mi = wid >> 2;
    const int r32 = lane & 31, h = lane >> 5, blk = (lane >> 4) & 1, q = (lane & 15) >> 2, p = lane & 3;
    const int head = gp * 2 + hh;
    const unsigned kvp_grp = WS_KVP + gp * 16 * KVP_BYTES;
    LAS char* const xk = smem + L_PHI + hh * 17408 + (wm * 64 + 32) * 136 + lane * 16;
    bf16x8 wf[2];
    f32x16 fq, fk, fv;
    auto load_wf = [&]() {
#pragma unroll
      for (int s = 0; s < 2; ++s) {
        s16x8 w;
#pragma unroll
        for (int j = 0; j < 8; ++j) w[j] = (short)WPHI[head * 1024 + (16 * s + 8 * (j >> 2) + 4 * h + (j & 3)) * 32 + r32];
        wf[s] = __builtin_bit_cast(bf16x8, w);
      }
    };
    if (loader) {
      const rsrc_t rb = mkrsrc(WQKV, D_ * 3 * D_ * 2), ro = mkrsrc(WO, D_ * D_ * 2);
      Loader<192, NS, NT, true> L;
      L.init(smem, mkrsrc(X, S_ * D_ * 2), rb, tm * 128 * D_ * 2, D_ * 2, gp * 128, 3 * D_ * 2, 2048, lw, lane, ROT1);
      L.issueA(0); L.issueB(0); L.issueA(1); L.issueB(1);
      tag = read_tag(WS);
      __builtin_amdgcn_s_setprio(3);
      if (wid == 4) STAMP(4, 1);
      L.step();
      colsum_tile<192>(smem + NS * 16384, rws, WS_CS1 + (gp * 16 + tm) * 768, lw, lane);
      if (lw == 1 && lane < 4) st128(*(LAS const u32x4*)(smem + L_XST + lane * 16), rws, WS_XST + (tm * 128 + gp * 8) * 8 + lane * 16);
      L.steps(3);
      L.step<0>();
      if (lane == 0) set_flag(ctl, C_CS1 + gp, tm * 4 + lw, tag);
      if (lw == 1 && lane < 4) set_flag(ctl, C_ST + tm, gp * 4 + lane, tag);
      L.steps(loc & 3);
      load_wtile<224>(smem + L_WT, mkrsrc(W1, D_ * FF_ * 2), tm * 64 * (FF_ * 2) + gp * 448, FF_ * 2, 128, lw, lane);
      touch_lines(smem, ro, bid * 8192, lane < 16 ? lw * 16 + lane : 4096, 64);
      L.step();
      L.step<18>();
      L.steps(3 - (loc & 3));
      const unsigned seen = peek(ctl, C_CS1 + gp, lane), seen2 = peek(ctl, C_ST + tm, lane);
      L.steps(2);
      confirm(seen, ctl, C_CS1 + gp, tag, lane);
      confirm(seen2, ctl, C_ST + tm, tag, lane);
      u32x4 gt[4];
#pragma unroll
      for (int j = 0; j < 4; ++j) gt[j] = ld128(rws, WS_CS1 + (gp * 16 + lw * 4) * 768 + lane * 16, j * 768);
      const u32x4 xs = ld128(rws, WS_XST + tm * 1024 + lane * 16, 0);
      L.steps(2);
      *(LAS f32x4*)(smem + L_CSS + lw * 1024 + lane * 16) = sum4(gt);
      if (lw == 1) *(LAS u32x4*)(smem + L_RST + lane * 16) = xs;
      wait_vm<10>(); lbar();
      if (lw == 0) *(LAS f32x4*)(smem + L_CS + lane * 16) = lds_sum4(smem + L_CSS + lane * 16);
      wait_vm<0>(); lbar();
      lbar();
      bar();
      __builtin_amdgcn_s_setprio(0);
      ld16(xk, fk);
      wf[0] = ld_b128(smem + L_WF + hh * 2048 + lane * 16);
      wf[1] = ld_b128(smem + L_WF + hh * 2048 + 1024 + lane * 16);
      ld16(smem + L_XV + lw * 4096 + lane * 16, fv);
    } else {
      {
        const int row = tm * 128 + gp * 8 + wid * 2;
        const u32x4* px = (const u32x4*)(X + (size_t)row * D_);
        u32x4 v[4] = {px[lane], px[lane + 64], px[lane + 128], px[lane + 192]};
        load_wf();
        tag = read_tag(WS);
        f32x2 st[2];
#pragma unroll
        for (int r = 0; r < 2; ++r) {
          float f[16];
#pragma unroll
          for (int i = 0; i < 4; ++i) { f[2 * i] = bflo(v[2 * r][i]); f[2 * i + 1] = bfhi(v[2 * r][i]); f[8 + 2 * i] = bflo(v[2 * r + 1][i]); f[9 + 2 * i] = bfhi(v[2 * r + 1][i]); }
          float s = 0.f;
#pragma unroll
          for (int i = 0; i < 16; ++i) s += f[i];
          const float mean = wave_sum(s) * (1.0f / D_);
          float qq = 0.f;
#pragma unroll
          for (int i = 0; i < 16; ++i) { const float d = f[i] - mean; qq += d * d; }
          st[r] = (f32x2){mean, 1.0f / __builtin_sqrtf(wave_sum(qq) * (1.0f / D_) + LN_EPS)};
        }
        if (lane == 0) *(LAS f32x4*)(smem + L_XST + wid * 16) = (f32x4){st[0][0], st[0][1], st[1][0], st[1][1]};
      }
      int aoff[4];
#pragma unroll
      for (int s = 0; s < 4; ++s) aoff[s] = (wm * 64 + r32) * 128 + (((2 * s + h) ^ ((r32 >> 1) & 7)) * 16);
      const int boff = NS * 16384 + (8 * h + q) * RS + (((hh * 4 + blk * 2 + (p >> 1)) ^ bswz<192>(q)) * 16) + 8 * (p & 1);
      f32x16 aq[2], ak[2], av[2];
#pragma unroll
      for (int i = 0; i < 2; ++i)
#pragma unroll
        for (int r = 0; r < 16; ++r) { aq[i][r] = 0.f; ak[i][r] = 0.f; av[i][r] = 0.f; }
      bf16x8 a0[2], b0[3], a1[2], b1[3];
      auto ldf = [&](bf16x8(&a)[2], bf16x8(&b)[3], int slot, int s) {
        LAS const char* pa = smem + slot * 16384 + aoff[s];
        LAS const char* pb = smem + slot * BST + boff + (16 * s) * RS;
        a[0] = ld_b128(pa);
#pragma unroll
        for (int j = 0; j < 3; ++j) b[j] = ld_tr2(pb + j * 128, pb + 4 * RS + j * 128);
        a[1] = ld_b128(pa + 4096);
      };
      auto mma = [&](bf16x8(&a)[2], bf16x8(&b)[3]) {
#pragma unroll
        for (int i = 0; i < 2; ++i) {
          aq[i] = MFMA32(b[0], a[i], aq[i]);
          ak[i] = MFMA32(b[1], a[i], ak[i]);
          av[i] = MFMA32(a[i], b[2], av[i]);
        }
      };
      auto sched = [&]() {
#pragma unroll
        for (int i = 0; i < 6; ++i) {
          __builtin_amdgcn_sched_group_barrier(0x8, 1, 0);
          if (i < 4) __builtin_amdgcn_sched_group_barrier(0x100, 2, 0);
        }
        __builtin_amdgcn_sched_barrier(0);
      };
      if (wid == 0) STAMP(4, 0);
      lbar();
      if (wid == 0) STAMP(0, 1);
      int slot = 0;
      TS_DECL; TS(0);
      ldf(a0, b0, 0, 0);
#pragma unroll 1
      for (int t = 0; t < NT - 1; ++t) {
        ldf(a1, b1, slot, 1); mma(a0, b0); sched();
        ldf(a0, b0, slot, 2); mma(a1, b1); sched();
        ldf(a1, b1, slot, 3); mma(a0, b0); sched();
        TS(32 + t);
        lbar();
        TS(t + 1);
        slot = (slot == NS - 1) ? 0 : slot + 1;
        ldf(a0, b0, slot, 0); mma(a1, b1); sched();
      }
      ldf(a1, b1, slot, 1); mma(a0, b0); sched();
      ldf(a0, b0, slot, 2); mma(a1, b1); sched();
      ldf(a1, b1, slot, 3); mma(a0, b0); sched();
      lbar();
      TS(NT); TS_OUT(0);
      ak[1] = MFMA32(b1[1], a1[1], ak[1]);
      av[1] = MFMA32(a1[1], b1[2], av[1]);
      ak[0] = MFMA32(b1[1], a1[0], ak[0]);
      av[0] = MFMA32(a1[0], b1[2], av[0]);
      aq[1] = MFMA32(b1[0], a1[1], aq[1]);
      aq[0] = MFMA32(b1[0], a1[0], aq[0]);
      if (wid == 0) STAMP(0, 2);
      if (wm == 0) {
        *(LAS bf16x8*)(smem + L_WF + hh * 2048 + lane * 16) = wf[0];
        *(LAS bf16x8*)(smem + L_WF + hh * 2048 + 1024 + lane * 16) = wf[1];
      }
      st16(xk, ak[1]);
      st16(smem + L_XV + wid * 4096 + lane * 16, av[1]);
      lbar();
      if (wid == 0) STAMP(4, 4);
      fq = aq[0]; fk = ak[0]; fv = av[0];
      st16(smem + L_XQ + wid * 4096 + lane * 16, aq[1]);
    }
    const int rbase = wm * 64 + mi * 32;
    LAS const float* cs = (LAS const float*)(smem + L_CS) + hh * 32;
    LAS const f32x2* rst = (LAS const f32x2*)(smem + L_RST) + rbase;
    auto feature = [&](f32x16& x, int part, f32x16& e1, f32x16& e2) {
      const f32x2 ms = rst[r32];
      const float a = ms[1], b = -ms[0] * ms[1];
      f32x4 c[4];
#pragma unroll
      for (int gq = 0; gq < 4; ++gq) c[gq] = *(LAS const f32x4*)(cs + part * 64 + 8 * gq + 4 * h);
#pragma unroll
      for (int gq = 0; gq < 4; ++gq) {
#pragma unroll
        for (int e = 0; e < 4; ++e) x[4 * gq + e] = __builtin_fmaf(x[4 * gq + e], a, b * c[gq][e]);
      }
      f32x16 pp;
#pragma unroll
      for (int r = 0; r < 16; ++r) pp[r] = 0.f;
      bf16x8 x0 = pack8(x, 0), x1 = pack8(x, 1);
      pp = MFMA32(wf[0], x0, pp);
      pp = MFMA32(wf[1], x1, pp);
      dual_softmax(pp, e1, e2);
    };
    if (wid == 0) STAMP(4, 5);
    {
      const float csv = cs[128 + r32];
#pragma unroll
      for (int gq = 0; gq < 4; ++gq) {
        const f32x4 s01 = *(LAS const f32x4*)(rst + 8 * gq + 4 * h), s23 = *(LAS const f32x4*)(rst + 8 * gq + 4 * h + 2);
        fv[4 * gq] = __builtin_fmaf(-s01[0], csv, fv[4 * gq]) * s01[1];
        fv[4 * gq + 1] = __builtin_fmaf(-s01[2], csv, fv[4 * gq + 1]) * s01[3];
        fv[4 * gq + 2] = __builtin_fmaf(-s23[0], csv, fv[4 * gq + 2]) * s23[1];
        fv[4 * gq + 3] = __builtin_fmaf(-s23[2], csv, fv[4 * gq + 3]) * s23[3];
      }
      LAS char* vd = smem + (loader ? L_XV + lw * 4096 : L_VF + lw * 2048) + lane * 16;
      *(LAS bf16x8*)vd = pack8(fv, 0);
      *(LAS bf16x8*)(vd + 1024) = pack8(fv, 1);
      f32x16 e1, e2;
      feature(fk, 1, e1, e2);
      LAS char* wr = smem + L_PHI + hh * 17408 + (rbase + r32) * 136 + h * 8;
#pragma unroll
      for (int gq = 0; gq < 4; ++gq) {
        *(LAS u32x2*)(wr + gq * 16) = (u32x2){pk2(e1[4 * gq], e1[4 * gq + 1]), pk2(e1[4 * gq + 2], e1[4 * gq + 3])};
        *(LAS u32x2*)(wr + 64 + gq * 16) = (u32x2){pk2(e2[4 * gq], e2[4 * gq + 1]), pk2(e2[4 * gq + 2], e2[4 * gq + 3])};
      }
    }
    if (wid == 0) STAMP(4, 7);
    if (wid == 4) STAMP(4, 9);
    lbar();
    if (wid == 0) STAMP(4, 8);
    bf16x8 pf[2][2];
    auto qpath = [&]() {
      f32x16 e1, e2;
      feature(fq, 0, e1, e2);
      pf[0][0] = pack8(e1, 0); pf[0][1] = pack8(e1, 1);
      pf[1][0] = pack8(e2, 0); pf[1][1] = pack8(e2, 1);
    };
    if (loader) {
      ld16(smem + L_XQ + lw * 4096 + lane * 16, fq);
    } else {
      f32x16 kv, zz;
#pragma unroll
      for (int r = 0; r < 16; ++r) { kv[r] = 0.f; zz[r] = 0.f; }
      const s16x8 one_ = {0x3f80, 0x3f80, 0x3f80, 0x3f80, 0x3f80, 0x3f80, 0x3f80, 0x3f80};
      const bf16x8 ones = __builtin_bit_cast(bf16x8, one_);
      LAS const char* rd = smem + L_PHI + hh * 17408 + (4 * h + q) * 136 + wm * 64 + blk * 32 + p * 8;
#pragma unroll
      for (int mt = 0; mt < 4; ++mt)
#pragma unroll
        for (int st = 0; st < 2; ++st) {
          const bf16x8 pa = ld_tr2(rd + (32 * mt + 16 * st) * 136, rd + (32 * mt + 16 * st + 8) * 136);
          const bf16x8 vf = ld_b128(smem + ((mt & 1) ? L_XV + ((mt >> 1) * 2 + hh) * 4096 : L_VF + ((mt >> 1) * 2 + hh) * 2048) + st * 1024 + lane * 16);
          kv = MFMA32(pa, vf, kv);
          zz = MFMA32(pa, ones, zz);
        }
      const int kb = kvp_grp + tm * KVP_BYTES;
      st128(__builtin_bit_cast(u32x4, pack8(kv, 0)), rws, kb + hh * 4096 + wm * 2048 + lane * 16);
      st128(__builtin_bit_cast(u32x4, pack8(kv, 1)), rws, kb + hh * 4096 + wm * 2048 + 1024 + lane * 16);
      if (r32 == 0) {
#pragma unroll
        for (int gq = 0; gq < 4; ++gq)
          st128(__builtin_bit_cast(u32x4, (f32x4){zz[4 * gq], zz[4 * gq + 1], zz[4 * gq + 2], zz[4 * gq + 3]}), rws, kb + 8192 + hh * 256 + (h * 2 + wm) * 64 + gq * 16);
      }
      if (wid == 0) STAMP(0, 3);
    }
    qpath();
    if (loader) {
      Loader<64, 5, 16, false, 1> L2;
      L2.init(smem, rws, mkrsrc(WO, D_ * D_ * 2), WS_O + tm * 128 * D_ * 2, 0, gp * 128, D_ * 2, 128, lw, lane, -1);
      L2.issueB(0); L2.issueB(1); L2.issueB(2); L2.issueB(3); L2.issueB(4);
    } else {
      wait_vm<0>();
    }
    bar();
    if (wid == 0) {
      if (lane == 0) set_flag(ctl, C_KV + gp, tm, tag);
      STAMP(0, 4);
      poll(ctl, C_KV + gp, 0, 16, tag, lane);
    }
    bar();
    if (wid == 0) STAMP(0, 6);
    {
      u32x4 t[16];
#pragma unroll
      for (int j = 0; j < 16; ++j) t[j] = ld128(rws, kvp_grp + tid * 16, j * KVP_BYTES);
      u32x4 tz[16];
      if (tid < 32) {
#pragma unroll
        for (int j = 0; j < 16; ++j) tz[j] = ld128(rws, kvp_grp + 8192 + tid * 16, j * KVP_BYTES);
      }
      f32x4 x = {0.f, 0.f, 0.f, 0.f}, y = x;
#pragma unroll
      for (int j = 0; j < 16; ++j) add_bf8(x, y, t[j]);
      *(LAS u32x4*)(smem + L_KVT + tid * 16) = pack_xy(x, y);
      if (tid < 32) {
        f32x4 z = __builtin_bit_cast(f32x4, tz[0]);
#pragma unroll
        for (int j = 1; j < 16; ++j) z += __builtin_bit_cast(f32x4, tz[j]);
        *(LAS u32x2*)(smem + L_KVT + 8192 + tid * 8) = (u32x2){pk2(z[0], z[1]), pk2(z[2], z[3])};
      }
    }
    lbar();
    if (wid == 0) STAMP(0, 7);
    {
      f32x16 o, dn;
#pragma unroll
      for (int r = 0; r < 16; ++r) { o[r] = 0.f; dn[r] = 0.f; }
#pragma unroll
      for (int ft = 0; ft < 2; ++ft)
#pragma unroll
        for (int s = 0; s < 2; ++s) {
          const bf16x8 ka = ld_b128(smem + L_KVT + hh * 4096 + (ft * 2 + s) * 1024 + lane * 16);
          const bf16x8 za = ld_b128(smem + L_KVT + 8192 + hh * 128 + (h * 2 + ft) * 32 + s * 16);
          o = MFMA32(ka, pf[ft][s], o);
          dn = MFMA32(za, pf[ft][s], dn);
        }
      const float rd = 1.0f / (dn[0] + 1e-6f);
      u32x4 c01, c23;
      {
        auto r0 = __builtin_amdgcn_permlane32_swap(pk2(o[0] * rd, o[1] * rd), pk2(o[4] * rd, o[5] * rd), false, false);
        auto r1 = __builtin_amdgcn_permlane32_swap(pk2(o[2] * rd, o[3] * rd), pk2(o[6] * rd, o[7] * rd), false, false);
        auto r2 = __builtin_amdgcn_permlane32_swap(pk2(o[8] * rd, o[9] * rd), pk2(o[12] * rd, o[13] * rd), false, false);
        auto r3 = __builtin_amdgcn_permlane32_swap(pk2(o[10] * rd, o[11] * rd), pk2(o[14] * rd, o[15] * rd), false, false);
        c01 = (u32x4){r0[0], r1[0], r0[1], r1[1]}; c23 = (u32x4){r2[0], r3[0], r2[1], r3[1]};
      }
#pragma unroll
      for (int e = 0; e < 4; ++e) { auto r = __builtin_amdgcn_permlane16_swap(c01[e], c23[e], false, false); c01[e] = r[0]; c23[e] = r[1]; }
      const int ob = WS_O + (tm * 32 + head) * 8192 + (rbase + (lane & 15)) * 64 + (((2 * blk + h) ^ aswz(lane & 15)) * 16);
      st128(c01, rws, ob);
      st128(c23, rws, ob + 1024);
    }
    wait_vm<0>();
    lbar();
    if (tid == 0) set_flag(ctl, C_O + tm, gp, tag);
    if (wid == 0) STAMP(0, 8);
  }

  const int kh = gp >> 3;
  if (loader) {
    __builtin_amdgcn_s_setprio(3);
    relane();
    Loader<64, 5, 16, false, 1> L2;
    L2.init(smem, rws, mkrsrc(WO, D_ * D_ * 2), WS_O + tm * 128 * D_ * 2, 0, gp * 128, D_ * 2, 128, lw, lane, -1);
    colsum_tile<224>(smem + L_WT, rws, WS_CS3 + (gp * 16 + tm) * 896, lw, lane);
    poll(ctl, C_O + tm, 0, 16, tag, lane);
    if (wid == 4) STAMP(1, 0);
    L2.issueA(0); L2.issueA(1); L2.issueA(2); L2.issueA(3); L2.issueA(4);
    if (bid == 0 && wid == 4 && lane == 0) __hip_atomic_store(ctl + C_EPOCH * 64, tag, __ATOMIC_RELAXED, __HIP_MEMORY_SCOPE_AGENT);
    wait_vm<16>(); lbar();
    wait_vm<12>(); lbar();
    L2.tn = 5;
    L2.step<8>();
    L2.step<4 + 6>();
    L2.steps(9);
    L2.finish();
    if (lane == 0) set_flag(ctl, C_CS3 + gp, tm * 4 + lw, tag);
    const rsrc_t r2 = mkrsrc(W2, FF_ * D_ * 2);
    {
      relane();
      Loader<224, 3, 16, false> L;
      L.init(smem, rws, mkrsrc(W1, D_ * FF_ * 2), WS_H + tm * 128 * D_ * 2, 0, gp * 448, FF_ * 2, 128, lw, lane, -1);
      bar();
      L.issueB(0); L.issueB(1);
      bar();
      if (wid == 4 && lane == 0) set_flag(ctl, C_H + tm, gp, tag);
      poll(ctl, C_H + tm, 0, 16, tag, lane);
      if (wid == 4) STAMP(2, 0);
      L.issueA(0); L.issueA(1);
      u32x4 gt[4], hs[4];
#pragma unroll
      for (int j = 0; j < 4; ++j) hs[j] = ld128(rws, WS_HST + tm * 16384 + lw * 4096 + lane * 16, j * 1024);
      const unsigned seen = peek(ctl, C_CS3 + gp, lane);
      L.step<4 + 5>();
      L.step<5 + 11>();
      confirm(seen, ctl, C_CS3 + gp, tag, lane);
#pragma unroll
      for (int j = 0; j < 4; ++j) gt[j] = ld128(rws, WS_CS3 + (gp * 16 + lw * 4) * 896 + lane * 16, j * 896);
      *(LAS f32x4*)(smem + L_CSS + 4096 + lw * 1024 + lane * 16) = sum4(hs);
      L.step();
      if (lw == 1) {
        const f32x4 sum = lds_sum4(smem + L_CSS + 4096 + lane * 16);
        const float m0 = sum[0] * (1.0f / D_), m1 = sum[2] * (1.0f / D_);
        const float v0 = __builtin_fmaxf(sum[1] * (1.0f / D_) - m0 * m0, 0.f), v1 = __builtin_fmaxf(sum[3] * (1.0f / D_) - m1 * m1, 0.f);
        *(LAS f32x4*)(smem + L_RST + lane * 16) = (f32x4){m0, 1.0f / __builtin_sqrtf(v0 + LN_EPS), m1, 1.0f / __builtin_sqrtf(v1 + LN_EPS)};
      }
      L.step();
      *(LAS f32x4*)(smem + L_CSS + lw * 1024 + lane * 16) = sum4(gt);
      L.step();
      if (lw == 0) *(LAS f32x4*)(smem + L_CS + lane * 16) = lds_sum4(smem + L_CSS + lane * 16);
      L.steps(loc & 7);
      touch_lines(smem, r2, bid * 28672, lane < 56 ? lw * 56 + lane : 4096, 224);
      L.step();
      L.step<12>();
      L.steps(7 - (loc & 7));
      L.finish();
    }
    {
      relane();
      Loader<128, 4, 28, false, 1> L;
      L.init(smem, rws, r2, WS_H1 + (tm * 112 + kh * 56) * 8192, 0, kh * 1792 * D_ * 2 + gp * 128, D_ * 2, kh ? -1024 : 1024, lw, lane, (7 * (gp & 7) + 1) >> 1);
      lbar();
      L.issueB(0); L.issueB(1); L.issueB(2); L.issueB(3);
      wait_vm<12>(); bar();
      wait_vm<8>(); bar();
      wait_vm<4>(); bar();
      if (wid == 4 && lane == 0) set_flag(ctl, C_H1 + tm, gp, tag);
      L.issueB(4);
      poll(ctl, C_H1 + tm, kh * 8, 8, tag, lane);
      if (wid == 4) STAMP(3, 0);
      L.issueA(3); L.issueA(4);
      L.tn = 5;
      L.step<4>();
      L.steps(22);
      L.finish();
      bar();
    }
  } else {
    relane();
    {
      const int row = tm * 128 + wid * 32 + r16, col = gp * 64 + cpair;
      u32x4 res[2][2];
#pragma unroll
      for (int i = 0; i < 2; ++i)
#pragma unroll
        for (int j = 0; j < 2; ++j) res[i][j] = *(const u32x4*)(X + (size_t)(row + i * 16) * D_ + col + j * 32);
      f32x4 acc[2][4];
      mma_loop<64, 5, 16, 2, 4, true, false>(smem, acc, wid * 32, lane, [](int j) { return j; }, []() {});
      if (wid == 0) STAMP(1, 2);
#pragma unroll
      for (int i = 0; i < 2; ++i) {
        float s = 0.f, qq = 0.f;
#pragma unroll
        for (int j = 0; j < 2; ++j) {
          f32x4 x = acc[i][2 * j], y = acc[i][2 * j + 1];
          swap_pair(x, y);
          add_bf8(x, y, res[i][j]);
          const u32x4 o = pack_xy(x, y);
          st128(o, rws, WS_H + (tm * 32 + gp * 2 + j) * 8192 + (wid * 32 + i * 16 + r16) * 64 + cswz);
#pragma unroll
          for (int e = 0; e < 4; ++e) { const float lo = bflo(o[e]), hi = bfhi(o[e]); s += lo + hi; qq = __builtin_fmaf(lo, lo, __builtin_fmaf(hi, hi, qq)); }
        }
        s = x32_sum(x16_sum(s)); qq = x32_sum(x16_sum(qq));
        if (g == 0) __builtin_amdgcn_raw_buffer_store_b64(__builtin_bit_cast(u32x2, (f32x2){s, qq}), rws, WS_HST + ((tm * 16 + gp) * 128 + wid * 32 + i * 16 + r16) * 8, 0, SC1);
      }
      wait_vm<0>();
      bar();
      if (wid == 0) STAMP(1, 3);
    }
    const int wm = wid >> 1, wn = wid & 1;
    relane();
    {
      f32x4 acc[4][7];
      mma_loop<224, 3, 16, 4, 7, false, false>(smem, acc, wm * 64, lane, [&](int j) { return j < 6 ? wn * 8 + j : 6 + wn; }, []() {});
      if (wid == 0) STAMP(2, 2);
      relane();
      f32x4 cs[7];
#pragma unroll
      for (int j = 0; j < 7; ++j) cs[j] = *(LAS const f32x4*)(smem + L_CS + ((j < 6 ? wn * 8 + j : 6 + wn) * 16 + g * 4) * 4);
      const int par = gp & 1, hb = WS_H1 + (tm * 112 + gp * 7) * 8192;
#pragma unroll
      for (int i = 0; i < 4; ++i) {
        const f32x2 ms = *(LAS const f32x2*)(smem + L_RST + (wm * 64 + i * 16 + r16) * 8);
        const float a = ms[1], b = -ms[0] * ms[1];
#pragma unroll
        for (int j = 0; j < 7; ++j)
#pragma unroll
          for (int e = 0; e < 4; ++e) acc[i][j][e] = __builtin_fmaxf(__builtin_fmaf(acc[i][j][e], a, b * cs[j][e]), 0.f);
        const int ro = (wm * 64 + i * 16 + r16) * 64 + cswz;
#pragma unroll
        for (int j = 0; j < 3; ++j) {
          f32x4 x = acc[i][2 * j], y = acc[i][2 * j + 1];
          swap_pair(x, y);
          const u32x4 o = pack_xy(x, y);
          const int sl = wn * 4 + j;
          st128(o, rws, hb + sl * 8192 + ro);
          if ((unsigned)(sl - par) < 6u) *(LAS u32x4*)(smem + (sl - par) * 8192 + ro) = o;
        }
        if (i & 1) {
          f32x4 x = acc[i - 1][6], y = acc[i][6];
          swap_pair(x, y);
          const u32x4 o = pack_xy(x, y);
          const int r3 = (wm * 64 + (i - 1 + (g & 1)) * 16 + r16) * 64 + (((2 * wn + (g >> 1)) ^ aswz(r16)) * 16);
          st128(o, rws, hb + 3 * 8192 + r3);
          *(LAS u32x4*)(smem + (3 - par) * 8192 + r3) = o;
        }
      }
      if (wid == 0) STAMP(2, 3);
    }
    relane();
    {
      const int row = tm * 128 + wm * 64 + r16, col = gp * 64 + wn * 32 + cpair;
      u32x4 res[4];
      f32x4 acc[4][4];
      mma_loop<128, 4, 28, 4, 4, true, true>(smem, acc, wm * 64, lane, [&](int j) { return (j >> 1) * 4 + wn * 2 + (j & 1); }, [&]() {
        wait_vm<0>();
        if (wid == 0) STAMP(2, 4);
#pragma unroll
        for (int i = 0; i < 4; ++i) res[i] = ld128(rws, WS_H + (tm * 32 + gp * 2 + wn) * 8192 + (wm * 64 + i * 16 + r16) * 64 + cswz, 0);
      });
      if (wid == 0) STAMP(3, 2);
      const int pb = WS_P4 + (tm * 16 + gp) * 16384 + wid * 4096 + lane * 16;
#pragma unroll
      for (int i = 0; i < 4; ++i) st128(pack_xy(acc[i][2], acc[i][3]), rws, pb + i * 1024);
      wait_vm<0>();
      if (lane == 0) set_flag(ctl, C_P4 + tm, gp * 4 + wid, tag);
      if (wid == 0) STAMP(3, 3);
      poll(ctl, C_P4 + tm, (gp ^ 8) * 4 + wid, 1, tag, lane);
      if (wid == 0) STAMP(3, 4);
      const int qb = WS_P4 + (tm * 16 + (gp ^ 8)) * 16384 + wid * 4096 + lane * 16;
      u32x4 t[4];
#pragma unroll
      for (int i = 0; i < 4; ++i) t[i] = ld128(rws, qb + i * 1024, 0);
#pragma unroll
      for (int i = 0; i < 4; ++i) {
        f32x4 x = acc[i][0], y = acc[i][1];
        add_bf8(x, y, t[i]);
        swap_pair(x, y);
        add_bf8(x, y, res[i]);
        *(u32x4*)(OUT + (size_t)(row + i * 16) * D_ + col) = pack_xy(x, y);
      }
      if (wid == 0) STAMP(3, 5);
    }
  }
#endif
}
#if defined(__HIP_DEVICE_COMPILE__)
#pragma clang attribute pop
#endif

extern "C" void kernel_launch(void* const* d_in, const int* in_sizes, int n_in, void* d_out, int out_size, void* d_ws, size_t ws_size,
                              hipStream_t stream) {
  (void)hipFuncSetAttribute((const void*)block_kernel, hipFuncAttributeMaxDynamicSharedMemorySize, LDS_BYTES);
  block_kernel<<<256, 512, LDS_BYTES, stream>>>((const u16*)d_in[0], (const u16*)d_in[1], (const u16*)d_in[2], (const u16*)d_in[3],
                                                (const u16*)d_in[5], (const u16*)d_in[6], (u16*)d_out, (char*)d_ws);
}
```

```cpp
#include <hip/hip_runtime.h>
#include <hip/hip_bf16.h>

#define LAS __attribute__((address_space(3)))
#define GAS __attribute__((address_space(1)))
#define DI __device__ __forceinline__
typedef __bf16 bf16x8 __attribute__((ext_vector_type(8)));
typedef __bf16 bf16x2 __attribute__((ext_vector_type(2)));
typedef short s16x4 __attribute__((ext_vector_type(4)));
typedef short s16x8 __attribute__((ext_vector_type(8)));
typedef float f32x2 __attribute__((ext_vector_type(2)));
typedef float f32x4 __attribute__((ext_vector_type(4)));
typedef unsigned u32x2 __attribute__((ext_vector_type(2)));
typedef unsigned u32x4 __attribute__((ext_vector_type(4)));
typedef unsigned short u16;

constexpr int S_ = 2048, D_ = 1024, FF_ = 3584;
constexpr float LN_EPS = 1e-5f;

constexpr int C_EPOCH = 0, C_TMO = 1, C_ST = 2, C_CS1 = 18, C_CS3 = 34, C_KV = 50, C_O = 66, C_H = 82, C_H1 = 98, C_P4 = 114, C_LINES = 130;
constexpr unsigned WS_XST = 36864;
constexpr unsigned WS_CS1 = WS_XST + S_ * 8;
constexpr unsigned WS_CS3 = WS_CS1 + 16 * 16 * 192 * 4;
constexpr unsigned WS_HST = WS_CS3 + 16 * 16 * 224 * 4;
constexpr unsigned KVP_BYTES = 8192 + 512;
constexpr unsigned WS_KVP = WS_HST + S_ * 16 * 8;
#define BLOCKED 1
constexpr unsigned WS_O = WS_KVP + 256 * KVP_BYTES;
constexpr unsigned WS_H = WS_O + S_ * D_ * 2;
constexpr unsigned WS_H1 = WS_H + S_ * D_ * 2;
constexpr unsigned WS_P4 = WS_O, WS_END = WS_H1 + S_ * FF_ * 2;
static_assert(C_LINES * 256 <= (int)WS_XST && WS_O % 256 == 0 && WS_KVP % 256 == 0, "");

constexpr int L_WT = 120 * 1024;
constexpr int L_TAB = 149 * 1024;
constexpr int L_DUMMY = L_TAB;
constexpr int L_RST = L_TAB + 256;
constexpr int L_CS = L_RST + 1024;
constexpr int L_CSS = L_CS + 1024;
constexpr int LDS_BYTES = L_CSS + 8192;
static_assert(LDS_BYTES <= 160 * 1024, "");

#define STAMP(k, i) do {} while (0)
#define TS_DECL
#define TS(t)
#define TS_OUT(k)

#if defined(__HIP_DEVICE_COMPILE__)
#pragma clang attribute push(__attribute__((target("no-packed-fp32-ops"))), apply_to = function)
typedef __amdgpu_buffer_rsrc_t rsrc_t;
DI float u2f(unsigned u) { return __builtin_bit_cast(float, u); }
DI unsigned f2u(float f) { return __builtin_bit_cast(unsigned, f); }
DI bool wave_all(bool p) { return __builtin_amdgcn_ballot_w64(p) == ~0ull; }
#define CODE_AHEAD 36864
constexpr int SC1 = 16;
template <int N> DI void wait_vm() { asm volatile("s_waitcnt vmcnt(%0)" ::"n"(N) : "memory"); }
DI void wait_lgkm0() { asm volatile("s_waitcnt lgkmcnt(0)" ::: "memory"); }
DI void bar() { asm volatile("" ::: "memory"); __builtin_amdgcn_s_barrier(); asm volatile("" ::: "memory"); }
DI void lbar() { wait_lgkm0(); bar(); }
DI unsigned pk2(float a, float b) { f32x2 v = {a, b}; return __builtin_bit_cast(unsigned, __builtin_convertvector(v, bf16x2)); }
DI float bflo(unsigned u) { return u2f(u << 16); }
DI float bfhi(unsigned u) { return u2f(u & 0xffff0000u); }
DI bf16x8 ld_b128(LAS const char* p) { return *(LAS const bf16x8*)p; }
DI bf16x8 ld_tr2(LAS const char* p0, LAS const char* p1) {
  s16x4 a = __builtin_amdgcn_ds_read_tr16_b64_v4i16((LAS s16x4*)p0);
  s16x4 b = __builtin_amdgcn_ds_read_tr16_b64_v4i16((LAS s16x4*)p1);
  return __builtin_bit_cast(bf16x8, __builtin_shufflevector(a, b, 0, 1, 2, 3, 4, 5, 6, 7));
}
DI float x32_sum(float v) { auto r = __builtin_amdgcn_permlane32_swap(f2u(v), f2u(v), false, false); return u2f(r[0]) + u2f(r[1]); }
DI float x16_sum(float v) { auto r = __builtin_amdgcn_permlane16_swap(f2u(v), f2u(v), false, false); return u2f(r[0]) + u2f(r[1]); }
template <int CTRL> DI float dpp_add(float v) { return v + u2f(__builtin_amdgcn_update_dpp(0, (int)f2u(v), CTRL, 0xf, 0xf, true)); }
DI float half_sum(float v) {
  v = dpp_add<0xB1>(v);
  v = dpp_add<0x4E>(v);
  v = dpp_add<0x141>(v);
  v = dpp_add<0x140>(v);
  return x16_sum(v);
}
#define MFMA16(a, b, c) __builtin_amdgcn_mfma_f32_16x16x32_bf16((a), (b), (c), 0, 0, 0)
DI rsrc_t mkrsrc(const void* p, unsigned bytes) { return __builtin_amdgcn_make_buffer_rsrc((void*)p, 0, bytes, 0x00020000); }
DI void st128(u32x4 v, rsrc_t r, int off) { __builtin_amdgcn_raw_buffer_store_b128(v, r, off, 0, SC1); }
DI u32x4 ld128(rsrc_t r, int off, int soff) { return __builtin_amdgcn_raw_buffer_load_b128(r, off, soff, SC1); }

DI unsigned read_tag(const char* ws) {
  unsigned e;
  asm volatile("s_load_dword %0, %1, 0x0\n\ts_waitcnt lgkmcnt(0)" : "=s"(e) : "s"(ws) : "memory");
  return e + 1u;
}
DI void set_flag(GAS unsigned* ctl, int line, int word, unsigned tag) { __hip_atomic_store(ctl + line * 64 + word, tag, __ATOMIC_RELAXED, __HIP_MEMORY_SCOPE_AGENT); }
DI void acquire() {
  asm volatile("" ::: "memory");
}
DI void poll(GAS unsigned* ctl, int line, int lo, int n, unsigned tag, int lane) {
  GAS unsigned* p = ctl + line * 64 + lo + (lane < n ? lane : 0);
  unsigned spins = 0;
  while (!wave_all(__hip_atomic_load(p, __ATOMIC_RELAXED, __HIP_MEMORY_SCOPE_AGENT) == tag)) {
    __builtin_amdgcn_s_sleep(1);
    if (++spins > (1u << 17)) { if (lane == 0) __hip_atomic_store(ctl + C_TMO * 64, (unsigned)line, __ATOMIC_RELAXED, __HIP_MEMORY_SCOPE_AGENT); break; }
  }
  acquire();
}

template <int BN> DI constexpr int bswz(int k) {
  return BN == 224 ? (((k >> 3) & 1) << 1)
       : BN == 128 ? (((k & 3) | (((k >> 3) & 1) << 2)) << 1)
                   : ((((k >> 1) & 1) | (((k >> 3) & 1) << 1)) << 1);
}

DI constexpr int aswz(int r) { return (-(r >> 2)) & 3; }

template <int BN, int NS, int NT, bool AROW, int LAG = 0>
struct Loader {
  static constexpr int PA = 4, PB = BN / 32, PW = PA + PB, BST = BN * 128, CPR = BN / 8;
  static_assert(NS * (16384 + BST) <= L_TAB, "");
  LAS char* dst0; rsrc_t ra, rb; int va[PA], vb[PB]; int ldb64, own0, tn;
  static constexpr int NOWN = BN == 128 ? 3 : BN == 224 ? 1 : 0;
  DI int ktile(int t) const { return AROW ? ((t + own0) & (NT - 1)) : (t < NOWN ? own0 + t : (t - NOWN < own0 ? t - NOWN : t)); }
  DI void issueA(int t) {
    const int kk = ktile(t), slot = t % NS;
#pragma unroll
    for (int i = 0; i < PA; ++i)
      __builtin_amdgcn_raw_ptr_buffer_load_lds(ra, dst0 + slot * 16384 + i * 4096, 16, va[i], kk * (AROW ? 128 : 16384), 0, AROW ? 0 : SC1);
  }
  DI void issueB(int t) {
    const int kk = ktile(t), slot = t % NS;
#pragma unroll
    for (int i = 0; i < PB; ++i) __builtin_amdgcn_raw_ptr_buffer_load_lds(rb, dst0 + NS * 16384 + slot * BST + i * 4096, 16, vb[i], kk * ldb64, 0, 0);
  }
  DI void init(LAS char* smem, rsrc_t ra_, rsrc_t rb_, int a_off, int lda_b, int b_off, int ldb_b, int segb, int lw, int lane, int own) {
    dst0 = smem + lw * 1024; ra = ra_; rb = rb_; ldb64 = 64 * ldb_b; own0 = own; tn = NS - 1;
#pragma unroll
    for (int i = 0; i < PA; ++i) {
      const int ins = lw + i * 4, r = ins * 8 + (lane >> 3), c = (lane & 7) ^ ((r >> 1) & 7);
      va[i] = AROW ? a_off + r * lda_b + c * 16 : a_off + ins * 1024 + lane * 16;
    }
#pragma unroll
    for (int i = 0; i < PB; ++i) {
      const int ch = (lw + i * 4) * 64 + lane, k = ch / CPR, c = (ch % CPR) ^ bswz<BN>(k);
      vb[i] = b_off + k * ldb_b + (c >> 3) * segb + (c & 7) * 16;
    }
  }
  template <int ALLOW = (NS - 2 - LAG) * PW> DI void step() { wait_vm<ALLOW>(); lbar(); issueA(tn); issueB(tn); ++tn; }
  DI void steps(int n) {
#pragma unroll 1
    for (int t = 0; t < n; ++t) step();
  }
  DI void finish() {
    if constexpr (NS - LAG >= 5) { wait_vm<3 * PW>(); lbar(); }
    if constexpr (NS - LAG >= 4) { wait_vm<2 * PW>(); lbar(); }
    if constexpr (NS - LAG >= 3) { wait_vm<1 * PW>(); lbar(); }
    wait_vm<0>(); lbar();
  }
};
template <int BN> DI void load_wtile(LAS char* dst, rsrc_t r, int off, int ldb_b, int segb, int lw, int lane) {
#pragma unroll
  for (int i = 0; i < BN / 32; ++i) {
    const int ch = (lw + i * 4) * 64 + lane, k = ch / (BN / 8), c = (ch % (BN / 8)) ^ bswz<BN>(k);
    __builtin_amdgcn_raw_ptr_buffer_load_lds(r, dst + (lw + i * 4) * 1024, 16, off + k * ldb_b + (c >> 3) * segb + (c & 7) * 16, 0, 0, 0);
  }
}
DI int opaque(int v) { asm volatile("" : "+v"(v)); return v; }

DI void touch_lines(LAS char* smem, rsrc_t r, int base, int line, int nlines) {
  __builtin_amdgcn_raw_ptr_buffer_load_lds(r, smem + L_DUMMY, 4, line < nlines ? base + line * 128 : (int)0x80000000, 0, 0, 0);
}

template <int BN> DI void colsum_tile(LAS char* img, rsrc_t rws, unsigned off, int lw, int lane) {
  constexpr int RS = BN * 2;
  const int r16 = lane & 15, g = lane >> 4, q = (lane & 15) >> 2, p = lane & 3;
  const s16x8 one_ = {0x3f80, 0x3f80, 0x3f80, 0x3f80, 0x3f80, 0x3f80, 0x3f80, 0x3f80};
  LAS const char* rd[4];
#pragma unroll
  for (int i = 0; i < 4; ++i) {
    const int nt = lw + 4 * i < BN / 16 ? lw + 4 * i : lw;
    rd[i] = img + (8 * g + q) * RS + (((2 * nt + (p >> 1)) ^ bswz<BN>(8 * g + q)) * 16) + 8 * (p & 1);
  }
  s16x4 v[4][4];
#define TR4(a, b, c, d, r) "ds_read_b64_tr_b16 %" #a ", %" #r "\n\tds_read_b64_tr_b16 %" #b ", %" #r " offset:%c20\n\t" \
                           "ds_read_b64_tr_b16 %" #c ", %" #r " offset:%c21\n\tds_read_b64_tr_b16 %" #d ", %" #r " offset:%c22\n\t"
  asm volatile(TR4(0, 1, 2, 3, 16) TR4(4, 5, 6, 7, 17) TR4(8, 9, 10, 11, 18) TR4(12, 13, 14, 15, 19) "s_waitcnt lgkmcnt(0)"
               : "=&v"(v[0][0]), "=&v"(v[0][1]), "=&v"(v[0][2]), "=&v"(v[0][3]), "=&v"(v[1][0]), "=&v"(v[1][1]), "=&v"(v[1][2]), "=&v"(v[1][3]),
                 "=&v"(v[2][0]), "=&v"(v[2][1]), "=&v"(v[2][2]), "=&v"(v[2][3]), "=&v"(v[3][0]), "=&v"(v[3][1]), "=&v"(v[3][2]), "=&v"(v[3][3])
               : "v"(rd[0]), "v"(rd[1]), "v"(rd[2]), "v"(rd[3]), "i"(4 * RS), "i"(32 * RS), "i"(36 * RS) : "memory");
#undef TR4
  f32x4 c[4];
#pragma unroll
  for (int i = 0; i < 4; ++i) {
    c[i] = (f32x4){0.f, 0.f, 0.f, 0.f};
    c[i] = MFMA16(__builtin_bit_cast(bf16x8, __builtin_shufflevector(v[i][0], v[i][1], 0, 1, 2, 3, 4, 5, 6, 7)), __builtin_bit_cast(bf16x8, one_), c[i]);
    c[i] = MFMA16(__builtin_bit_cast(bf16x8, __builtin_shufflevector(v[i][2], v[i][3], 0, 1, 2, 3, 4, 5, 6, 7)), __builtin_bit_cast(bf16x8, one_), c[i]);
  }
  const f32x4 out = r16 == 0 ? c[0] : r16 == 1 ? c[1] : r16 == 2 ? c[2] : c[3];
  st128(__builtin_bit_cast(u32x4, out), rws, lw + 4 * r16 < BN / 16 ? off + (lw + 4 * r16) * 64 + g * 16 : 0x80000000u);
}
DI f32x4 sum4(u32x4 (&t)[4]) {
  asm volatile("" : "+v"(t[0]), "+v"(t[1]), "+v"(t[2]), "+v"(t[3]));
  return (__builtin_bit_cast(f32x4, t[0]) + __builtin_bit_cast(f32x4, t[1])) + (__builtin_bit_cast(f32x4, t[2]) + __builtin_bit_cast(f32x4, t[3]));
}
DI f32x4 lds_sum4(LAS const char* p) {
  return (*(LAS const f32x4*)p + *(LAS const f32x4*)(p + 1024)) + (*(LAS const f32x4*)(p + 2048) + *(LAS const f32x4*)(p + 3072));
}
DI unsigned peek(GAS unsigned* ctl, int line, int lane) { return __hip_atomic_load(ctl + line * 64 + lane, __ATOMIC_RELAXED, __HIP_MEMORY_SCOPE_AGENT); }
DI void confirm(unsigned seen, GAS unsigned* ctl, int line, unsigned tag, int lane) {
  asm volatile("" : "+v"(seen));
  if (!wave_all(seen == tag)) poll(ctl, line, 0, 64, tag, lane);
  else acquire();
}

DI void mfma16(f32x4& c, const bf16x8& a, const bf16x8& b) { asm volatile("v_mfma_f32_16x16x32_bf16 %0, %1, %2, %0" : "+v"(c) : "v"(a), "v"(b)); }
DI void mfma_drain() { asm volatile("s_nop 7\n\ts_nop 7" ::: "memory"); }
template <int TM, int TN> struct Frag { s16x4 b[TN][2]; bf16x8 a[TM]; };
template <int BN, int NS, int NT, int TM, int TN, bool DEEP, bool OWN, class NMap, class Mid>
DI void mma_loop(LAS char* smem, f32x4 (&acc)[TM][TN], int row0, int lane, NMap&& nt, Mid&& mid) {
  constexpr int RS = BN * 2, BST = BN * 128, NL = TM + 2 * TN, NM = TM * TN;
  typedef Frag<TM, TN> F;
  const int r16 = lane & 15, g = lane >> 4, q = (lane & 15) >> 2, p = lane & 3;
  constexpr bool AROW = BN == 192;
  const int aoff = AROW ? (row0 + r16) * 128 + ((g ^ (r16 >> 1)) * 16) : (row0 + r16) * 64 + ((g ^ aswz(r16)) * 16);
  const int aoff1 = AROW ? aoff ^ 64 : aoff + 8192;
  int boff[TN];
#pragma unroll
  for (int j = 0; j < TN; ++j) boff[j] = NS * 16384 + (8 * g + q) * RS + (((2 * nt(j) + (p >> 1)) ^ bswz<BN>(8 * g + q)) * 16) + 8 * (p & 1);
#pragma unroll
  for (int i = 0; i < TM; ++i)
#pragma unroll
    for (int j = 0; j < TN; ++j) { acc[i][j] = (f32x4){0.f, 0.f, 0.f, 0.f}; asm volatile("" : "+v"(acc[i][j])); }
  auto ld1 = [&](F& f, int slot, int s, int k) {
    if (k == 0 || k > 2 * TN) { const int i = k == 0 ? 0 : k - 2 * TN; f.a[i] = ld_b128(smem + slot * 16384 + (s ? aoff1 : aoff) + i * (AROW ? 2048 : 1024)); }
    else { const int j = (k - 1) >> 1, u = (k - 1) & 1; f.b[j][u] = __builtin_amdgcn_ds_read_tr16_b64_v4i16((LAS s16x4*)(smem + slot * BST + boff[j] + (32 * s + 4 * u) * RS)); }
  };
  auto mm1 = [&](const F& f, int m) {
    const int i = m / TN, j = m % TN;
    const bf16x8 b = __builtin_bit_cast(bf16x8, __builtin_shufflevector(f.b[j][0], f.b[j][1], 0, 1, 2, 3, 4, 5, 6, 7));
    if (AROW && j >= 4) mfma16(acc[i][j], f.a[i], b); else mfma16(acc[i][j], b, f.a[i]);
  };
  auto ldall = [&](F& f, int slot, int s) {
#pragma unroll
    for (int k = 0; k < NL; ++k) ld1(f, slot, s, k);
  };
  auto mmall = [&](const F& f) {
#pragma unroll
    for (int m = 0; m < NM; ++m) mm1(f, m);
  };
  auto sub = [&](const F& x, F& u, int slot, int s) {
    constexpr int SP = DEEP ? NM : (NL < NM ? NL : NM - 2);
#pragma unroll
    for (int m = 0; m < NM; ++m) {
      mm1(x, m);
#pragma unroll
      for (int k = (m * NL + SP - 1) / SP; k < NL && k < ((m + 1) * NL + SP - 1) / SP; ++k) ld1(u, slot, s, k);
      __builtin_amdgcn_sched_barrier(0);
    }
  };
  auto nxt = [](int s) { return s == NS - 1 ? 0 : s + 1; };
  lbar();
  if (threadIdx.x < 64) STAMP(BN == 192 ? 0 : BN == 64 ? 1 : BN == 224 ? 2 : 3, 1);
  TS_DECL; TS(0);
  int slot = 0;
  if constexpr (!DEEP) {
    F f0, f1;
    ldall(f0, 0, 0);
    if constexpr (OWN) {
      sub(f0, f1, 0, 1);
      mid();
      bar();
      mmall(f1);
      __builtin_amdgcn_sched_barrier(0);
      TS(32);
      lbar();
      TS(1);
      slot = 1;
      ldall(f0, 1, 0);
    }
#pragma unroll 1
    for (int t = OWN ? 1 : 0; t < NT - 1; ++t) {
      sub(f0, f1, slot, 1);
      TS(32 + t);
      lbar();
      TS(t + 1);
      slot = nxt(slot);
      sub(f1, f0, slot, 0);
    }
    sub(f0, f1, slot, 1);
    lbar();
    TS(NT); TS_OUT(BN == 192 ? 0 : BN == 64 ? 1 : BN == 224 ? 2 : 3);
    mmall(f1);
  } else {
    F f0, f1, g0, g1;
    auto tile = [&](const F& x0, const F& x1, F& u0, F& u1, int t) {
      TS(32 + t);
      bar();
      TS(t + 1);
      slot = nxt(slot);
      sub(x0, u0, slot, 0);
      sub(x1, u1, slot, 1);
    };
    ldall(f0, 0, 0); ldall(f1, 0, 1);
    int t0 = 0;
    if constexpr (OWN) {
      tile(f0, f1, g0, g1, 0);
      mid();
      tile(g0, g1, f0, f1, 1);
      mmall(f0); mmall(f1);
      __builtin_amdgcn_sched_barrier(0);
      TS(34);
      lbar();
      TS(3);
      slot = 3;
      ldall(g0, 3, 0); ldall(g1, 3, 1);
      tile(g0, g1, f0, f1, 3);
      t0 = 4;
    }
#pragma unroll 1
    for (int t = t0; t < NT - 2; t += 2) {
      tile(f0, f1, g0, g1, t);
      tile(g0, g1, f0, f1, t + 1);
    }
    tile(f0, f1, g0, g1, NT - 2);
    lbar();
    TS(NT); TS_OUT(BN == 192 ? 0 : BN == 64 ? 1 : BN == 224 ? 2 : 3);
    mmall(g0); mmall(g1);
  }
  mfma_drain();
#pragma unroll
  for (int i = 0; i < TM; ++i)
#pragma unroll
    for (int j = 0; j < TN; ++j) asm volatile("" : "+v"(acc[i][j]));
}
DI void swap_pair(f32x4& x, f32x4& y) {
#pragma unroll
  for (int e = 0; e < 4; ++e) {
    auto r = __builtin_amdgcn_permlane16_swap(f2u(x[e]), f2u(y[e]), false, false);
    x[e] = u2f(r[0]); y[e] = u2f(r[1]);
  }
}
DI void add_bf8(f32x4& x, f32x4& y, const u32x4& rv) {
  x[0] += bflo(rv[0]); x[1] += bfhi(rv[0]); x[2] += bflo(rv[1]); x[3] += bfhi(rv[1]);
  y[0] += bflo(rv[2]); y[1] += bfhi(rv[2]); y[2] += bflo(rv[3]); y[3] += bfhi(rv[3]);
}
DI u32x4 pack_xy(const f32x4& x, const f32x4& y) { return (u32x4){pk2(x[0], x[1]), pk2(x[2], x[3]), pk2(y[0], y[1]), pk2(y[2], y[3])}; }

template <class Op> DI void red2(float& a, float& b, Op&& op) {
  auto r = __builtin_amdgcn_permlane16_swap(f2u(a), f2u(b), false, false);
  const float c = op(u2f(r[0]), u2f(r[1]));
  auto t = __builtin_amdgcn_permlane32_swap(f2u(c), f2u(c), false, false);
  const float d = op(u2f(t[0]), u2f(t[1]));
  auto u = __builtin_amdgcn_permlane16_swap(f2u(d), f2u(d), false, false);
  a = u2f(u[0]); b = u2f(u[1]);
}
DI void dual_softmax(const f32x4 (&p)[2][2], const float (&sc)[2], f32x4 (&e)[2][2][2]) {
  float mx[2], mn[2], s1[2], s2[2];
#pragma unroll
  for (int t = 0; t < 2; ++t) {
    mx[t] = p[t][0][0]; mn[t] = p[t][0][0];
#pragma unroll
    for (int r = 1; r < 8; ++r) { mx[t] = __builtin_fmaxf(mx[t], p[t][r >> 2][r & 3]); mn[t] = __builtin_fminf(mn[t], p[t][r >> 2][r & 3]); }
  }
  red2(mx[0], mx[1], [](float x, float y) { return __builtin_fmaxf(x, y); });
  red2(mn[0], mn[1], [](float x, float y) { return __builtin_fminf(x, y); });
#pragma unroll
  for (int t = 0; t < 2; ++t) {
    const float a = -mx[t] * sc[t], b = mn[t] * sc[t];
    s1[t] = 0.f; s2[t] = 0.f;
#pragma unroll
    for (int r = 0; r < 8; ++r) {
      const float x = __builtin_amdgcn_exp2f(__builtin_fmaf(p[t][r >> 2][r & 3], sc[t], a));
      const float y = __builtin_amdgcn_exp2f(__builtin_fmaf(p[t][r >> 2][r & 3], -sc[t], b));
      e[0][t][r >> 2][r & 3] = x; e[1][t][r >> 2][r & 3] = y;
      s1[t] += x; s2[t] += y;
    }
  }
  red2(s1[0], s1[1], [](float x, float y) { return x + y; });
  red2(s2[0], s2[1], [](float x, float y) { return x + y; });
#pragma unroll
  for (int t = 0; t < 2; ++t) {
    const float r1 = __builtin_amdgcn_rcpf(s1[t]), r2 = __builtin_amdgcn_rcpf(s2[t]);
#pragma unroll
    for (int r = 0; r < 8; ++r) { e[0][t][r >> 2][r & 3] *= r1; e[1][t][r >> 2][r & 3] *= r2; }
  }
}
DI bf16x8 pack_frag(const f32x4& x, const f32x4& y) { return __builtin_bit_cast(bf16x8, pack_xy(x, y)); }
DI void st4(LAS char* p, const f32x4& a, const f32x4& b, const f32x4& c, const f32x4& d) {
  *(LAS f32x4*)p = a; *(LAS f32x4*)(p + 1024) = b; *(LAS f32x4*)(p + 2048) = c; *(LAS f32x4*)(p + 3072) = d;
}
DI void ld4(LAS const char* p, f32x4 (&v)[2][2]) {
  v[0][0] = *(LAS const f32x4*)p; v[0][1] = *(LAS const f32x4*)(p + 1024); v[1][0] = *(LAS const f32x4*)(p + 2048); v[1][1] = *(LAS const f32x4*)(p + 3072);
}
#endif

__global__ __launch_bounds__(512, 2) void block_kernel(const u16* __restrict__ X, const u16* __restrict__ WQKV, const u16* __restrict__ WPHI,
                                                        const u16* __restrict__ WO, const u16* __restrict__ W1, const u16* __restrict__ W2,
                                                        u16* __restrict__ OUT, char* WS) {
#if defined(__HIP_DEVICE_COMPILE__)
  extern __shared__ __attribute__((aligned(16))) char smem_[];
  LAS char* smem = (LAS char*)smem_;
  const int tid = threadIdx.x, wid = __builtin_amdgcn_readfirstlane(tid >> 6);
  int lane = tid & 63;
  const int bid = blockIdx.x, xcd = bid & 7, loc = bid >> 3;
  const int tm = (xcd & 1) * 8 + (loc & 7), gp = (xcd >> 1) * 4 + (loc >> 3);
  const bool loader = wid >= 4;
  const int lw = wid & 3;
  GAS unsigned* ctl = (GAS unsigned*)WS;
  const rsrc_t rws = mkrsrc(WS, WS_END);
  if (wid == 0) STAMP(0, 0);
  unsigned tag = 0;

  int r16, g, cpair, cswz;
  auto relane = [&]() {
    lane = opaque(lane);
    r16 = lane & 15; g = lane >> 4;
    cpair = (g & 1) * 16 + (g >> 1) * 8;
    cswz = ((cpair >> 3) ^ aswz(r16)) * 16;
  };
  {
    constexpr int NT = 16, NS = 3;
    constexpr int L_PHI = 0;
    constexpr int L_XV = 2 * 17408;
    constexpr int L_VF = L_XV + 16384;
    constexpr int L_XQ = L_VF + 8192;
    constexpr int L_KVT = L_XQ;
    constexpr int L_WF = L_XQ + 16384;
    static_assert(L_WF + 4096 <= 5 * 16384, "phase 2's B ring is filled during this epilogue");
    relane();
    const int wm = (wid >> 1) & 1, hh = wid & 1, mi = wid >> 2;
    const int head = gp * 2 + hh;
    const unsigned kvp_grp = WS_KVP + gp * 16 * KVP_BYTES;
    LAS char* const xk = smem + L_PHI + hh * 17408 + (wm * 64 + 32) * 136 + lane * 16;
    bf16x8 wf[2];
    f32x4 fq[2][2], fk[2][2], fv[2][2];
    if (loader) {
      const rsrc_t rb = mkrsrc(WQKV, D_ * 3 * D_ * 2), ro = mkrsrc(WO, D_ * D_ * 2);
      Loader<192, NS, NT, true> L;
      const rsrc_t rx = mkrsrc(X, S_ * D_ * 2);
      L.init(smem, rx, rb, tm * 128 * D_ * 2, D_ * 2, gp * 128, 3 * D_ * 2, 2048, lw, lane, (tm & 3) * 4);
      L.issueA(0); L.issueB(0); L.issueA(1); L.issueB(1);
      if (lw == 0) {
        unsigned long long pc;
        asm volatile("s_getpc_b64 %0" : "=s"(pc));
        touch_lines(smem, mkrsrc((const void*)(pc & ~127ull), CODE_AHEAD), (loc * 16) * 128, lane < 16 ? lane : 4096, 16);
      }
      tag = read_tag(WS);
      __builtin_amdgcn_s_setprio(3);
      if (wid == 4) STAMP(4, 1);
      auto cs1 = [&](int t) { if ((tm >> 2) == t) colsum_tile<192>(smem + NS * 16384 + (t % NS) * 24576, rws, WS_CS1 + (gp * 16 + tm) * 768, lw, lane); };
      L.step();
      cs1(0);
      const int xrow = tm * 128 + gp * 8 + lw * 2 + (lane >> 5);
      u32x4 xv[4];
#pragma unroll
      for (int j = 0; j < 4; ++j) xv[j] = __builtin_amdgcn_raw_buffer_load_b128(rx, xrow * (D_ * 2) + (lane & 31) * 16, j * 512, 0);
      L.step<14>();
      cs1(1);
      L.step<14>();
      cs1(2);
      L.step();
      cs1(3);
      {
        asm volatile("" : "+v"(xv[0]), "+v"(xv[1]), "+v"(xv[2]), "+v"(xv[3]));
        float f[32], sum = 0.f, qq = 0.f;
#pragma unroll
        for (int i = 0; i < 16; ++i) { f[2 * i] = bflo(xv[i >> 2][i & 3]); f[2 * i + 1] = bfhi(xv[i >> 2][i & 3]); sum += f[2 * i] + f[2 * i + 1]; }
        const float mean = half_sum(sum) * (1.0f / D_);
#pragma unroll
        for (int i = 0; i < 32; ++i) { const float d = f[i] - mean; qq = __builtin_fmaf(d, d, qq); }
        const float rstd = 1.0f / __builtin_sqrtf(half_sum(qq) * (1.0f / D_) + LN_EPS);
        __builtin_amdgcn_raw_buffer_store_b64(__builtin_bit_cast(u32x2, (f32x2){mean, rstd}), rws, (lane & 31) == 0 ? WS_XST + xrow * 8 : 0x80000000u, 0, SC1);
      }
      L.step();
      L.step<0>();
      if (lane == 0) { set_flag(ctl, C_CS1 + gp, tm * 4 + lw, tag); set_flag(ctl, C_ST + tm, gp * 4 + lw, tag); }
      L.steps(loc & 3);
      load_wtile<224>(smem + L_WT, mkrsrc(W1, D_ * FF_ * 2), tm * 64 * (FF_ * 2) + gp * 448, FF_ * 2, 128, lw, lane);
      touch_lines(smem, ro, bid * 8192, lane < 16 ? lw * 16 + lane : 4096, 64);
      L.step();
      L.step<18>();
      L.steps(3 - (loc & 3));
      const unsigned seen = peek(ctl, C_CS1 + gp, lane), seen2 = peek(ctl, C_ST + tm, lane);
      L.steps(2);
      confirm(seen, ctl, C_CS1 + gp, tag, lane);
      confirm(seen2, ctl, C_ST + tm, tag, lane);
      u32x4 gt[4];
#pragma unroll
      for (int j = 0; j < 4; ++j) gt[j] = ld128(rws, WS_CS1 + (gp * 16 + lw * 4) * 768 + lane * 16, j * 768);
      u32x4 xs = ld128(rws, WS_XST + tm * 1024 + lane * 16, 0);
      L.step();
      wait_vm<10>(); lbar();
      *(LAS f32x4*)(smem + L_CSS + lw * 1024 + lane * 16) = sum4(gt);
      asm volatile("" : "+v"(xs));
      if (lw == 1) *(LAS u32x4*)(smem + L_RST + lane * 16) = xs;
      wait_vm<0>(); lbar();
      if (lw == 0) *(LAS f32x4*)(smem + L_CS + lane * 16) = lds_sum4(smem + L_CSS + lane * 16);
      lbar();
      bar();
      __builtin_amdgcn_s_setprio(1);
      ld4(xk, fk);
      wf[0] = ld_b128(smem + L_WF + hh * 2048 + lane * 16);
      wf[1] = ld_b128(smem + L_WF + hh * 2048 + 1024 + lane * 16);
      ld4(smem + L_XV + lw * 4096 + lane * 16, fv);
    } else {
      {
#pragma unroll
        for (int t = 0; t < 2; ++t) {
          s16x8 w;
#pragma unroll
          for (int j = 0; j < 8; ++j) w[j] = (short)WPHI[head * 1024 + (16 * (j >> 2) + 4 * g + (j & 3)) * 32 + 16 * t + r16];
          wf[t] = __builtin_bit_cast(bf16x8, w);
        }
        tag = read_tag(WS);
      }
      if (wid == 0) STAMP(4, 0);
      f32x4 acc[4][6];
      mma_loop<192, NS, NT, 4, 6, false, false>(smem, acc, wm * 64, lane, [&](int j) { return (j >> 1) * 4 + hh * 2 + (j & 1); }, []() {});
      if (wid == 0) STAMP(0, 2);
      if (wm == 0) {
        *(LAS bf16x8*)(smem + L_WF + hh * 2048 + lane * 16) = wf[0];
        *(LAS bf16x8*)(smem + L_WF + hh * 2048 + 1024 + lane * 16) = wf[1];
      }
      st4(xk, acc[2][2], acc[2][3], acc[3][2], acc[3][3]);
      st4(smem + L_XV + wid * 4096 + lane * 16, acc[2][4], acc[2][5], acc[3][4], acc[3][5]);
      lbar();
      if (wid == 0) STAMP(4, 4);
      st4(smem + L_XQ + wid * 4096 + lane * 16, acc[2][0], acc[2][1], acc[3][0], acc[3][1]);
#pragma unroll
      for (int t = 0; t < 2; ++t)
#pragma unroll
        for (int u = 0; u < 2; ++u) { fq[t][u] = acc[t][u]; fk[t][u] = acc[t][2 + u]; fv[t][u] = acc[t][4 + u]; }
    }
    const int rbase = wm * 64 + mi * 32, q = r16 >> 2, p = lane & 3;
    LAS const float* cs = (LAS const float*)(smem + L_CS) + hh * 32;
    LAS const f32x2* rst = (LAS const f32x2*)(smem + L_RST) + rbase;
    auto feature = [&](const f32x4 (&x)[2][2], int part, f32x4 (&e)[2][2][2]) {
      const f32x4 c0 = *(LAS const f32x4*)(cs + part * 64 + 4 * g), c1 = *(LAS const f32x4*)(cs + part * 64 + 16 + 4 * g);
      f32x4 pp[2][2];
      float sc[2];
#pragma unroll
      for (int t = 0; t < 2; ++t) {
        const f32x2 ms = rst[t * 16 + r16];
        sc[t] = ms[1] * 1.4426950408889634f;
        f32x4 y0, y1;
#pragma unroll
        for (int e_ = 0; e_ < 4; ++e_) { y0[e_] = __builtin_fmaf(-ms[0], c0[e_], x[t][0][e_]); y1[e_] = __builtin_fmaf(-ms[0], c1[e_], x[t][1][e_]); }
        const bf16x8 yb = pack_frag(y0, y1);
        pp[t][0] = MFMA16(wf[0], yb, ((f32x4){0.f, 0.f, 0.f, 0.f}));
        pp[t][1] = MFMA16(wf[1], yb, ((f32x4){0.f, 0.f, 0.f, 0.f}));
      }
      dual_softmax(pp, sc, e);
    };
    if (wid == 0) STAMP(4, 5);
    {
      const float cv0 = cs[128 + r16], cv1 = cs[144 + r16];
#pragma unroll
      for (int t = 0; t < 2; ++t) {
        const f32x4 s01 = *(LAS const f32x4*)(rst + t * 16 + 4 * g), s23 = *(LAS const f32x4*)(rst + t * 16 + 4 * g + 2);
        const float mv[4] = {s01[0], s01[2], s23[0], s23[2]}, rv[4] = {s01[1], s01[3], s23[1], s23[3]};
#pragma unroll
        for (int e_ = 0; e_ < 4; ++e_) {
          fv[t][0][e_] = __builtin_fmaf(-mv[e_], cv0, fv[t][0][e_]) * rv[e_];
          fv[t][1][e_] = __builtin_fmaf(-mv[e_], cv1, fv[t][1][e_]) * rv[e_];
        }
      }
      LAS char* vd = smem + (loader ? L_XV + lw * 4096 : L_VF + lw * 2048) + lane * 16;
      *(LAS bf16x8*)vd = pack_frag(fv[0][0], fv[1][0]);
      *(LAS bf16x8*)(vd + 1024) = pack_frag(fv[0][1], fv[1][1]);
      f32x4 e[2][2][2];
      feature(fk, 1, e);
      LAS char* wr = smem + L_PHI + hh * 17408 + (rbase + r16) * 136 + g * 8;
#pragma unroll
      for (int w = 0; w < 2; ++w)
#pragma unroll
        for (int t = 0; t < 2; ++t)
#pragma unroll
          for (int u = 0; u < 2; ++u)
            *(LAS u32x2*)(wr + t * 16 * 136 + w * 64 + u * 32) = (u32x2){pk2(e[w][t][u][0], e[w][t][u][1]), pk2(e[w][t][u][2], e[w][t][u][3])};
    }
    if (wid == 0) STAMP(4, 7);
    if (wid == 4) STAMP(4, 9);
    lbar();
    __builtin_amdgcn_s_setprio(0);
    if (wid == 0) STAMP(4, 8);
    {
      f32x4 k0 = {0.f, 0.f, 0.f, 0.f}, k1 = k0, zz = k0;
      const s16x8 one_ = {0x3f80, 0x3f80, 0x3f80, 0x3f80, 0x3f80, 0x3f80, 0x3f80, 0x3f80};
      LAS const char* rd = smem + L_PHI + hh * 17408 + (4 * g + q) * 136 + wm * 64 + p * 8;
#pragma unroll
      for (int u = 0; u < 4; ++u) {
        const bf16x8 pa0 = ld_tr2(rd + 32 * u * 136, rd + (32 * u + 16) * 136), pa1 = ld_tr2(rd + 32 * u * 136 + 32, rd + (32 * u + 16) * 136 + 32);
        const bf16x8 vf = ld_b128(smem + ((u & 1) ? L_XV + ((u >> 1) * 2 + hh) * 4096 : L_VF + ((u >> 1) * 2 + hh) * 2048) + mi * 1024 + lane * 16);
        k0 = MFMA16(pa0, vf, k0);
        k1 = MFMA16(pa1, vf, k1);
        zz = MFMA16(mi ? pa1 : pa0, __builtin_bit_cast(bf16x8, one_), zz);
      }
      if (loader) ld4(smem + L_XQ + lw * 4096 + lane * 16, fq);
      const int kb = kvp_grp + tm * KVP_BYTES;
      st128(pack_xy(k0, k1), rws, kb + hh * 4096 + (wm * 2 + mi) * 1024 + lane * 16);
      st128(__builtin_bit_cast(u32x4, zz), rws, r16 == 0 ? kb + 8192 + hh * 256 + wm * 128 + g * 32 + mi * 16 : (int)0x80000000);
      if (wid == 0) STAMP(0, 3);
      wait_vm<0>();
    }
    lbar();
    if (tid == 0) set_flag(ctl, C_KV + gp, tm, tag);
    if (wid == 0) STAMP(0, 4);
    if (loader) {
      Loader<64, 5, 16, false, 1> L2;
      L2.init(smem, rws, mkrsrc(WO, D_ * D_ * 2), WS_O + tm * 128 * D_ * 2, 0, gp * 128, D_ * 2, 128, lw, lane, 0);
      L2.issueB(0); L2.issueB(1); L2.issueB(2); L2.issueB(3); L2.issueB(4);
    }
    bf16x8 pf[2][2];
    {
      f32x4 e[2][2][2];
      feature(fq, 0, e);
#pragma unroll
      for (int t = 0; t < 2; ++t)
#pragma unroll
        for (int w = 0; w < 2; ++w) { pf[t][w] = pack_frag(e[w][t][0], e[w][t][1]); asm volatile("" : "+v"(pf[t][w])); }
    }
    if (wid == 0) {
      STAMP(0, 5);
      poll(ctl, C_KV + gp, 0, 16, tag, lane);
    }
    bar();
    if (wid == 0) STAMP(0, 6);
    {
      u32x4 tz[16];
      if (wid == 7) {
#pragma unroll
        for (int j = 0; j < 16; ++j) tz[j] = ld128(rws, lane < 32 ? kvp_grp + 8192 + lane * 16 : 0x80000000u, j * KVP_BYTES);
      }
      u32x4 t[16];
#pragma unroll
      for (int j = 0; j < 16; ++j) t[j] = ld128(rws, kvp_grp + tid * 16, j * KVP_BYTES);
      if (wid == 7) {
        f32x4 z = __builtin_bit_cast(f32x4, tz[0]);
#pragma unroll
        for (int j = 1; j < 16; ++j) z += __builtin_bit_cast(f32x4, tz[j]);
        if (lane < 32) *(LAS u32x2*)(smem + L_KVT + 8192 + lane * 8) = (u32x2){pk2(z[0], z[1]), pk2(z[2], z[3])};
      }
      f32x4 x = {0.f, 0.f, 0.f, 0.f}, y = x;
#pragma unroll
      for (int j = 0; j < 16; ++j) add_bf8(x, y, t[j]);
      *(LAS u32x4*)(smem + L_KVT + tid * 16) = pack_xy(x, y);
    }
    lbar();
    if (wid == 0) STAMP(0, 7);
    {
      bf16x8 ka[2][2], za[2];
#pragma unroll
      for (int w = 0; w < 2; ++w) {
        ka[w][0] = ld_b128(smem + L_KVT + hh * 4096 + w * 2048 + lane * 16);
        ka[w][1] = ld_b128(smem + L_KVT + hh * 4096 + w * 2048 + 1024 + lane * 16);
        za[w] = ld_b128(smem + L_KVT + 8192 + hh * 128 + w * 64 + g * 16);
      }
#pragma unroll
      for (int t = 0; t < 2; ++t) {
        f32x4 o0 = {0.f, 0.f, 0.f, 0.f}, o1 = o0, dn = o0;
#pragma unroll
        for (int w = 0; w < 2; ++w) { dn = MFMA16(za[w], pf[t][w], dn); o0 = MFMA16(ka[w][0], pf[t][w], o0); o1 = MFMA16(ka[w][1], pf[t][w], o1); }
        const float rd = 1.0f / (dn[0] + 1e-6f);
#pragma unroll
        for (int e_ = 0; e_ < 4; ++e_) { o0[e_] *= rd; o1[e_] *= rd; }
        swap_pair(o0, o1);
        st128(pack_xy(o0, o1), rws, WS_O + (tm * 32 + head) * 8192 + (rbase + t * 16 + r16) * 64 + cswz);
      }
    }
    wait_vm<0>();
    lbar();
    if (tid == 0) set_flag(ctl, C_O + tm, gp, tag);
    if (wid == 0) STAMP(0, 8);
  }

  const int kh = gp >> 3;
  if (loader) {
    __builtin_amdgcn_s_setprio(3);
    relane();
    Loader<64, 5, 16, false, 1> L2;
    L2.init(smem, rws, mkrsrc(WO, D_ * D_ * 2), WS_O + tm * 128 * D_ * 2, 0, gp * 128, D_ * 2, 128, lw, lane, 0);
    colsum_tile<224>(smem + L_WT, rws, WS_CS3 + (gp * 16 + tm) * 896, lw, lane);
    poll(ctl, C_O + tm, 0, 16, tag, lane);
    if (wid == 4) STAMP(1, 0);
    L2.issueA(0); L2.issueA(1); L2.issueA(2); L2.issueA(3); L2.issueA(4);
    if (bid == 0 && wid == 4 && lane == 0) __hip_atomic_store(ctl + C_EPOCH * 64, tag, __ATOMIC_RELAXED, __HIP_MEMORY_SCOPE_AGENT);
    wait_vm<16>(); lbar();
    wait_vm<12>(); lbar();
    L2.tn = 5;
    L2.step<8>();
    L2.step<4 + 6>();
    L2.steps(9);
    L2.finish();
    if (lane == 0) set_flag(ctl, C_CS3 + gp, tm * 4 + lw, tag);
    const rsrc_t r2 = mkrsrc(W2, FF_ * D_ * 2);
    {
      relane();
      Loader<224, 3, 16, false> L;
      L.init(smem, rws, mkrsrc(W1, D_ * FF_ * 2), WS_H + tm * 128 * D_ * 2, 0, gp * 448, FF_ * 2, 128, lw, lane, gp);
      bar();
      L.issueB(0); L.issueB(1);
      wait_vm<7>(); bar();
      L.issueB(2);
      bar();
      if (wid == 4 && lane == 0) set_flag(ctl, C_H + tm, gp, tag);
      poll(ctl, C_H + tm, 0, 16, tag, lane);
      if (wid == 4) STAMP(2, 0);
      L.issueA(1); L.issueA(2);
      u32x4 gt[4], hs[4];
#pragma unroll
      for (int j = 0; j < 4; ++j) hs[j] = ld128(rws, WS_HST + tm * 16384 + lw * 4096 + lane * 16, j * 1024);
      const unsigned seen = peek(ctl, C_CS3 + gp, lane);
      L.tn = 3;
      L.step<4 + 5>();
      L.step<5 + 11>();
      confirm(seen, ctl, C_CS3 + gp, tag, lane);
#pragma unroll
      for (int j = 0; j < 4; ++j) gt[j] = ld128(rws, WS_CS3 + (gp * 16 + lw * 4) * 896 + lane * 16, j * 896);
      *(LAS f32x4*)(smem + L_CSS + 4096 + lw * 1024 + lane * 16) = sum4(hs);
      L.step();
      if (lw == 1) {
        const f32x4 sum = lds_sum4(smem + L_CSS + 4096 + lane * 16);
        const float m0 = sum[0] * (1.0f / D_), m1 = sum[2] * (1.0f / D_);
        const float v0 = __builtin_fmaxf(sum[1] * (1.0f / D_) - m0 * m0, 0.f), v1 = __builtin_fmaxf(sum[3] * (1.0f / D_) - m1 * m1, 0.f);
        *(LAS f32x4*)(smem + L_RST + lane * 16) = (f32x4){m0, 1.0f / __builtin_sqrtf(v0 + LN_EPS), m1, 1.0f / __builtin_sqrtf(v1 + LN_EPS)};
      }
      L.step();
      *(LAS f32x4*)(smem + L_CSS + lw * 1024 + lane * 16) = sum4(gt);
      L.step();
      if (lw == 0) *(LAS f32x4*)(smem + L_CS + lane * 16) = lds_sum4(smem + L_CSS + lane * 16);
      const int stag = (loc & 7) < 6 ? (loc & 7) : 6;
      L.steps(stag);
      touch_lines(smem, r2, bid * 28672, lane < 56 ? lw * 56 + lane : 4096, 224);
      L.step();
      L.step<12>();
      L.steps(6 - stag);
      L.finish();
    }
    {
      relane();
      Loader<128, 4, 28, false, 1> L;
      L.init(smem, rws, r2, WS_H1 + (tm * 112 + kh * 56) * 8192, 0, kh * 1792 * D_ * 2 + gp * 128, D_ * 2, kh ? -1024 : 1024, lw, lane, (7 * (gp & 7) + 1) >> 1);
      lbar();
      L.issueB(0); L.issueB(1); L.issueB(2); L.issueB(3);
      wait_vm<12>(); bar();
      wait_vm<8>(); bar();
      wait_vm<4>(); bar();
      if (wid == 4 && lane == 0) set_flag(ctl, C_H1 + tm, gp, tag);
      L.issueB(4);
      poll(ctl, C_H1 + tm, kh * 8, 8, tag, lane);
      if (wid == 4) STAMP(3, 0);
      L.issueA(3); L.issueA(4);
      L.tn = 5;
      L.step<4>();
      L.steps(22);
      L.finish();
      bar();
    }
  } else {
    relane();
    {
      const int row = tm * 128 + wid * 32 + r16, col = gp * 64 + cpair;
      u32x4 res[2][2];
#pragma unroll
      for (int i = 0; i < 2; ++i)
#pragma unroll
        for (int j = 0; j < 2; ++j) res[i][j] = *(const u32x4*)(X + (size_t)(row + i * 16) * D_ + col + j * 32);
      f32x4 acc[2][4];
      mma_loop<64, 5, 16, 2, 4, true, false>(smem, acc, wid * 32, lane, [](int j) { return j; }, []() {});
      if (wid == 0) STAMP(1, 2);
#pragma unroll
      for (int i = 0; i < 2; ++i) {
        float s = 0.f, qq = 0.f;
#pragma unroll
        for (int j = 0; j < 2; ++j) {
          f32x4 x = acc[i][2 * j], y = acc[i][2 * j + 1];
          swap_pair(x, y);
          add_bf8(x, y, res[i][j]);
          const u32x4 o = pack_xy(x, y);
          st128(o, rws, WS_H + (tm * 32 + gp * 2 + j) * 8192 + (wid * 32 + i * 16 + r16) * 64 + cswz);
          *(LAS u32x4*)(smem + j * 8192 + (wid * 32 + i * 16 + r16) * 64 + cswz) = o;
#pragma unroll
          for (int e = 0; e < 4; ++e) { const float lo = bflo(o[e]), hi = bfhi(o[e]); s += lo + hi; qq = __builtin_fmaf(lo, lo, __builtin_fmaf(hi, hi, qq)); }
        }
        s = x32_sum(x16_sum(s)); qq = x32_sum(x16_sum(qq));
        if (g == 0) __builtin_amdgcn_raw_buffer_store_b64(__builtin_bit_cast(u32x2, (f32x2){s, qq}), rws, WS_HST + ((tm * 16 + gp) * 128 + wid * 32 + i * 16 + r16) * 8, 0, SC1);
      }
      if (wid == 0) STAMP(1, 3);
    }
    const int wm = wid >> 1, wn = wid & 1;
    relane();
    {
      f32x4 acc[4][7];
      mma_loop<224, 3, 16, 4, 7, false, true>(smem, acc, wm * 64, lane, [&](int j) { return j < 6 ? wn * 8 + j : 6 + wn; }, []() { wait_vm<0>(); });
      if (wid == 0) STAMP(2, 2);
      relane();
      f32x4 cs[7];
#pragma unroll
      for (int j = 0; j < 7; ++j) cs[j] = *(LAS const f32x4*)(smem + L_CS + ((j < 6 ? wn * 8 + j : 6 + wn) * 16 + g * 4) * 4);
      const int par = gp & 1, hb = WS_H1 + (tm * 112 + gp * 7) * 8192;
#pragma unroll
      for (int i = 0; i < 4; ++i) {
        const f32x2 ms = *(LAS const f32x2*)(smem + L_RST + (wm * 64 + i * 16 + r16) * 8);
        const float a = ms[1], b = -ms[0] * ms[1];
#pragma unroll
        for (int j = 0; j < 7; ++j)
#pragma unroll
          for (int e = 0; e < 4; ++e) acc[i][j][e] = __builtin_fmaxf(__builtin_fmaf(acc[i][j][e], a, b * cs[j][e]), 0.f);
        const int ro = (wm * 64 + i * 16 + r16) * 64 + cswz;
#pragma unroll
        for (int j = 0; j < 3; ++j) {
          f32x4 x = acc[i][2 * j], y = acc[i][2 * j + 1];
          swap_pair(x, y);
          const u32x4 o = pack_xy(x, y);
          const int sl = wn * 4 + j;
          st128(o, rws, hb + sl * 8192 + ro);
          if ((unsigned)(sl - par) < 6u) *(LAS u32x4*)(smem + (sl - par) * 8192 + ro) = o;
        }
        if (i & 1) {
          f32x4 x = acc[i - 1][6], y = acc[i][6];
          swap_pair(x, y);
          const u32x4 o = pack_xy(x, y);
          const int r3 = (wm * 64 + (i - 1 + (g & 1)) * 16 + r16) * 64 + (((2 * wn + (g >> 1)) ^ aswz(r16)) * 16);
          st128(o, rws, hb + 3 * 8192 + r3);
          *(LAS u32x4*)(smem + (3 - par) * 8192 + r3) = o;
        }
      }
      if (wid == 0) STAMP(2, 3);
    }
    relane();
    {
      const int row = tm * 128 + wm * 64 + r16, col = gp * 64 + wn * 32 + cpair;
      u32x4 res[4];
      f32x4 acc[4][4];
      mma_loop<128, 4, 28, 4, 4, true, true>(smem, acc, wm * 64, lane, [&](int j) { return (j >> 1) * 4 + wn * 2 + (j & 1); }, [&]() {
        wait_vm<0>();
        if (wid == 0) STAMP(2, 4);
#pragma unroll
        for (int i = 0; i < 4; ++i) res[i] = ld128(rws, WS_H + (tm * 32 + gp * 2 + wn) * 8192 + (wm * 64 + i * 16 + r16) * 64 + cswz, 0);
      });
      if (wid == 0) STAMP(3, 2);
      const int pb = WS_P4 + (tm * 16 + gp) * 16384 + wid * 4096 + lane * 16;
#pragma unroll
      for (int i = 0; i < 4; ++i) st128(pack_xy(acc[i][2], acc[i][3]), rws, pb + i * 1024);
      wait_vm<0>();
      if (lane == 0) set_flag(ctl, C_P4 + tm, gp * 4 + wid, tag);
      if (wid == 0) STAMP(3, 3);
      poll(ctl, C_P4 + tm, (gp ^ 8) * 4 + wid, 1, tag, lane);
      if (wid == 0) STAMP(3, 4);
      const int qb = WS_P4 + (tm * 16 + (gp ^ 8)) * 16384 + wid * 4096 + lane * 16;
      u32x4 t[4];
#pragma unroll
      for (int i = 0; i < 4; ++i) t[i] = ld128(rws, qb + i * 1024, 0);
#pragma unroll
      for (int i = 0; i < 4; ++i) {
        f32x4 x = acc[i][0], y = acc[i][1];
        add_bf8(x, y, t[i]);
        swap_pair(x, y);
        add_bf8(x, y, res[i]);
        *(u32x4*)(OUT + (size_t)(row + i * 16) * D_ + col) = pack_xy(x, y);
      }
      if (wid == 0) STAMP(3, 5);
    }
  }
#endif
}
#if defined(__HIP_DEVICE_COMPILE__)
#pragma clang attribute pop
#endif

extern "C" void kernel_launch(void* const* d_in, const int* in_sizes, int n_in, void* d_out, int out_size, void* d_ws, size_t ws_size,
                              hipStream_t stream) {
  (void)hipFuncSetAttribute((const void*)block_kernel, hipFuncAttributeMaxDynamicSharedMemorySize, LDS_BYTES);
  block_kernel<<<256, 512, LDS_BYTES, stream>>>((const u16*)d_in[0], (const u16*)d_in[1], (const u16*)d_in[2], (const u16*)d_in[3],
                                                (const u16*)d_in[5], (const u16*)d_in[6], (u16*)d_out, (char*)d_ws);
}
```

```cpp
#include <hip/hip_runtime.h>
#include <hip/hip_bf16.h>

#define LAS __attribute__((address_space(3)))
#define GAS __attribute__((address_space(1)))
#define DI __device__ __forceinline__
typedef __bf16 bf16x8 __attribute__((ext_vector_type(8)));
typedef __bf16 bf16x2 __attribute__((ext_vector_type(2)));
typedef short s16x4 __attribute__((ext_vector_type(4)));
typedef short s16x8 __attribute__((ext_vector_type(8)));
typedef float f32x2 __attribute__((ext_vector_type(2)));
typedef float f32x4 __attribute__((ext_vector_type(4)));
typedef unsigned u32x2 __attribute__((ext_vector_type(2)));
typedef unsigned u32x4 __attribute__((ext_vector_type(4)));
typedef unsigned short u16;

constexpr int S_ = 2048, D_ = 1024, FF_ = 3584;
constexpr float LN_EPS = 1e-5f;

constexpr int C_EPOCH = 0, C_TMO = 1, C_ST = 2, C_CS1 = 18, C_CS3 = 34, C_KV = 50, C_O = 66, C_H = 82, C_H1 = 98, C_P4 = 114, C_LINES = 130;
constexpr unsigned WS_XST = 36864;
constexpr unsigned WS_CS1 = WS_XST + S_ * 8;
constexpr unsigned WS_CS3 = WS_CS1 + 16 * 16 * 192 * 4;
constexpr unsigned WS_HST = WS_CS3 + 16 * 16 * 224 * 4;
constexpr unsigned KVP_BYTES = 8192 + 512;
constexpr unsigned WS_KVP = WS_HST + S_ * 16 * 8;
#define BLOCKED 1
constexpr unsigned WS_O = WS_KVP + 256 * KVP_BYTES;
constexpr unsigned WS_H = WS_O + S_ * D_ * 2;
constexpr unsigned WS_H1 = WS_H + S_ * D_ * 2;
constexpr unsigned WS_P4 = WS_O, WS_END = WS_H1 + S_ * FF_ * 2;
static_assert(C_LINES * 256 <= (int)WS_XST && WS_O % 256 == 0 && WS_KVP % 256 == 0, "");

constexpr int L_WT = 120 * 1024;
constexpr int L_TAB = 149 * 1024;
constexpr int L_DUMMY = L_TAB;
constexpr int L_RST = L_TAB + 256;
constexpr int L_CS = L_RST + 1024;
constexpr int L_CSS = L_CS + 1024;
constexpr int LDS_BYTES = L_CSS + 8192;
static_assert(LDS_BYTES <= 160 * 1024, "");

#define STAMP(k, i) do {} while (0)
#define TS_DECL
#define TS(t)
#define TS_OUT(k)

#if defined(__HIP_DEVICE_COMPILE__)
#pragma clang attribute push(__attribute__((target("no-packed-fp32-ops"))), apply_to = function)
typedef __amdgpu_buffer_rsrc_t rsrc_t;
DI float u2f(unsigned u) { return __builtin_bit_cast(float, u); }
DI unsigned f2u(float f) { return __builtin_bit_cast(unsigned, f); }
DI bool wave_all(bool p) { return __builtin_amdgcn_ballot_w64(p) == ~0ull; }
#define CODE_AHEAD 36864
constexpr int SC1 = 16;
template <int N> DI void wait_vm() { asm volatile("s_waitcnt vmcnt(%0)" ::"n"(N) : "memory"); }
DI void wait_lgkm0() { asm volatile("s_waitcnt lgkmcnt(0)" ::: "memory"); }
DI void bar() { asm volatile("" ::: "memory"); __builtin_amdgcn_s_barrier(); asm volatile("" ::: "memory"); }
DI void lbar() { wait_lgkm0(); bar(); }
DI unsigned pk2(float a, float b) { f32x2 v = {a, b}; return __builtin_bit_cast(unsigned, __builtin_convertvector(v, bf16x2)); }
DI float bflo(unsigned u) { return u2f(u << 16); }
DI float bfhi(unsigned u) { return u2f(u & 0xffff0000u); }
DI bf16x8 ld_b128(LAS const char* p) { return *(LAS const bf16x8*)p; }
DI bf16x8 ld_tr2(LAS const char* p0, LAS const char* p1) {
  s16x4 a = __builtin_amdgcn_ds_read_tr16_b64_v4i16((LAS s16x4*)p0);
  s16x4 b = __builtin_amdgcn_ds_read_tr16_b64_v4i16((LAS s16x4*)p1);
  return __builtin_bit_cast(bf16x8, __builtin_shufflevector(a, b, 0, 1, 2, 3, 4, 5, 6, 7));
}
DI float x32_sum(float v) { auto r = __builtin_amdgcn_permlane32_swap(f2u(v), f2u(v), false, false); return u2f(r[0]) + u2f(r[1]); }
DI float x16_sum(float v) { auto r = __builtin_amdgcn_permlane16_swap(f2u(v), f2u(v), false, false); return u2f(r[0]) + u2f(r[1]); }
template <int CTRL> DI float dpp_add(float v) { return v + u2f(__builtin_amdgcn_update_dpp(0, (int)f2u(v), CTRL, 0xf, 0xf, true)); }
DI float half_sum(float v) {
  v = dpp_add<0xB1>(v);
  v = dpp_add<0x4E>(v);
  v = dpp_add<0x141>(v);
  v = dpp_add<0x140>(v);
  return x16_sum(v);
}
#define MFMA16(a, b, c) __builtin_amdgcn_mfma_f32_16x16x32_bf16((a), (b), (c), 0, 0, 0)
DI rsrc_t mkrsrc(const void* p, unsigned bytes) { return __builtin_amdgcn_make_buffer_rsrc((void*)p, 0, bytes, 0x00020000); }
DI void st128(u32x4 v, rsrc_t r, int off) { __builtin_amdgcn_raw_buffer_store_b128(v, r, off, 0, SC1); }
DI u32x4 ld128(rsrc_t r, int off, int soff) { return __builtin_amdgcn_raw_buffer_load_b128(r, off, soff, SC1); }

DI unsigned read_tag(const char* ws) {
  unsigned e;
  asm volatile("s_load_dword %0, %1, 0x0\n\ts_waitcnt lgkmcnt(0)" : "=s"(e) : "s"(ws) : "memory");
  return e + 1u;
}
DI void set_flag(GAS unsigned* ctl, int line, int word, unsigned tag) { __hip_atomic_store(ctl + line * 64 + word, tag, __ATOMIC_RELAXED, __HIP_MEMORY_SCOPE_AGENT); }
DI void acquire() {
  asm volatile("" ::: "memory");
}
DI void poll(GAS unsigned* ctl, int line, int lo, int n, unsigned tag, int lane) {
  GAS unsigned* p = ctl + line * 64 + lo + (lane < n ? lane : 0);
  unsigned spins = 0;
  while (!wave_all(__hip_atomic_load(p, __ATOMIC_RELAXED, __HIP_MEMORY_SCOPE_AGENT) == tag)) {
    __builtin_amdgcn_s_sleep(1);
    if (++spins > (1u << 17)) { if (lane == 0) __hip_atomic_store(ctl + C_TMO * 64, (unsigned)line, __ATOMIC_RELAXED, __HIP_MEMORY_SCOPE_AGENT); break; }
  }
  acquire();
}

template <int BN> DI constexpr int bswz(int k) {
  return BN == 224 ? (((k >> 3) & 1) << 1)
       : BN == 128 ? (((k & 3) | (((k >> 3) & 1) << 2)) << 1)
                   : ((((k >> 1) & 1) | (((k >> 3) & 1) << 1)) << 1);
}

DI constexpr int aswz(int r) { return (-(r >> 2)) & 3; }

template <int BN, int NS, int NT, bool AROW, int LAG = 0>
struct Loader {
  static constexpr int PA = 4, PB = BN / 32, PW = PA + PB, BST = BN * 128, CPR = BN / 8;
  static_assert(NS * (16384 + BST) <= L_TAB, "");
  LAS char* dst0; rsrc_t ra, rb; int va[PA], vb[PB]; int ldb64, own0, tn;
  static constexpr int NOWN = BN == 128 ? 3 : BN == 224 ? 1 : 0;
  DI int ktile(int t) const { return AROW ? ((t + own0) & (NT - 1)) : (t < NOWN ? own0 + t : (t - NOWN < own0 ? t - NOWN : t)); }
  DI void issueA(int t) {
    const int kk = ktile(t), slot = t % NS;
#pragma unroll
    for (int i = 0; i < PA; ++i)
      __builtin_amdgcn_raw_ptr_buffer_load_lds(ra, dst0 + slot * 16384 + i * 4096, 16, va[i], kk * (AROW ? 128 : 16384), 0, AROW ? 0 : SC1);
  }
  DI void issueB(int t) {
    const int kk = ktile(t), slot = t % NS;
#pragma unroll
    for (int i = 0; i < PB; ++i) __builtin_amdgcn_raw_ptr_buffer_load_lds(rb, dst0 + NS * 16384 + slot * BST + i * 4096, 16, vb[i], kk * ldb64, 0, 0);
  }
  DI void init(LAS char* smem, rsrc_t ra_, rsrc_t rb_, int a_off, int lda_b, int b_off, int ldb_b, int segb, int lw, int lane, int own) {
    dst0 = smem + lw * 1024; ra = ra_; rb = rb_; ldb64 = 64 * ldb_b; own0 = own; tn = NS - 1;
#pragma unroll
    for (int i = 0; i < PA; ++i) {
      const int ins = lw + i * 4, r = ins * 8 + (lane >> 3), c = (lane & 7) ^ ((r >> 1) & 7);
      va[i] = AROW ? a_off + r * lda_b + c * 16 : a_off + ins * 1024 + lane * 16;
    }
#pragma unroll
    for (int i = 0; i < PB; ++i) {
      const int ch = (lw + i * 4) * 64 + lane, k = ch / CPR, c = (ch % CPR) ^ bswz<BN>(k);
      vb[i] = b_off + k * ldb_b + (c >> 3) * segb + (c & 7) * 16;
    }
  }
  template <int ALLOW = (NS - 2 - LAG) * PW> DI void step() { wait_vm<ALLOW>(); lbar(); issueA(tn); issueB(tn); ++tn; }
  DI void steps(int n) {
#pragma unroll 1
    for (int t = 0; t < n; ++t) step();
  }
  DI void finish() {
    if constexpr (NS - LAG >= 5) { wait_vm<3 * PW>(); lbar(); }
    if constexpr (NS - LAG >= 4) { wait_vm<2 * PW>(); lbar(); }
    if constexpr (NS - LAG >= 3) { wait_vm<1 * PW>(); lbar(); }
    wait_vm<0>(); lbar();
  }
};
template <int BN> DI void load_wtile(LAS char* dst, rsrc_t r, int off, int ldb_b, int segb, int lw, int lane) {
#pragma unroll
  for (int i = 0; i < BN / 32; ++i) {
    const int ch = (lw + i * 4) * 64 + lane, k = ch / (BN / 8), c = (ch % (BN / 8)) ^ bswz<BN>(k);
    __builtin_amdgcn_raw_ptr_buffer_load_lds(r, dst + (lw + i * 4) * 1024, 16, off + k * ldb_b + (c >> 3) * segb + (c & 7) * 16, 0, 0, 0);
  }
}
DI int opaque(int v) { asm volatile("" : "+v"(v)); return v; }

DI void touch_lines(LAS char* smem, rsrc_t r, int base, int line, int nlines) {
  __builtin_amdgcn_raw_ptr_buffer_load_lds(r, smem + L_DUMMY, 4, line < nlines ? base + line * 128 : (int)0x80000000, 0, 0, 0);
}

template <int BN> DI void colsum_tile(LAS char* img, rsrc_t rws, unsigned off, int lw, int lane) {
  constexpr int RS = BN * 2;
  const int r16 = lane & 15, g = lane >> 4, q = (lane & 15) >> 2, p = lane & 3;
  const s16x8 one_ = {0x3f80, 0x3f80, 0x3f80, 0x3f80, 0x3f80, 0x3f80, 0x3f80, 0x3f80};
  LAS const char* rd[4];
#pragma unroll
  for (int i = 0; i < 4; ++i) {
    const int nt = lw + 4 * i < BN / 16 ? lw + 4 * i : lw;
    rd[i] = img + (8 * g + q) * RS + (((2 * nt + (p >> 1)) ^ bswz<BN>(8 * g + q)) * 16) + 8 * (p & 1);
  }
  s16x4 v[4][4];
#define TR4(a, b, c, d, r) "ds_read_b64_tr_b16 %" #a ", %" #r "\n\tds_read_b64_tr_b16 %" #b ", %" #r " offset:%c20\n\t" \
                           "ds_read_b64_tr_b16 %" #c ", %" #r " offset:%c21\n\tds_read_b64_tr_b16 %" #d ", %" #r " offset:%c22\n\t"
  asm volatile(TR4(0, 1, 2, 3, 16) TR4(4, 5, 6, 7, 17) TR4(8, 9, 10, 11, 18) TR4(12, 13, 14, 15, 19) "s_waitcnt lgkmcnt(0)"
               : "=&v"(v[0][0]), "=&v"(v[0][1]), "=&v"(v[0][2]), "=&v"(v[0][3]), "=&v"(v[1][0]), "=&v"(v[1][1]), "=&v"(v[1][2]), "=&v"(v[1][3]),
                 "=&v"(v[2][0]), "=&v"(v[2][1]), "=&v"(v[2][2]), "=&v"(v[2][3]), "=&v"(v[3][0]), "=&v"(v[3][1]), "=&v"(v[3][2]), "=&v"(v[3][3])
               : "v"(rd[0]), "v"(rd[1]), "v"(rd[2]), "v"(rd[3]), "i"(4 * RS), "i"(32 * RS), "i"(36 * RS) : "memory");
#undef TR4
  f32x4 c[4];
#pragma unroll
  for (int i = 0; i < 4; ++i) {
    c[i] = (f32x4){0.f, 0.f, 0.f, 0.f};
    c[i] = MFMA16(__builtin_bit_cast(bf16x8, __builtin_shufflevector(v[i][0], v[i][1], 0, 1, 2, 3, 4, 5, 6, 7)), __builtin_bit_cast(bf16x8, one_), c[i]);
    c[i] = MFMA16(__builtin_bit_cast(bf16x8, __builtin_shufflevector(v[i][2], v[i][3], 0, 1, 2, 3, 4, 5, 6, 7)), __builtin_bit_cast(bf16x8, one_), c[i]);
  }
  const f32x4 out = r16 == 0 ? c[0] : r16 == 1 ? c[1] : r16 == 2 ? c[2] : c[3];
  st128(__builtin_bit_cast(u32x4, out), rws, lw + 4 * r16 < BN / 16 ? off + (lw + 4 * r16) * 64 + g * 16 : 0x80000000u);
}
DI f32x4 sum4(u32x4 (&t)[4]) {
  asm volatile("" : "+v"(t[0]), "+v"(t[1]), "+v"(t[2]), "+v"(t[3]));
  return (__builtin_bit_cast(f32x4, t[0]) + __builtin_bit_cast(f32x4, t[1])) + (__builtin_bit_cast(f32x4, t[2]) + __builtin_bit_cast(f32x4, t[3]));
}
DI f32x4 lds_sum4(LAS const char* p) {
  return (*(LAS const f32x4*)p + *(LAS const f32x4*)(p + 1024)) + (*(LAS const f32x4*)(p + 2048) + *(LAS const f32x4*)(p + 3072));
}
DI unsigned peek(GAS unsigned* ctl, int line, int lane) { return __hip_atomic_load(ctl + line * 64 + lane, __ATOMIC_RELAXED, __HIP_MEMORY_SCOPE_AGENT); }
DI void confirm(unsigned seen, GAS unsigned* ctl, int line, unsigned tag, int lane) {
  asm volatile("" : "+v"(seen));
  if (!wave_all(seen == tag)) poll(ctl, line, 0, 64, tag, lane);
  else acquire();
}

DI void mfma16(f32x4& c, const bf16x8& a, const bf16x8& b) { asm volatile("v_mfma_f32_16x16x32_bf16 %0, %1, %2, %0" : "+v"(c) : "v"(a), "v"(b)); }
DI void mfma_drain() { asm volatile("s_nop 7\n\ts_nop 7" ::: "memory"); }
template <int TM, int TN> struct Frag { s16x4 b[TN][2]; bf16x8 a[TM]; };
template <int BN, int NS, int NT, int TM, int TN, bool DEEP, bool OWN, class NMap, class Mid>
DI void mma_loop(LAS char* smem, f32x4 (&acc)[TM][TN], int row0, int lane, NMap&& nt, Mid&& mid) {
  constexpr int RS = BN * 2, BST = BN * 128, NL = TM + 2 * TN, NM = TM * TN;
  typedef Frag<TM, TN> F;
  const int r16 = lane & 15, g = lane >> 4, q = (lane & 15) >> 2, p = lane & 3;
  constexpr bool AROW = BN == 192;
  const int aoff = AROW ? (row0 + r16) * 128 + ((g ^ (r16 >> 1)) * 16) : (row0 + r16) * 64 + ((g ^ aswz(r16)) * 16);
  const int aoff1 = AROW ? aoff ^ 64 : aoff + 8192;
  int boff[TN];
#pragma unroll
  for (int j = 0; j < TN; ++j) boff[j] = NS * 16384 + (8 * g + q) * RS + (((2 * nt(j) + (p >> 1)) ^ bswz<BN>(8 * g + q)) * 16) + 8 * (p & 1);
#pragma unroll
  for (int i = 0; i < TM; ++i)
#pragma unroll
    for (int j = 0; j < TN; ++j) { acc[i][j] = (f32x4){0.f, 0.f, 0.f, 0.f}; asm volatile("" : "+v"(acc[i][j])); }
  auto ld1 = [&](F& f, int slot, int s, int k) {
    if (k == 0 || k > 2 * TN) { const int i = k == 0 ? 0 : k - 2 * TN; f.a[i] = ld_b128(smem + slot * 16384 + (s ? aoff1 : aoff) + i * (AROW ? 2048 : 1024)); }
    else { const int j = (k - 1) >> 1, u = (k - 1) & 1; f.b[j][u] = __builtin_amdgcn_ds_read_tr16_b64_v4i16((LAS s16x4*)(smem + slot * BST + boff[j] + (32 * s + 4 * u) * RS)); }
  };
  auto mm1 = [&](const F& f, int m) {
    const int i = m / TN, j = m % TN;
    const bf16x8 b = __builtin_bit_cast(bf16x8, __builtin_shufflevector(f.b[j][0], f.b[j][1], 0, 1, 2, 3, 4, 5, 6, 7));
    if (AROW && j >= 4) mfma16(acc[i][j], f.a[i], b); else mfma16(acc[i][j], b, f.a[i]);
  };
  auto ldall = [&](F& f, int slot, int s) {
#pragma unroll
    for (int k = 0; k < NL; ++k) ld1(f, slot, s, k);
  };
  auto mmall = [&](const F& f) {
#pragma unroll
    for (int m = 0; m < NM; ++m) mm1(f, m);
  };
  auto sub = [&](const F& x, F& u, int slot, int s) {
    constexpr int SP = DEEP ? NM : (NL < NM ? NL : NM - 2);
#pragma unroll
    for (int m = 0; m < NM; ++m) {
      mm1(x, m);
#pragma unroll
      for (int k = (m * NL + SP - 1) / SP; k < NL && k < ((m + 1) * NL + SP - 1) / SP; ++k) ld1(u, slot, s, k);
      __builtin_amdgcn_sched_barrier(0);
    }
  };
  auto nxt = [](int s) { return s == NS - 1 ? 0 : s + 1; };
  lbar();
  if (threadIdx.x < 64) STAMP(BN == 192 ? 0 : BN == 64 ? 1 : BN == 224 ? 2 : 3, 1);
  TS_DECL; TS(0);
  int slot = 0;
  if constexpr (!DEEP) {
    F f0, f1;
    ldall(f0, 0, 0);
    if constexpr (OWN) {
      sub(f0, f1, 0, 1);
      mid();
      bar();
      mmall(f1);
      __builtin_amdgcn_sched_barrier(0);
      TS(32);
      lbar();
      TS(1);
      slot = 1;
      ldall(f0, 1, 0);
    }
#pragma unroll 1
    for (int t = OWN ? 1 : 0; t < NT - 1; ++t) {
      sub(f0, f1, slot, 1);
      TS(32 + t);
      lbar();
      TS(t + 1);
      slot = nxt(slot);
      sub(f1, f0, slot, 0);
    }
    sub(f0, f1, slot, 1);
    lbar();
    TS(NT); TS_OUT(BN == 192 ? 0 : BN == 64 ? 1 : BN == 224 ? 2 : 3);
    mmall(f1);
  } else {
    F f0, f1, g0, g1;
    auto tile = [&](const F& x0, const F& x1, F& u0, F& u1, int t) {
      TS(32 + t);
      bar();
      TS(t + 1);
      slot = nxt(slot);
      sub(x0, u0, slot, 0);
      sub(x1, u1, slot, 1);
    };
    ldall(f0, 0, 0); ldall(f1, 0, 1);
    int t0 = 0;
    if constexpr (OWN) {
      tile(f0, f1, g0, g1, 0);
      mid();
      tile(g0, g1, f0, f1, 1);
      mmall(f0); mmall(f1);
      __builtin_amdgcn_sched_barrier(0);
      TS(34);
      lbar();
      TS(3);
      slot = 3;
      ldall(g0, 3, 0); ldall(g1, 3, 1);
      tile(g0, g1, f0, f1, 3);
      t0 = 4;
    }
#pragma unroll 1
    for (int t = t0; t < NT - 2; t += 2) {
      tile(f0, f1, g0, g1, t);
      tile(g0, g1, f0, f1, t + 1);
    }
    tile(f0, f1, g0, g1, NT - 2);
    lbar();
    TS(NT); TS_OUT(BN == 192 ? 0 : BN == 64 ? 1 : BN == 224 ? 2 : 3);
    mmall(g0); mmall(g1);
  }
  mfma_drain();
#pragma unroll
  for (int i = 0; i < TM; ++i)
#pragma unroll
    for (int j = 0; j < TN; ++j) asm volatile("" : "+v"(acc[i][j]));
}
DI void swap_pair(f32x4& x, f32x4& y) {
#pragma unroll
  for (int e = 0; e < 4; ++e) {
    auto r = __builtin_amdgcn_permlane16_swap(f2u(x[e]), f2u(y[e]), false, false);
    x[e] = u2f(r[0]); y[e] = u2f(r[1]);
  }
}
DI void add_bf8(f32x4& x, f32x4& y, const u32x4& rv) {
  x[0] += bflo(rv[0]); x[1] += bfhi(rv[0]); x[2] += bflo(rv[1]); x[3] += bfhi(rv[1]);
  y[0] += bflo(rv[2]); y[1] += bfhi(rv[2]); y[2] += bflo(rv[3]); y[3] += bfhi(rv[3]);
}
DI u32x4 pack_xy(const f32x4& x, const f32x4& y) { return (u32x4){pk2(x[0], x[1]), pk2(x[2], x[3]), pk2(y[0], y[1]), pk2(y[2], y[3])}; }

template <class Op> DI void red2(float& a, float& b, Op&& op) {
  auto r = __builtin_amdgcn_permlane16_swap(f2u(a), f2u(b), false, false);
  const float c = op(u2f(r[0]), u2f(r[1]));
  auto t = __builtin_amdgcn_permlane32_swap(f2u(c), f2u(c), false, false);
  const float d = op(u2f(t[0]), u2f(t[1]));
  auto u = __builtin_amdgcn_permlane16_swap(f2u(d), f2u(d), false, false);
  a = u2f(u[0]); b = u2f(u[1]);
}
DI void dual_softmax(const f32x4 (&p)[2][2], const float (&sc)[2], f32x4 (&e)[2][2][2]) {
  float mx[2], mn[2], s1[2], s2[2];
#pragma unroll
  for (int t = 0; t < 2; ++t) {
    mx[t] = p[t][0][0]; mn[t] = p[t][0][0];
#pragma unroll
    for (int r = 1; r < 8; ++r) { mx[t] = __builtin_fmaxf(mx[t], p[t][r >> 2][r & 3]); mn[t] = __builtin_fminf(mn[t], p[t][r >> 2][r & 3]); }
  }
  red2(mx[0], mx[1], [](float x, float y) { return __builtin_fmaxf(x, y); });
  red2(mn[0], mn[1], [](float x, float y) { return __builtin_fminf(x, y); });
#pragma unroll
  for (int t = 0; t < 2; ++t) {
    const float a = -mx[t] * sc[t], b = mn[t] * sc[t];
    s1[t] = 0.f; s2[t] = 0.f;
#pragma unroll
    for (int r = 0; r < 8; ++r) {
      const float x = __builtin_amdgcn_exp2f(__builtin_fmaf(p[t][r >> 2][r & 3], sc[t], a));
      const float y = __builtin_amdgcn_exp2f(__builtin_fmaf(p[t][r >> 2][r & 3], -sc[t], b));
      e[0][t][r >> 2][r & 3] = x; e[1][t][r >> 2][r & 3] = y;
      s1[t] += x; s2[t] += y;
    }
  }
  red2(s1[0], s1[1], [](float x, float y) { return x + y; });
  red2(s2[0], s2[1], [](float x, float y) { return x + y; });
#pragma unroll
  for (int t = 0; t < 2; ++t) {
    const float r1 = __builtin_amdgcn_rcpf(s1[t]), r2 = __builtin_amdgcn_rcpf(s2[t]);
#pragma unroll
    for (int r = 0; r < 8; ++r) { e[0][t][r >> 2][r & 3] *= r1; e[1][t][r >> 2][r & 3] *= r2; }
  }
}
DI bf16x8 pack_frag(const f32x4& x, const f32x4& y) { return __builtin_bit_cast(bf16x8, pack_xy(x, y)); }
DI void st4(LAS char* p, const f32x4& a, const f32x4& b, const f32x4& c, const f32x4& d) {
  *(LAS f32x4*)p = a; *(LAS f32x4*)(p + 1024) = b; *(LAS f32x4*)(p + 2048) = c; *(LAS f32x4*)(p + 3072) = d;
}
DI void ld4(LAS const char* p, f32x4 (&v)[2][2]) {
  v[0][0] = *(LAS const f32x4*)p; v[0][1] = *(LAS const f32x4*)(p + 1024); v[1][0] = *(LAS const f32x4*)(p + 2048); v[1][1] = *(LAS const f32x4*)(p + 3072);
}
#endif

__global__ __launch_bounds__(512, 2) void block_kernel(const u16* __restrict__ X, const u16* __restrict__ WQKV, const u16* __restrict__ WPHI,
                                                        const u16* __restrict__ WO, const u16* __restrict__ W1, const u16* __restrict__ W2,
                                                        u16* __restrict__ OUT, char* WS) {
#if defined(__HIP_DEVICE_COMPILE__)
  extern __shared__ __attribute__((aligned(16))) char smem_[];
  LAS char* smem = (LAS char*)smem_;
  const int tid = threadIdx.x, wid = __builtin_amdgcn_readfirstlane(tid >> 6);
  int lane = tid & 63;
  const int bid = blockIdx.x, xcd = bid & 7, loc = bid >> 3;
  const int tm = (xcd & 1) * 8 + (loc & 7), gp = (xcd >> 1) * 4 + (loc >> 3);
  const bool loader = wid >= 4;
  const int lw = wid & 3;
  GAS unsigned* ctl = (GAS unsigned*)WS;
  const rsrc_t rws = mkrsrc(WS, WS_END);
  if (wid == 0) STAMP(0, 0);
  unsigned tag = 0;

  int r16, g, cpair, cswz;
  auto relane = [&]() {
    lane = opaque(lane);
    r16 = lane & 15; g = lane >> 4;
    cpair = (g & 1) * 16 + (g >> 1) * 8;
    cswz = ((cpair >> 3) ^ aswz(r16)) * 16;
  };
  {
    constexpr int NT = 16, NS = 3;
    constexpr int L_PHI = 0;
    constexpr int L_XV = 2 * 17408;
    constexpr int L_VF = L_XV + 16384;
    constexpr int L_XQ = L_VF + 8192;
    constexpr int L_KVT = L_XQ;
    constexpr int L_WF = L_XQ + 16384;
    static_assert(L_WF + 4096 <= 5 * 16384, "phase 2's B ring is filled during this epilogue");
    relane();
    const int wm = (wid >> 1) & 1, hh = wid & 1, mi = wid >> 2;
    const int head = gp * 2 + hh;
    const unsigned kvp_grp = WS_KVP + gp * 16 * KVP_BYTES;
    LAS char* const xk = smem + L_PHI + hh * 17408 + (wm * 64 + 32) * 136 + lane * 16;
    bf16x8 wf[2];
    f32x4 fq[2][2], fk[2][2], fv[2][2];
    if (loader) {
      const rsrc_t rb = mkrsrc(WQKV, D_ * 3 * D_ * 2), ro = mkrsrc(WO, D_ * D_ * 2);
      Loader<192, NS, NT, true> L;
      const rsrc_t rx = mkrsrc(X, S_ * D_ * 2);
      L.init(smem, rx, rb, tm * 128 * D_ * 2, D_ * 2, gp * 128, 3 * D_ * 2, 2048, lw, lane, (tm & 3) * 4);
      L.issueA(0); L.issueB(0); L.issueA(1); L.issueB(1);
      if (lw == 0) {
        unsigned long long pc;
        asm volatile("s_getpc_b64 %0" : "=s"(pc));
        touch_lines(smem, mkrsrc((const void*)(pc & ~127ull), CODE_AHEAD), (loc * 16) * 128, lane < 16 ? lane : 4096, 16);
      }
      tag = read_tag(WS);
      __builtin_amdgcn_s_setprio(3);
      if (wid == 4) STAMP(4, 1);
      auto cs1 = [&](int t) { if ((tm >> 2) == t) colsum_tile<192>(smem + NS * 16384 + (t % NS) * 24576, rws, WS_CS1 + (gp * 16 + tm) * 768, lw, lane); };
      L.step();
      cs1(0);
      const int xrow = tm * 128 + gp * 8 + lw * 2 + (lane >> 5);
      u32x4 xv[4];
#pragma unroll
      for (int j = 0; j < 4; ++j) xv[j] = __builtin_amdgcn_raw_buffer_load_b128(rx, xrow * (D_ * 2) + (lane & 31) * 16, j * 512, 0);
      L.step<14>();
      cs1(1);
      L.step<14>();
      cs1(2);
      L.step();
      cs1(3);
      {
        asm volatile("" : "+v"(xv[0]), "+v"(xv[1]), "+v"(xv[2]), "+v"(xv[3]));
        float f[32], sum = 0.f, qq = 0.f;
#pragma unroll
        for (int i = 0; i < 16; ++i) { f[2 * i] = bflo(xv[i >> 2][i & 3]); f[2 * i + 1] = bfhi(xv[i >> 2][i & 3]); sum += f[2 * i] + f[2 * i + 1]; }
        const float mean = half_sum(sum) * (1.0f / D_);
#pragma unroll
        for (int i = 0; i < 32; ++i) { const float d = f[i] - mean; qq = __builtin_fmaf(d, d, qq); }
        const float rstd = 1.0f / __builtin_sqrtf(half_sum(qq) * (1.0f / D_) + LN_EPS);
        __builtin_amdgcn_raw_buffer_store_b64(__builtin_bit_cast(u32x2, (f32x2){mean, rstd}), rws, (lane & 31) == 0 ? WS_XST + xrow * 8 : 0x80000000u, 0, SC1);
      }
      L.step();
      L.step<0>();
      if (lane == 0) { set_flag(ctl, C_CS1 + gp, tm * 4 + lw, tag); set_flag(ctl, C_ST + tm, gp * 4 + lw, tag); }
      L.steps(loc & 3);
      load_wtile<224>(smem + L_WT, mkrsrc(W1, D_ * FF_ * 2), tm * 64 * (FF_ * 2) + gp * 448, FF_ * 2, 128, lw, lane);
      touch_lines(smem, ro, bid * 8192, lane < 16 ? lw * 16 + lane : 4096, 64);
      L.step();
      L.step<18>();
      L.steps(3 - (loc & 3));
      const unsigned seen = peek(ctl, C_CS1 + gp, lane), seen2 = peek(ctl, C_ST + tm, lane);
      L.steps(2);
      confirm(seen, ctl, C_CS1 + gp, tag, lane);
      confirm(seen2, ctl, C_ST + tm, tag, lane);
      u32x4 gt[4];
#pragma unroll
      for (int j = 0; j < 4; ++j) gt[j] = ld128(rws, WS_CS1 + (gp * 16 + lw * 4) * 768 + lane * 16, j * 768);
      u32x4 xs = ld128(rws, WS_XST + tm * 1024 + lane * 16, 0);
      L.step();
      wait_vm<10>(); lbar();
      *(LAS f32x4*)(smem + L_CSS + lw * 1024 + lane * 16) = sum4(gt);
      asm volatile("" : "+v"(xs));
      if (lw == 1) *(LAS u32x4*)(smem + L_RST + lane * 16) = xs;
      wait_vm<0>(); lbar();
      if (lw == 0) *(LAS f32x4*)(smem + L_CS + lane * 16) = lds_sum4(smem + L_CSS + lane * 16);
      lbar();
      bar();
      __builtin_amdgcn_s_setprio(1);
      ld4(xk, fk);
      wf[0] = ld_b128(smem + L_WF + hh * 2048 + lane * 16);
      wf[1] = ld_b128(smem + L_WF + hh * 2048 + 1024 + lane * 16);
      ld4(smem + L_XV + lw * 4096 + lane * 16, fv);
    } else {
      {
#pragma unroll
        for (int t = 0; t < 2; ++t) {
          s16x8 w;
#pragma unroll
          for (int j = 0; j < 8; ++j) w[j] = (short)WPHI[head * 1024 + (16 * (j >> 2) + 4 * g + (j & 3)) * 32 + 16 * t + r16];
          wf[t] = __builtin_bit_cast(bf16x8, w);
        }
        {
          const int id = loc * 4 + wid;
          const char* a0 = WS + (size_t)id * (256 * 1024 - 4096);
          const char* a1 = (const char*)W1 + (size_t)id * 57344;
          const char* a2 = (const char*)W2 + (size_t)id * 57344;
          const char* a3 = (const char*)OUT + (size_t)id * 32768;
          const char* a4 = (const char*)WO + (size_t)id * 16384;
          unsigned d0, d1, d2, d3, d4, d5, d6, d7;
          asm volatile("s_load_dword %0, %8, 0x0\n\ts_load_dword %1, %8, 0x10000\n\ts_load_dword %2, %8, 0x20000\n\ts_load_dword %3, %8, 0x30000\n\t"
                       "s_load_dword %4, %9, 0x0\n\ts_load_dword %5, %10, 0x0\n\ts_load_dword %6, %11, 0x0\n\ts_load_dword %7, %12, 0x0\n\ts_waitcnt lgkmcnt(0)"
                       : "=&s"(d0), "=&s"(d1), "=&s"(d2), "=&s"(d3), "=&s"(d4), "=&s"(d5), "=&s"(d6), "=&s"(d7)
                       : "s"(a0), "s"(a1), "s"(a2), "s"(a3), "s"(a4) : "memory");
        }
        tag = read_tag(WS);
      }
      if (wid == 0) STAMP(4, 0);
      f32x4 acc[4][6];
      mma_loop<192, NS, NT, 4, 6, false, false>(smem, acc, wm * 64, lane, [&](int j) { return (j >> 1) * 4 + hh * 2 + (j & 1); }, []() {});
      if (wid == 0) STAMP(0, 2);
      if (wm == 0) {
        *(LAS bf16x8*)(smem + L_WF + hh * 2048 + lane * 16) = wf[0];
        *(LAS bf16x8*)(smem + L_WF + hh * 2048 + 1024 + lane * 16) = wf[1];
      }
      st4(xk, acc[2][2], acc[2][3], acc[3][2], acc[3][3]);
      st4(smem + L_XV + wid * 4096 + lane * 16, acc[2][4], acc[2][5], acc[3][4], acc[3][5]);
      lbar();
      if (wid == 0) STAMP(4, 4);
      st4(smem + L_XQ + wid * 4096 + lane * 16, acc[2][0], acc[2][1], acc[3][0], acc[3][1]);
#pragma unroll
      for (int t = 0; t < 2; ++t)
#pragma unroll
        for (int u = 0; u < 2; ++u) { fq[t][u] = acc[t][u]; fk[t][u] = acc[t][2 + u]; fv[t][u] = acc[t][4 + u]; }
    }
    const int rbase = wm * 64 + mi * 32, q = r16 >> 2, p = lane & 3;
    LAS const float* cs = (LAS const float*)(smem + L_CS) + hh * 32;
    LAS const f32x2* rst = (LAS const f32x2*)(smem + L_RST) + rbase;
    auto feature = [&](const f32x4 (&x)[2][2], int part, f32x4 (&e)[2][2][2]) {
      const f32x4 c0 = *(LAS const f32x4*)(cs + part * 64 + 4 * g), c1 = *(LAS const f32x4*)(cs + part * 64 + 16 + 4 * g);
      f32x4 pp[2][2];
      float sc[2];
#pragma unroll
      for (int t = 0; t < 2; ++t) {
        const f32x2 ms = rst[t * 16 + r16];
        sc[t] = ms[1] * 1.4426950408889634f;
        f32x4 y0, y1;
#pragma unroll
        for (int e_ = 0; e_ < 4; ++e_) { y0[e_] = __builtin_fmaf(-ms[0], c0[e_], x[t][0][e_]); y1[e_] = __builtin_fmaf(-ms[0], c1[e_], x[t][1][e_]); }
        const bf16x8 yb = pack_frag(y0, y1);
        pp[t][0] = MFMA16(wf[0], yb, ((f32x4){0.f, 0.f, 0.f, 0.f}));
        pp[t][1] = MFMA16(wf[1], yb, ((f32x4){0.f, 0.f, 0.f, 0.f}));
      }
      dual_softmax(pp, sc, e);
    };
    if (wid == 0) STAMP(4, 5);
    {
      const float cv0 = cs[128 + r16], cv1 = cs[144 + r16];
#pragma unroll
      for (int t = 0; t < 2; ++t) {
        const f32x4 s01 = *(LAS const f32x4*)(rst + t * 16 + 4 * g), s23 = *(LAS const f32x4*)(rst + t * 16 + 4 * g + 2);
        const float mv[4] = {s01[0], s01[2], s23[0], s23[2]}, rv[4] = {s01[1], s01[3], s23[1], s23[3]};
#pragma unroll
        for (int e_ = 0; e_ < 4; ++e_) {
          fv[t][0][e_] = __builtin_fmaf(-mv[e_], cv0, fv[t][0][e_]) * rv[e_];
          fv[t][1][e_] = __builtin_fmaf(-mv[e_], cv1, fv[t][1][e_]) * rv[e_];
        }
      }
      LAS char* vd = smem + (loader ? L_XV + lw * 4096 : L_VF + lw * 2048) + lane * 16;
      *(LAS bf16x8*)vd = pack_frag(fv[0][0], fv[1][0]);
      *(LAS bf16x8*)(vd + 1024) = pack_frag(fv[0][1], fv[1][1]);
      f32x4 e[2][2][2];
      feature(fk, 1, e);
      LAS char* wr = smem + L_PHI + hh * 17408 + (rbase + r16) * 136 + g * 8;
#pragma unroll
      for (int w = 0; w < 2; ++w)
#pragma unroll
        for (int t = 0; t < 2; ++t)
#pragma unroll
          for (int u = 0; u < 2; ++u)
            *(LAS u32x2*)(wr + t * 16 * 136 + w * 64 + u * 32) = (u32x2){pk2(e[w][t][u][0], e[w][t][u][1]), pk2(e[w][t][u][2], e[w][t][u][3])};
    }
    if (wid == 0) STAMP(4, 7);
    if (wid == 4) STAMP(4, 9);
    lbar();
    __builtin_amdgcn_s_setprio(0);
    if (wid == 0) STAMP(4, 8);
    {
      f32x4 k0 = {0.f, 0.f, 0.f, 0.f}, k1 = k0, zz = k0;
      const s16x8 one_ = {0x3f80, 0x3f80, 0x3f80, 0x3f80, 0x3f80, 0x3f80, 0x3f80, 0x3f80};
      LAS const char* rd = smem + L_PHI + hh * 17408 + (4 * g + q) * 136 + wm * 64 + p * 8;
#pragma unroll
      for (int u = 0; u < 4; ++u) {
        const bf16x8 pa0 = ld_tr2(rd + 32 * u * 136, rd + (32 * u + 16) * 136), pa1 = ld_tr2(rd + 32 * u * 136 + 32, rd + (32 * u + 16) * 136 + 32);
        const bf16x8 vf = ld_b128(smem + ((u & 1) ? L_XV + ((u >> 1) * 2 + hh) * 4096 : L_VF + ((u >> 1) * 2 + hh) * 2048) + mi * 1024 + lane * 16);
        k0 = MFMA16(pa0, vf, k0);
        k1 = MFMA16(pa1, vf, k1);
        zz = MFMA16(mi ? pa1 : pa0, __builtin_bit_cast(bf16x8, one_), zz);
      }
      if (loader) ld4(smem + L_XQ + lw * 4096 + lane * 16, fq);
      const int kb = kvp_grp + tm * KVP_BYTES;
      st128(pack_xy(k0, k1), rws, kb + hh * 4096 + (wm * 2 + mi) * 1024 + lane * 16);
      st128(__builtin_bit_cast(u32x4, zz), rws, r16 == 0 ? kb + 8192 + hh * 256 + wm * 128 + g * 32 + mi * 16 : (int)0x80000000);
      if (wid == 0) STAMP(0, 3);
      wait_vm<0>();
    }
    lbar();
    if (tid == 0) set_flag(ctl, C_KV + gp, tm, tag);
    if (wid == 0) STAMP(0, 4);
    if (loader) {
      Loader<64, 5, 16, false, 1> L2;
      L2.init(smem, rws, mkrsrc(WO, D_ * D_ * 2), WS_O + tm * 128 * D_ * 2, 0, gp * 128, D_ * 2, 128, lw, lane, 0);
      L2.issueB(0); L2.issueB(1); L2.issueB(2); L2.issueB(3); L2.issueB(4);
    }
    bf16x8 pf[2][2];
    {
      f32x4 e[2][2][2];
      feature(fq, 0, e);
#pragma unroll
      for (int t = 0; t < 2; ++t)
#pragma unroll
        for (int w = 0; w < 2; ++w) { pf[t][w] = pack_frag(e[w][t][0], e[w][t][1]); asm volatile("" : "+v"(pf[t][w])); }
    }
    if (wid == 0) {
      STAMP(0, 5);
      poll(ctl, C_KV + gp, 0, 16, tag, lane);
    }
    bar();
    if (wid == 0) STAMP(0, 6);
    {
      u32x4 tz[16];
      if (wid == 7) {
#pragma unroll
        for (int j = 0; j < 16; ++j) tz[j] = ld128(rws, lane < 32 ? kvp_grp + 8192 + lane * 16 : 0x80000000u, j * KVP_BYTES);
      }
      u32x4 t[16];
#pragma unroll
      for (int j = 0; j < 16; ++j) t[j] = ld128(rws, kvp_grp + tid * 16, j * KVP_BYTES);
      if (wid == 7) {
        f32x4 z = __builtin_bit_cast(f32x4, tz[0]);
#pragma unroll
        for (int j = 1; j < 16; ++j) z += __builtin_bit_cast(f32x4, tz[j]);
        if (lane < 32) *(LAS u32x2*)(smem + L_KVT + 8192 + lane * 8) = (u32x2){pk2(z[0], z[1]), pk2(z[2], z[3])};
      }
      f32x4 x = {0.f, 0.f, 0.f, 0.f}, y = x;
#pragma unroll
      for (int j = 0; j < 16; ++j) add_bf8(x, y, t[j]);
      *(LAS u32x4*)(smem + L_KVT + tid * 16) = pack_xy(x, y);
    }
    lbar();
    if (wid == 0) STAMP(0, 7);
    {
      bf16x8 ka[2][2], za[2];
#pragma unroll
      for (int w = 0; w < 2; ++w) {
        ka[w][0] = ld_b128(smem + L_KVT + hh * 4096 + w * 2048 + lane * 16);
        ka[w][1] = ld_b128(smem + L_KVT + hh * 4096 + w * 2048 + 1024 + lane * 16);
        za[w] = ld_b128(smem + L_KVT + 8192 + hh * 128 + w * 64 + g * 16);
      }
#pragma unroll
      for (int t = 0; t < 2; ++t) {
        f32x4 o0 = {0.f, 0.f, 0.f, 0.f}, o1 = o0, dn = o0;
#pragma unroll
        for (int w = 0; w < 2; ++w) { dn = MFMA16(za[w], pf[t][w], dn); o0 = MFMA16(ka[w][0], pf[t][w], o0); o1 = MFMA16(ka[w][1], pf[t][w], o1); }
        const float rd = 1.0f / (dn[0] + 1e-6f);
#pragma unroll
        for (int e_ = 0; e_ < 4; ++e_) { o0[e_] *= rd; o1[e_] *= rd; }
        swap_pair(o0, o1);
        st128(pack_xy(o0, o1), rws, WS_O + (tm * 32 + head) * 8192 + (rbase + t * 16 + r16) * 64 + cswz);
      }
    }
    wait_vm<0>();
    lbar();
    if (tid == 0) set_flag(ctl, C_O + tm, gp, tag);
    if (wid == 0) STAMP(0, 8);
  }

  const int kh = gp >> 3;
  if (loader) {
    __builtin_amdgcn_s_setprio(3);
    relane();
    Loader<64, 5, 16, false, 1> L2;
    L2.init(smem, rws, mkrsrc(WO, D_ * D_ * 2), WS_O + tm * 128 * D_ * 2, 0, gp * 128, D_ * 2, 128, lw, lane, 0);
    colsum_tile<224>(smem + L_WT, rws, WS_CS3 + (gp * 16 + tm) * 896, lw, lane);
    poll(ctl, C_O + tm, 0, 16, tag, lane);
    if (wid == 4) STAMP(1, 0);
    L2.issueA(0); L2.issueA(1); L2.issueA(2); L2.issueA(3); L2.issueA(4);
    if (bid == 0 && wid == 4 && lane == 0) __hip_atomic_store(ctl + C_EPOCH * 64, tag, __ATOMIC_RELAXED, __HIP_MEMORY_SCOPE_AGENT);
    wait_vm<16>(); lbar();
    wait_vm<12>(); lbar();
    L2.tn = 5;
    L2.step<8>();
    L2.step<4 + 6>();
    L2.steps(9);
    L2.finish();
    if (lane == 0) set_flag(ctl, C_CS3 + gp, tm * 4 + lw, tag);
    const rsrc_t r2 = mkrsrc(W2, FF_ * D_ * 2);
    {
      relane();
      Loader<224, 3, 16, false> L;
      L.init(smem, rws, mkrsrc(W1, D_ * FF_ * 2), WS_H + tm * 128 * D_ * 2, 0, gp * 448, FF_ * 2, 128, lw, lane, gp);
      bar();
      L.issueB(0); L.issueB(1);
      wait_vm<7>(); bar();
      L.issueB(2);
      bar();
      if (wid == 4 && lane == 0) set_flag(ctl, C_H + tm, gp, tag);
      poll(ctl, C_H + tm, 0, 16, tag, lane);
      if (wid == 4) STAMP(2, 0);
      L.issueA(1); L.issueA(2);
      u32x4 gt[4], hs[4];
#pragma unroll
      for (int j = 0; j < 4; ++j) hs[j] = ld128(rws, WS_HST + tm * 16384 + lw * 4096 + lane * 16, j * 1024);
      const unsigned seen = peek(ctl, C_CS3 + gp, lane);
      L.tn = 3;
      L.step<4 + 5>();
      L.step<5 + 11>();
      confirm(seen, ctl, C_CS3 + gp, tag, lane);
#pragma unroll
      for (int j = 0; j < 4; ++j) gt[j] = ld128(rws, WS_CS3 + (gp * 16 + lw * 4) * 896 + lane * 16, j * 896);
      *(LAS f32x4*)(smem + L_CSS + 4096 + lw * 1024 + lane * 16) = sum4(hs);
      L.step();
      if (lw == 1) {
        const f32x4 sum = lds_sum4(smem + L_CSS + 4096 + lane * 16);
        const float m0 = sum[0] * (1.0f / D_), m1 = sum[2] * (1.0f / D_);
        const float v0 = __builtin_fmaxf(sum[1] * (1.0f / D_) - m0 * m0, 0.f), v1 = __builtin_fmaxf(sum[3] * (1.0f / D_) - m1 * m1, 0.f);
        *(LAS f32x4*)(smem + L_RST + lane * 16) = (f32x4){m0, 1.0f / __builtin_sqrtf(v0 + LN_EPS), m1, 1.0f / __builtin_sqrtf(v1 + LN_EPS)};
      }
      L.step();
      *(LAS f32x4*)(smem + L_CSS + lw * 1024 + lane * 16) = sum4(gt);
      L.step();
      if (lw == 0) *(LAS f32x4*)(smem + L_CS + lane * 16) = lds_sum4(smem + L_CSS + lane * 16);
      const int stag = (loc & 7) < 6 ? (loc & 7) : 6;
      L.steps(stag);
      touch_lines(smem, r2, bid * 28672, lane < 56 ? lw * 56 + lane : 4096, 224);
      L.step();
      L.step<12>();
      L.steps(6 - stag);
      L.finish();
    }
    {
      relane();
      Loader<128, 4, 28, false, 1> L;
      L.init(smem, rws, r2, WS_H1 + (tm * 112 + kh * 56) * 8192, 0, kh * 1792 * D_ * 2 + gp * 128, D_ * 2, kh ? -1024 : 1024, lw, lane, (7 * (gp & 7) + 1) >> 1);
      lbar();
      L.issueB(0); L.issueB(1); L.issueB(2); L.issueB(3);
      wait_vm<12>(); bar();
      wait_vm<8>(); bar();
      wait_vm<4>(); bar();
      if (wid == 4 && lane == 0) set_flag(ctl, C_H1 + tm, gp, tag);
      L.issueB(4);
      poll(ctl, C_H1 + tm, kh * 8, 8, tag, lane);
      if (wid == 4) STAMP(3, 0);
      L.issueA(3); L.issueA(4);
      L.tn = 5;
      L.step<4>();
      L.steps(22);
      L.finish();
      bar();
    }
  } else {
    relane();
    {
      const int row = tm * 128 + wid * 32 + r16, col = gp * 64 + cpair;
      u32x4 res[2][2];
#pragma unroll
      for (int i = 0; i < 2; ++i)
#pragma unroll
        for (int j = 0; j < 2; ++j) res[i][j] = *(const u32x4*)(X + (size_t)(row + i * 16) * D_ + col + j * 32);
      f32x4 acc[2][4];
      mma_loop<64, 5, 16, 2, 4, true, false>(smem, acc, wid * 32, lane, [](int j) { return j; }, []() {});
      if (wid == 0) STAMP(1, 2);
#pragma unroll
      for (int i = 0; i < 2; ++i) {
        float s = 0.f, qq = 0.f;
#pragma unroll
        for (int j = 0; j < 2; ++j) {
          f32x4 x = acc[i][2 * j], y = acc[i][2 * j + 1];
          swap_pair(x, y);
          add_bf8(x, y, res[i][j]);
          const u32x4 o = pack_xy(x, y);
          st128(o, rws, WS_H + (tm * 32 + gp * 2 + j) * 8192 + (wid * 32 + i * 16 + r16) * 64 + cswz);
          *(LAS u32x4*)(smem + j * 8192 + (wid * 32 + i * 16 + r16) * 64 + cswz) = o;
#pragma unroll
          for (int e = 0; e < 4; ++e) { const float lo = bflo(o[e]), hi = bfhi(o[e]); s += lo + hi; qq = __builtin_fmaf(lo, lo, __builtin_fmaf(hi, hi, qq)); }
        }
        s = x32_sum(x16_sum(s)); qq = x32_sum(x16_sum(qq));
        if (g == 0) __builtin_amdgcn_raw_buffer_store_b64(__builtin_bit_cast(u32x2, (f32x2){s, qq}), rws, WS_HST + ((tm * 16 + gp) * 128 + wid * 32 + i * 16 + r16) * 8, 0, SC1);
      }
      if (wid == 0) STAMP(1, 3);
    }
    const int wm = wid >> 1, wn = wid & 1;
    relane();
    {
      f32x4 acc[4][7];
      mma_loop<224, 3, 16, 4, 7, false, true>(smem, acc, wm * 64, lane, [&](int j) { return j < 6 ? wn * 8 + j : 6 + wn; }, []() { wait_vm<0>(); });
      if (wid == 0) STAMP(2, 2);
      relane();
      f32x4 cs[7];
#pragma unroll
      for (int j = 0; j < 7; ++j) cs[j] = *(LAS const f32x4*)(smem + L_CS + ((j < 6 ? wn * 8 + j : 6 + wn) * 16 + g * 4) * 4);
      const int par = gp & 1, hb = WS_H1 + (tm * 112 + gp * 7) * 8192;
#pragma unroll
      for (int i = 0; i < 4; ++i) {
        const f32x2 ms = *(LAS const f32x2*)(smem + L_RST + (wm * 64 + i * 16 + r16) * 8);
        const float a = ms[1], b = -ms[0] * ms[1];
#pragma unroll
        for (int j = 0; j < 7; ++j)
#pragma unroll
          for (int e = 0; e < 4; ++e) acc[i][j][e] = __builtin_fmaxf(__builtin_fmaf(acc[i][j][e], a, b * cs[j][e]), 0.f);
        const int ro = (wm * 64 + i * 16 + r16) * 64 + cswz;
#pragma unroll
        for (int j = 0; j < 3; ++j) {
          f32x4 x = acc[i][2 * j], y = acc[i][2 * j + 1];
          swap_pair(x, y);
          const u32x4 o = pack_xy(x, y);
          const int sl = wn * 4 + j;
          st128(o, rws, hb + sl * 8192 + ro);
          if ((unsigned)(sl - par) < 6u) *(LAS u32x4*)(smem + (sl - par) * 8192 + ro) = o;
        }
        if (i & 1) {
          f32x4 x = acc[i - 1][6], y = acc[i][6];
          swap_pair(x, y);
          const u32x4 o = pack_xy(x, y);
          const int r3 = (wm * 64 + (i - 1 + (g & 1)) * 16 + r16) * 64 + (((2 * wn + (g >> 1)) ^ aswz(r16)) * 16);
          st128(o, rws, hb + 3 * 8192 + r3);
          *(LAS u32x4*)(smem + (3 - par) * 8192 + r3) = o;
        }
      }
      if (wid == 0) STAMP(2, 3);
    }
    relane();
    {
      const int row = tm * 128 + wm * 64 + r16, col = gp * 64 + wn * 32 + cpair;
      u32x4 res[4];
      f32x4 acc[4][4];
      mma_loop<128, 4, 28, 4, 4, true, true>(smem, acc, wm * 64, lane, [&](int j) { return (j >> 1) * 4 + wn * 2 + (j & 1); }, [&]() {
        wait_vm<0>();
        if (wid == 0) STAMP(2, 4);
#pragma unroll
        for (int i = 0; i < 4; ++i) res[i] = ld128(rws, WS_H + (tm * 32 + gp * 2 + wn) * 8192 + (wm * 64 + i * 16 + r16) * 64 + cswz, 0);
      });
      if (wid == 0) STAMP(3, 2);
      const int pb = WS_P4 + (tm * 16 + gp) * 16384 + wid * 4096 + lane * 16;
#pragma unroll
      for (int i = 0; i < 4; ++i) st128(pack_xy(acc[i][2], acc[i][3]), rws, pb + i * 1024);
      wait_vm<0>();
      if (lane == 0) set_flag(ctl, C_P4 + tm, gp * 4 + wid, tag);
      if (wid == 0) STAMP(3, 3);
      poll(ctl, C_P4 + tm, (gp ^ 8) * 4 + wid, 1, tag, lane);
      if (wid == 0) STAMP(3, 4);
      const int qb = WS_P4 + (tm * 16 + (gp ^ 8)) * 16384 + wid * 4096 + lane * 16;
      u32x4 t[4];
#pragma unroll
      for (int i = 0; i < 4; ++i) t[i] = ld128(rws, qb + i * 1024, 0);
#pragma unroll
      for (int i = 0; i < 4; ++i) {
        f32x4 x = acc[i][0], y = acc[i][1];
        add_bf8(x, y, t[i]);
        swap_pair(x, y);
        add_bf8(x, y, res[i]);
        *(u32x4*)(OUT + (size_t)(row + i * 16) * D_ + col) = pack_xy(x, y);
      }
      if (wid == 0) STAMP(3, 5);
    }
  }
#endif
}
#if defined(__HIP_DEVICE_COMPILE__)
#pragma clang attribute pop
#endif

extern "C" void kernel_launch(void* const* d_in, const int* in_sizes, int n_in, void* d_out, int out_size, void* d_ws, size_t ws_size,
                              hipStream_t stream) {
  (void)hipFuncSetAttribute((const void*)block_kernel, hipFuncAttributeMaxDynamicSharedMemorySize, LDS_BYTES);
  block_kernel<<<256, 512, LDS_BYTES, stream>>>((const u16*)d_in[0], (const u16*)d_in[1], (const u16*)d_in[2], (const u16*)d_in[3],
                                                (const u16*)d_in[5], (const u16*)d_in[6], (u16*)d_out, (char*)d_ws);
}
```

```cpp
#include <hip/hip_runtime.h>
#include <hip/hip_bf16.h>
#include <type_traits>

#define LAS __attribute__((address_space(3)))
#define GAS __attribute__((address_space(1)))
#define DI __device__ __forceinline__
typedef __bf16 bf16x8 __attribute__((ext_vector_type(8)));
typedef __bf16 bf16x2 __attribute__((ext_vector_type(2)));
typedef short s16x4 __attribute__((ext_vector_type(4)));
typedef short s16x8 __attribute__((ext_vector_type(8)));
typedef float f32x2 __attribute__((ext_vector_type(2)));
typedef float f32x4 __attribute__((ext_vector_type(4)));
typedef unsigned u32x2 __attribute__((ext_vector_type(2)));
typedef unsigned u32x4 __attribute__((ext_vector_type(4)));
typedef unsigned short u16;

constexpr int S_ = 2048, D_ = 1024, FF_ = 3584;
constexpr float LN_EPS = 1e-5f;

constexpr int C_EPOCH = 0, C_TMO = 1, C_ST = 2, C_CS1 = 18, C_CS3 = 34, C_KV = 50, C_O = 66, C_H = 82, C_H1 = 98, C_P4 = 114, C_LINES = 130;
constexpr unsigned WS_XST = 36864;
constexpr unsigned WS_CS1 = WS_XST + S_ * 8;
constexpr unsigned WS_CS3 = WS_CS1 + 16 * 16 * 192 * 4;
constexpr unsigned WS_HST = WS_CS3 + 16 * 16 * 224 * 4;
constexpr unsigned KVP_BYTES = 8192 + 512;
constexpr unsigned WS_KVP = WS_HST + S_ * 16 * 8;
#define BLOCKED 1
constexpr unsigned WS_O = WS_KVP + 256 * KVP_BYTES;
constexpr unsigned WS_H = WS_O + S_ * D_ * 2;
constexpr unsigned WS_H1 = WS_H + S_ * D_ * 2;
constexpr unsigned WS_P4 = WS_O, WS_END = WS_H1 + S_ * FF_ * 2;
static_assert(C_LINES * 256 <= (int)WS_XST && WS_O % 256 == 0 && WS_KVP % 256 == 0, "");

constexpr int L_WT = 120 * 1024;
constexpr int L_TAB = 149 * 1024;
constexpr int L_DUMMY = L_TAB;
constexpr int L_RST = L_TAB + 256;
constexpr int L_CS = L_RST + 1024;
constexpr int L_CSS = L_CS + 1024;
constexpr int LDS_BYTES = L_CSS + 8192;
static_assert(LDS_BYTES <= 160 * 1024, "");

#define STAMP(k, i) do {} while (0)
#define TS_DECL
#define TS(t)
#define TS_OUT(k)

#if defined(__HIP_DEVICE_COMPILE__)
#pragma clang attribute push(__attribute__((target("no-packed-fp32-ops"))), apply_to = function)
typedef __amdgpu_buffer_rsrc_t rsrc_t;
DI float u2f(unsigned u) { return __builtin_bit_cast(float, u); }
DI unsigned f2u(float f) { return __builtin_bit_cast(unsigned, f); }
DI bool wave_all(bool p) { return __builtin_amdgcn_ballot_w64(p) == ~0ull; }
#define CODE_AHEAD 36864
constexpr int SC1 = 16;
template <int N> DI void wait_vm() { asm volatile("s_waitcnt vmcnt(%0)" ::"n"(N) : "memory"); }
DI void wait_lgkm0() { asm volatile("s_waitcnt lgkmcnt(0)" ::: "memory"); }
DI void bar() { asm volatile("" ::: "memory"); __builtin_amdgcn_s_barrier(); asm volatile("" ::: "memory"); }
DI void lbar() { wait_lgkm0(); bar(); }
DI unsigned pk2(float a, float b) { f32x2 v = {a, b}; return __builtin_bit_cast(unsigned, __builtin_convertvector(v, bf16x2)); }
DI float bflo(unsigned u) { return u2f(u << 16); }
DI float bfhi(unsigned u) { return u2f(u & 0xffff0000u); }
DI bf16x8 ld_b128(LAS const char* p) { return *(LAS const bf16x8*)p; }
DI bf16x8 ld_tr2(LAS const char* p0, LAS const char* p1) {
  s16x4 a = __builtin_amdgcn_ds_read_tr16_b64_v4i16((LAS s16x4*)p0);
  s16x4 b = __builtin_amdgcn_ds_read_tr16_b64_v4i16((LAS s16x4*)p1);
  return __builtin_bit_cast(bf16x8, __builtin_shufflevector(a, b, 0, 1, 2, 3, 4, 5, 6, 7));
}
DI float x32_sum(float v) { auto r = __builtin_amdgcn_permlane32_swap(f2u(v), f2u(v), false, false); return u2f(r[0]) + u2f(r[1]); }
DI float x16_sum(float v) { auto r = __builtin_amdgcn_permlane16_swap(f2u(v), f2u(v), false, false); return u2f(r[0]) + u2f(r[1]); }
template <int CTRL> DI float dpp_add(float v) { return v + u2f(__builtin_amdgcn_update_dpp(0, (int)f2u(v), CTRL, 0xf, 0xf, true)); }
DI float half_sum(float v) {
  v = dpp_add<0xB1>(v);
  v = dpp_add<0x4E>(v);
  v = dpp_add<0x141>(v);
  v = dpp_add<0x140>(v);
  return x16_sum(v);
}
#define MFMA16(a, b, c) __builtin_amdgcn_mfma_f32_16x16x32_bf16((a), (b), (c), 0, 0, 0)
DI rsrc_t mkrsrc(const void* p, unsigned bytes) { return __builtin_amdgcn_make_buffer_rsrc((void*)p, 0, bytes, 0x00020000); }
DI void st128(u32x4 v, rsrc_t r, int off) { __builtin_amdgcn_raw_buffer_store_b128(v, r, off, 0, SC1); }
DI u32x4 ld128(rsrc_t r, int off, int soff) { return __builtin_amdgcn_raw_buffer_load_b128(r, off, soff, SC1); }

DI unsigned read_tag(const char* ws) {
  unsigned e;
  asm volatile("s_load_dword %0, %1, 0x0\n\ts_waitcnt lgkmcnt(0)" : "=s"(e) : "s"(ws) : "memory");
  return e + 1u;
}
DI void set_flag(GAS unsigned* ctl, int line, int word, unsigned tag) { __hip_atomic_store(ctl + line * 64 + word, tag, __ATOMIC_RELAXED, __HIP_MEMORY_SCOPE_AGENT); }
DI void acquire() {
  asm volatile("" ::: "memory");
}
DI void poll(GAS unsigned* ctl, int line, int lo, int n, unsigned tag, int lane) {
  GAS unsigned* p = ctl + line * 64 + lo + (lane < n ? lane : 0);
  unsigned spins = 0;
  while (!wave_all(__hip_atomic_load(p, __ATOMIC_RELAXED, __HIP_MEMORY_SCOPE_AGENT) == tag)) {
    __builtin_amdgcn_s_sleep(1);
    if (++spins > (1u << 17)) { if (lane == 0) __hip_atomic_store(ctl + C_TMO * 64, (unsigned)line, __ATOMIC_RELAXED, __HIP_MEMORY_SCOPE_AGENT); break; }
  }
  acquire();
}

template <int BN> DI constexpr int bswz(int k) {
  return BN == 224 ? (((k >> 3) & 1) << 1)
       : BN == 128 ? (((k & 3) | (((k >> 3) & 1) << 2)) << 1)
                   : ((((k >> 1) & 1) | (((k >> 3) & 1) << 1)) << 1);
}

DI constexpr int aswz(int r) { return (-(r >> 2)) & 3; }

template <int BN, int NS, int NT, bool AROW, int LAG = 0>
struct Loader {
  static constexpr int PA = 4, PB = BN / 32, PW = PA + PB, BST = BN * 128, CPR = BN / 8;
  static_assert(NS * (16384 + BST) <= L_TAB, "");
  LAS char* dst0; rsrc_t ra, rb; int va[PA], vb[PB], vh[2]; int ldb64, own0, tn;
  static constexpr int NOWN = BN == 128 ? 3 : BN == 224 ? 1 : 0;
  int kh;
  DI int ktile(int t) const {
    if (AROW) return (t + own0) & (NT - 1);
    const int u = (BN == 128 && t >= 28) ? t - 3 : t, k = u < NOWN ? own0 + u : (u - NOWN < own0 ? u - NOWN : u);
    return BN == 128 ? k + ((t >= 28) != (kh != 0) ? 28 : 0) : k;
  }
  DI void issueA(int t) {
    const int kk = ktile(t), slot = t % NS;
#pragma unroll
    for (int i = 0; i < PA; ++i)
      __builtin_amdgcn_raw_ptr_buffer_load_lds(ra, dst0 + slot * 16384 + i * 4096, 16, va[i], kk * (AROW ? 128 : 16384), 0, AROW ? 0 : SC1);
  }
  DI void issueB(int t) {
    const int kk = ktile(t), slot = t % NS;
#pragma unroll
    for (int i = 0; i < PB; ++i) __builtin_amdgcn_raw_ptr_buffer_load_lds(rb, dst0 + NS * 16384 + slot * BST + i * 4096, 16, vb[i], kk * ldb64, 0, 0);
  }
  DI void issueH(int t) {
    issueA(t);
    const int kk = ktile(t), slot = t % NS;
#pragma unroll
    for (int i = 0; i < 2; ++i) __builtin_amdgcn_raw_ptr_buffer_load_lds(rb, dst0 + NS * 16384 + slot * BST + i * 4096, 16, vh[i], kk * ldb64, 0, 0);
  }
  DI void init(LAS char* smem, rsrc_t ra_, rsrc_t rb_, int a_off, int lda_b, int b_off, int ldb_b, int segb, int lw, int lane, int own) {
    dst0 = smem + lw * 1024; ra = ra_; rb = rb_; ldb64 = 64 * ldb_b; own0 = own; tn = NS - 1;
#pragma unroll
    for (int i = 0; i < PA; ++i) {
      const int ins = lw + i * 4, r = ins * 8 + (lane >> 3), c = (lane & 7) ^ ((r >> 1) & 7);
      va[i] = AROW ? a_off + r * lda_b + c * 16 : a_off + ins * 1024 + lane * 16;
    }
#pragma unroll
    for (int i = 0; i < PB; ++i) {
      const int ch = (lw + i * 4) * 64 + lane, k = ch / CPR, c = (ch % CPR) ^ bswz<BN>(k);
      vb[i] = b_off + k * ldb_b + (c >> 3) * segb + (c & 7) * 16;
    }
  }
  DI void init_half(int b_off, int ldb_b, int lw, int lane) {
#pragma unroll
    for (int i = 0; i < 2; ++i) { const int k = (lw + i * 4) * 8 + (lane >> 3); vh[i] = b_off + k * ldb_b + (((lane & 7) ^ bswz<64>(k)) * 16); }
  }
  template <int ALLOW = (NS - 2 - LAG) * PW> DI void step() { wait_vm<ALLOW>(); lbar(); issueA(tn); issueB(tn); ++tn; }
  DI void steps(int n) {
#pragma unroll 1
    for (int t = 0; t < n; ++t) step();
  }
  DI void finish() {
    if constexpr (NS - LAG >= 5) { wait_vm<3 * PW>(); lbar(); }
    if constexpr (NS - LAG >= 4) { wait_vm<2 * PW>(); lbar(); }
    if constexpr (NS - LAG >= 3) { wait_vm<1 * PW>(); lbar(); }
    wait_vm<0>(); lbar();
  }
};
template <int BN, int I0, int I1> DI void load_wtile(LAS char* dst, rsrc_t r, int off, int ldb_b, int segb, int lw, int lane) {
#pragma unroll
  for (int i = I0; i < I1; ++i) {
    const int ch = (lw + i * 4) * 64 + lane, k = ch / (BN / 8), c = (ch % (BN / 8)) ^ bswz<BN>(k);
    __builtin_amdgcn_raw_ptr_buffer_load_lds(r, dst + (lw + i * 4) * 1024, 16, off + k * ldb_b + (c >> 3) * segb + (c & 7) * 16, 0, 0, 0);
  }
}
DI int opaque(int v) { asm volatile("" : "+v"(v)); return v; }

DI void touch_lines(LAS char* smem, rsrc_t r, int base, int line, int nlines) {
  __builtin_amdgcn_raw_ptr_buffer_load_lds(r, smem + L_DUMMY, 4, line < nlines ? base + line * 128 : (int)0x80000000, 0, 0, 0);
}

template <int BN> DI void colsum_tile(LAS char* img, rsrc_t rws, unsigned off, int lw, int lane) {
  constexpr int RS = BN * 2;
  const int r16 = lane & 15, g = lane >> 4, q = (lane & 15) >> 2, p = lane & 3;
  const s16x8 one_ = {0x3f80, 0x3f80, 0x3f80, 0x3f80, 0x3f80, 0x3f80, 0x3f80, 0x3f80};
  LAS const char* rd[4];
#pragma unroll
  for (int i = 0; i < 4; ++i) {
    const int nt = lw + 4 * i < BN / 16 ? lw + 4 * i : lw;
    rd[i] = img + (8 * g + q) * RS + (((2 * nt + (p >> 1)) ^ bswz<BN>(8 * g + q)) * 16) + 8 * (p & 1);
  }
  s16x4 v[4][4];
#define TR4(a, b, c, d, r) "ds_read_b64_tr_b16 %" #a ", %" #r "\n\tds_read_b64_tr_b16 %" #b ", %" #r " offset:%c20\n\t" \
                           "ds_read_b64_tr_b16 %" #c ", %" #r " offset:%c21\n\tds_read_b64_tr_b16 %" #d ", %" #r " offset:%c22\n\t"
  asm volatile(TR4(0, 1, 2, 3, 16) TR4(4, 5, 6, 7, 17) TR4(8, 9, 10, 11, 18) TR4(12, 13, 14, 15, 19) "s_waitcnt lgkmcnt(0)"
               : "=&v"(v[0][0]), "=&v"(v[0][1]), "=&v"(v[0][2]), "=&v"(v[0][3]), "=&v"(v[1][0]), "=&v"(v[1][1]), "=&v"(v[1][2]), "=&v"(v[1][3]),
                 "=&v"(v[2][0]), "=&v"(v[2][1]), "=&v"(v[2][2]), "=&v"(v[2][3]), "=&v"(v[3][0]), "=&v"(v[3][1]), "=&v"(v[3][2]), "=&v"(v[3][3])
               : "v"(rd[0]), "v"(rd[1]), "v"(rd[2]), "v"(rd[3]), "i"(4 * RS), "i"(32 * RS), "i"(36 * RS) : "memory");
#undef TR4
  f32x4 c[4];
#pragma unroll
  for (int i = 0; i < 4; ++i) {
    c[i] = (f32x4){0.f, 0.f, 0.f, 0.f};
    c[i] = MFMA16(__builtin_bit_cast(bf16x8, __builtin_shufflevector(v[i][0], v[i][1], 0, 1, 2, 3, 4, 5, 6, 7)), __builtin_bit_cast(bf16x8, one_), c[i]);
    c[i] = MFMA16(__builtin_bit_cast(bf16x8, __builtin_shufflevector(v[i][2], v[i][3], 0, 1, 2, 3, 4, 5, 6, 7)), __builtin_bit_cast(bf16x8, one_), c[i]);
  }
  const f32x4 out = r16 == 0 ? c[0] : r16 == 1 ? c[1] : r16 == 2 ? c[2] : c[3];
  st128(__builtin_bit_cast(u32x4, out), rws, lw + 4 * r16 < BN / 16 ? off + (lw + 4 * r16) * 64 + g * 16 : 0x80000000u);
}
DI f32x4 sum4(u32x4 (&t)[4]) {
  asm volatile("" : "+v"(t[0]), "+v"(t[1]), "+v"(t[2]), "+v"(t[3]));
  return (__builtin_bit_cast(f32x4, t[0]) + __builtin_bit_cast(f32x4, t[1])) + (__builtin_bit_cast(f32x4, t[2]) + __builtin_bit_cast(f32x4, t[3]));
}
DI f32x4 lds_sum4(LAS const char* p) {
  return (*(LAS const f32x4*)p + *(LAS const f32x4*)(p + 1024)) + (*(LAS const f32x4*)(p + 2048) + *(LAS const f32x4*)(p + 3072));
}
DI unsigned peek(GAS unsigned* ctl, int line, int lane, int n = 64) { return __hip_atomic_load(ctl + line * 64 + (lane & (n - 1)), __ATOMIC_RELAXED, __HIP_MEMORY_SCOPE_AGENT); }
DI void confirm(unsigned seen, GAS unsigned* ctl, int line, unsigned tag, int lane, int n = 64) {
  asm volatile("" : "+v"(seen));
  if (!wave_all(seen == tag)) poll(ctl, line, 0, n, tag, lane);
  else acquire();
}

DI void mfma16(f32x4& c, const bf16x8& a, const bf16x8& b) { asm volatile("v_mfma_f32_16x16x32_bf16 %0, %1, %2, %0" : "+v"(c) : "v"(a), "v"(b)); }
DI void mfma_drain() { asm volatile("s_nop 7\n\ts_nop 7" ::: "memory"); }
template <int TM, int TN> struct Frag { s16x4 b[TN][2]; bf16x8 a[TM]; };
template <int BN, int NS, int NT, int TM, int TN, bool DEEP, bool OWN, class NMap, class Mid, class Send>
DI void mma_loop(LAS char* smem, f32x4 (&acc)[TM][TN], int row0, int lane, NMap&& nt, Mid&& mid, Send&& send) {
  constexpr int RS = BN * 2, BST = BN * 128, NL = TM + 2 * TN, NM = TM * TN;
  typedef Frag<TM, TN> F;
  const int r16 = lane & 15, g = lane >> 4, q = (lane & 15) >> 2, p = lane & 3;
  constexpr bool AROW = BN == 192;
  const int aoff = AROW ? (row0 + r16) * 128 + ((g ^ (r16 >> 1)) * 16) : (row0 + r16) * 64 + ((g ^ aswz(r16)) * 16);
  const int aoff1 = AROW ? aoff ^ 64 : aoff + 8192;
  int boff[TN];
#pragma unroll
  for (int j = 0; j < TN; ++j) boff[j] = NS * 16384 + (8 * g + q) * RS + (((2 * nt(j) + (p >> 1)) ^ bswz<BN>(8 * g + q)) * 16) + 8 * (p & 1);
#pragma unroll
  for (int i = 0; i < TM; ++i)
#pragma unroll
    for (int j = 0; j < TN; ++j) { acc[i][j] = (f32x4){0.f, 0.f, 0.f, 0.f}; asm volatile("" : "+v"(acc[i][j])); }
  int boffh[2];
  auto ld1 = [&](F& f, int slot, int s, int k, int tn = TN) {
    if (k == 0 || k > 2 * tn) { const int i = k == 0 ? 0 : k - 2 * tn; f.a[i] = ld_b128(smem + slot * 16384 + (s ? aoff1 : aoff) + i * (AROW ? 2048 : 1024)); }
    else {
      const int j = (k - 1) >> 1, u = (k - 1) & 1;
      f.b[j][u] = __builtin_amdgcn_ds_read_tr16_b64_v4i16((LAS s16x4*)(smem + slot * BST + (tn == TN ? boff[j] + (32 * s + 4 * u) * RS : boffh[j & 1] + (32 * s + 4 * u) * 128)));
    }
  };
  auto mm1 = [&](const F& f, int m, int tn = TN) {
    const int i = m / tn, j = m % tn;
    const bf16x8 b = __builtin_bit_cast(bf16x8, __builtin_shufflevector(f.b[j][0], f.b[j][1], 0, 1, 2, 3, 4, 5, 6, 7));
    if (AROW && j >= 4) mfma16(acc[i][j], f.a[i], b); else mfma16(acc[i][j], b, f.a[i]);
  };
  auto ldall = [&](F& f, int slot, int s) {
#pragma unroll
    for (int k = 0; k < NL; ++k) ld1(f, slot, s, k);
  };
  auto mmall = [&](const F& f) {
#pragma unroll
    for (int m = 0; m < NM; ++m) mm1(f, m);
  };
  auto sub = [&](const F& x, F& u, int slot, int s) {
    constexpr int SP = DEEP ? NM : (NL < NM ? NL : NM - 2);
#pragma unroll
    for (int m = 0; m < NM; ++m) {
      mm1(x, m);
#pragma unroll
      for (int k = (m * NL + SP - 1) / SP; k < NL && k < ((m + 1) * NL + SP - 1) / SP; ++k) ld1(u, slot, s, k);
      __builtin_amdgcn_sched_barrier(0);
    }
  };
  auto subh = [&](const F& x, F& u, int slot, int s, auto tx, auto tu) {
    constexpr int TX = decltype(tx)::value, TU = decltype(tu)::value, M = TM * TX, LN = TM + 2 * TU;
#pragma unroll
    for (int m = 0; m < M; ++m) {
      mm1(x, m, TX);
#pragma unroll
      for (int k = (m * LN + M - 1) / M; k < LN && k < ((m + 1) * LN + M - 1) / M; ++k) ld1(u, slot, s, k, TU);
      __builtin_amdgcn_sched_barrier(0);
    }
  };
  auto nxt = [](int s) { return s == NS - 1 ? 0 : s + 1; };
  lbar();
  if (threadIdx.x < 64) STAMP(BN == 192 ? 0 : BN == 64 ? 1 : BN == 224 ? 2 : 3, 1);
  TS_DECL; TS(0);
  int slot = 0;
  if constexpr (!DEEP) {
    F f0, f1;
    ldall(f0, 0, 0);
    if constexpr (OWN) {
      sub(f0, f1, 0, 1);
      mid();
      bar();
      mmall(f1);
      __builtin_amdgcn_sched_barrier(0);
      TS(32);
      lbar();
      TS(1);
      slot = 1;
      ldall(f0, 1, 0);
    }
#pragma unroll 1
    for (int t = OWN ? 1 : 0; t < NT - 1; ++t) {
      sub(f0, f1, slot, 1);
      TS(32 + t);
      lbar();
      TS(t + 1);
      slot = nxt(slot);
      sub(f1, f0, slot, 0);
    }
    sub(f0, f1, slot, 1);
    lbar();
    TS(NT); TS_OUT(BN == 192 ? 0 : BN == 64 ? 1 : BN == 224 ? 2 : 3);
    mmall(f1);
  } else {
    F f0, f1, g0, g1;
    auto tile = [&](const F& x0, const F& x1, F& u0, F& u1, int t) {
      TS(32 + t);
      bar();
      TS(t + 1);
      slot = nxt(slot);
      sub(x0, u0, slot, 0);
      sub(x1, u1, slot, 1);
    };
    ldall(f0, 0, 0); ldall(f1, 0, 1);
    int t0 = 0;
    if constexpr (OWN) {
      tile(f0, f1, g0, g1, 0);
      mid();
      tile(g0, g1, f0, f1, 1);
      mmall(f0); mmall(f1);
      __builtin_amdgcn_sched_barrier(0);
      TS(34);
      lbar();
      TS(3);
      slot = 3;
      ldall(g0, 3, 0); ldall(g1, 3, 1);
      tile(g0, g1, f0, f1, 3);
      t0 = 4;
    }
    if constexpr (!OWN) {
#pragma unroll 1
      for (int t = t0; t < NT - 2; t += 2) {
        tile(f0, f1, g0, g1, t);
        tile(g0, g1, f0, f1, t + 1);
      }
      tile(f0, f1, g0, g1, NT - 2);
      lbar();
      TS(NT); TS_OUT(BN == 192 ? 0 : BN == 64 ? 1 : BN == 224 ? 2 : 3);
      mmall(g0); mmall(g1);
    } else {
#pragma unroll 1
      for (int t = t0; t < 24; t += 2) {
        tile(f0, f1, g0, g1, t);
        tile(g0, g1, f0, f1, t + 1);
      }
      typedef std::integral_constant<int, TN> Full;
      typedef std::integral_constant<int, TN / 2> Half;
      auto tileh = [&](const F& x0, const F& x1, F& u0, F& u1, auto tx) {
        bar();
        slot = nxt(slot);
        subh(x0, u0, slot, 0, tx, Half{});
        subh(x1, u1, slot, 1, tx, Half{});
      };
#pragma unroll
      for (int j = 0; j < 2; ++j) {
        const int l2 = opaque(lane), k = 8 * (l2 >> 4) + ((l2 & 15) >> 2);
        boffh[j] = NS * 16384 + k * 128 + (((2 * nt(j) + ((l2 & 3) >> 1)) ^ bswz<64>(k)) * 16) + 8 * (l2 & 1);
      }
      tileh(f0, f1, g0, g1, Full{});
      mfma_drain();
#pragma unroll
      for (int i = 0; i < TM; ++i)
#pragma unroll
        for (int j = TN / 2; j < TN; ++j) asm volatile("" : "+v"(acc[i][j]));
      send(0);
      __builtin_amdgcn_sched_barrier(0);
      tileh(g0, g1, f0, f1, Half{});
      tileh(f0, f1, g0, g1, Half{});
      send(1);
      tileh(g0, g1, f0, f1, Half{});
      tileh(f0, f1, g0, g1, Half{});
      send(2);
      tileh(g0, g1, f0, f1, Half{});
      send(3);
      lbar();
      TS(28); TS_OUT(3);
#pragma unroll
      for (int m = 0; m < TM * TN / 2; ++m) mm1(f0, m, TN / 2);
#pragma unroll
      for (int m = 0; m < TM * TN / 2; ++m) mm1(f1, m, TN / 2);
    }
  }
  mfma_drain();
#pragma unroll
  for (int i = 0; i < TM; ++i)
#pragma unroll
    for (int j = 0; j < TN; ++j) asm volatile("" : "+v"(acc[i][j]));
}
DI void swap_pair(f32x4& x, f32x4& y) {
#pragma unroll
  for (int e = 0; e < 4; ++e) {
    auto r = __builtin_amdgcn_permlane16_swap(f2u(x[e]), f2u(y[e]), false, false);
    x[e] = u2f(r[0]); y[e] = u2f(r[1]);
  }
}
DI void add_bf8(f32x4& x, f32x4& y, const u32x4& rv) {
  x[0] += bflo(rv[0]); x[1] += bfhi(rv[0]); x[2] += bflo(rv[1]); x[3] += bfhi(rv[1]);
  y[0] += bflo(rv[2]); y[1] += bfhi(rv[2]); y[2] += bflo(rv[3]); y[3] += bfhi(rv[3]);
}
DI u32x4 pack_xy(const f32x4& x, const f32x4& y) { return (u32x4){pk2(x[0], x[1]), pk2(x[2], x[3]), pk2(y[0], y[1]), pk2(y[2], y[3])}; }

template <class Op> DI void red2(float& a, float& b, Op&& op) {
  auto r = __builtin_amdgcn_permlane16_swap(f2u(a), f2u(b), false, false);
  const float c = op(u2f(r[0]), u2f(r[1]));
  auto t = __builtin_amdgcn_permlane32_swap(f2u(c), f2u(c), false, false);
  const float d = op(u2f(t[0]), u2f(t[1]));
  auto u = __builtin_amdgcn_permlane16_swap(f2u(d), f2u(d), false, false);
  a = u2f(u[0]); b = u2f(u[1]);
}
DI void dual_softmax(const f32x4 (&p)[2][2], const float (&sc)[2], f32x4 (&e)[2][2][2]) {
  float mx[2], mn[2], s1[2], s2[2];
#pragma unroll
  for (int t = 0; t < 2; ++t) {
    mx[t] = p[t][0][0]; mn[t] = p[t][0][0];
#pragma unroll
    for (int r = 1; r < 8; ++r) { mx[t] = __builtin_fmaxf(mx[t], p[t][r >> 2][r & 3]); mn[t] = __builtin_fminf(mn[t], p[t][r >> 2][r & 3]); }
  }
  red2(mx[0], mx[1], [](float x, float y) { return __builtin_fmaxf(x, y); });
  red2(mn[0], mn[1], [](float x, float y) { return __builtin_fminf(x, y); });
#pragma unroll
  for (int t = 0; t < 2; ++t) {
    const float a = -mx[t] * sc[t], b = mn[t] * sc[t];
    s1[t] = 0.f; s2[t] = 0.f;
#pragma unroll
    for (int r = 0; r < 8; ++r) {
      const float x = __builtin_amdgcn_exp2f(__builtin_fmaf(p[t][r >> 2][r & 3], sc[t], a));
      const float y = __builtin_amdgcn_exp2f(__builtin_fmaf(p[t][r >> 2][r & 3], -sc[t], b));
      e[0][t][r >> 2][r & 3] = x; e[1][t][r >> 2][r & 3] = y;
      s1[t] += x; s2[t] += y;
    }
  }
  red2(s1[0], s1[1], [](float x, float y) { return x + y; });
  red2(s2[0], s2[1], [](float x, float y) { return x + y; });
#pragma unroll
  for (int t = 0; t < 2; ++t) {
    const float r1 = __builtin_amdgcn_rcpf(s1[t]), r2 = __builtin_amdgcn_rcpf(s2[t]);
#pragma unroll
    for (int r = 0; r < 8; ++r) { e[0][t][r >> 2][r & 3] *= r1; e[1][t][r >> 2][r & 3] *= r2; }
  }
}
DI bf16x8 pack_frag(const f32x4& x, const f32x4& y) { return __builtin_bit_cast(bf16x8, pack_xy(x, y)); }
DI void st4(LAS char* p, const f32x4& a, const f32x4& b, const f32x4& c, const f32x4& d) {
  *(LAS f32x4*)p = a; *(LAS f32x4*)(p + 1024) = b; *(LAS f32x4*)(p + 2048) = c; *(LAS f32x4*)(p + 3072) = d;
}
DI void ld4(LAS const char* p, f32x4 (&v)[2][2]) {
  v[0][0] = *(LAS const f32x4*)p; v[0][1] = *(LAS const f32x4*)(p + 1024); v[1][0] = *(LAS const f32x4*)(p + 2048); v[1][1] = *(LAS const f32x4*)(p + 3072);
}
#endif

__global__ __launch_bounds__(512, 2) void block_kernel(const u16* __restrict__ X, const u16* __restrict__ WQKV, const u16* __restrict__ WPHI,
                                                        const u16* __restrict__ WO, const u16* __restrict__ W1, const u16* __restrict__ W2,
                                                        u16* __restrict__ OUT, char* WS) {
#if defined(__HIP_DEVICE_COMPILE__)
  extern __shared__ __attribute__((aligned(16))) char smem_[];
  LAS char* smem = (LAS char*)smem_;
  const int tid = threadIdx.x, wid = __builtin_amdgcn_readfirstlane(tid >> 6);
  int lane = tid & 63;
  const int bid = blockIdx.x, xcd = bid & 7, loc = bid >> 3;
  const int tm = (xcd & 1) * 8 + (loc & 7), gp = (xcd >> 1) * 4 + (loc >> 3);
  const bool loader = wid >= 4;
  const int lw = wid & 3;
  GAS unsigned* ctl = (GAS unsigned*)WS;
  const rsrc_t rws = mkrsrc(WS, WS_END);
  if (wid == 0) STAMP(0, 0);
  unsigned tag = 0;

  int r16, g, cpair, cswz;
  auto relane = [&]() {
    lane = opaque(lane);
    r16 = lane & 15; g = lane >> 4;
    cpair = (g & 1) * 16 + (g >> 1) * 8;
    cswz = ((cpair >> 3) ^ aswz(r16)) * 16;
  };
  {
    constexpr int NT = 16, NS = 3;
    constexpr int L_PHI = 0;
    constexpr int L_XV = 2 * 17408;
    constexpr int L_VF = L_XV + 16384;
    constexpr int L_XQ = L_VF + 8192;
    constexpr int L_KVT = L_XQ;
    constexpr int L_WF = L_XQ + 16384;
    static_assert(L_WF + 4096 <= 5 * 16384, "phase 2's B ring is filled during this epilogue");
    relane();
    const int wm = (wid >> 1) & 1, hh = wid & 1, mi = wid >> 2;
    const int head = gp * 2 + hh;
    const unsigned kvp_grp = WS_KVP + gp * 16 * KVP_BYTES;
    LAS char* const xk = smem + L_PHI + hh * 17408 + (wm * 64 + 32) * 136 + lane * 16;
    bf16x8 wf[2];
    f32x4 fq[2][2], fk[2][2], fv[2][2];
    if (loader) {
      const rsrc_t rb = mkrsrc(WQKV, D_ * 3 * D_ * 2), ro = mkrsrc(WO, D_ * D_ * 2);
      Loader<192, NS, NT, true> L;
      const rsrc_t rx = mkrsrc(X, S_ * D_ * 2);
      L.init(smem, rx, rb, tm * 128 * D_ * 2, D_ * 2, gp * 128, 3 * D_ * 2, 2048, lw, lane, (tm & 3) * 4);
      L.issueA(0); L.issueB(0); L.issueA(1); L.issueB(1);
      if (lw == 0) {
        unsigned long long pc;
        asm volatile("s_getpc_b64 %0" : "=s"(pc));
        touch_lines(smem, mkrsrc((const void*)(pc & ~127ull), CODE_AHEAD), (loc * 16) * 128, lane < 16 ? lane : 4096, 16);
      }
      tag = read_tag(WS);
      __builtin_amdgcn_s_setprio(3);
      if (wid == 4) STAMP(4, 1);
      auto cs1 = [&](int t) { if ((tm >> 2) == t) colsum_tile<192>(smem + NS * 16384 + (t % NS) * 24576, rws, WS_CS1 + (gp * 16 + tm) * 768, lw, lane); };
      L.step();
      cs1(0);
      const int xrow = tm * 128 + gp * 8 + lw * 2 + (lane >> 5);
      u32x4 xv[4];
#pragma unroll
      for (int j = 0; j < 4; ++j) xv[j] = __builtin_amdgcn_raw_buffer_load_b128(rx, xrow * (D_ * 2) + (lane & 31) * 16, j * 512, 0);
      L.step<14>();
      cs1(1);
      L.step<14>();
      cs1(2);
      L.step();
      cs1(3);
      {
        asm volatile("" : "+v"(xv[0]), "+v"(xv[1]), "+v"(xv[2]), "+v"(xv[3]));
        float f[32], sum = 0.f, qq = 0.f;
#pragma unroll
        for (int i = 0; i < 16; ++i) { f[2 * i] = bflo(xv[i >> 2][i & 3]); f[2 * i + 1] = bfhi(xv[i >> 2][i & 3]); sum += f[2 * i] + f[2 * i + 1]; }
        const float mean = half_sum(sum) * (1.0f / D_);
#pragma unroll
        for (int i = 0; i < 32; ++i) { const float d = f[i] - mean; qq = __builtin_fmaf(d, d, qq); }
        const float rstd = 1.0f / __builtin_sqrtf(half_sum(qq) * (1.0f / D_) + LN_EPS);
        __builtin_amdgcn_raw_buffer_store_b64(__builtin_bit_cast(u32x2, (f32x2){mean, rstd}), rws, (lane & 31) == 0 ? WS_XST + xrow * 8 : 0x80000000u, 0, SC1);
      }
      L.step();
      L.step<0>();
      if (lane == 0) { set_flag(ctl, C_CS1 + gp, tm * 4 + lw, tag); set_flag(ctl, C_ST + tm, gp * 4 + lw, tag); }
      touch_lines(smem, ro, bid * 8192, lane < 16 ? lw * 16 + lane : 4096, 64);
      L.step();
      L.step<13>();
      L.steps(3);
      const unsigned seen = peek(ctl, C_CS1 + gp, lane), seen2 = peek(ctl, C_ST + tm, lane);
      L.steps(2);
      confirm(seen, ctl, C_CS1 + gp, tag, lane);
      confirm(seen2, ctl, C_ST + tm, tag, lane);
      u32x4 gt[4];
#pragma unroll
      for (int j = 0; j < 4; ++j) gt[j] = ld128(rws, WS_CS1 + (gp * 16 + lw * 4) * 768 + lane * 16, j * 768);
      u32x4 xs = ld128(rws, WS_XST + tm * 1024 + lane * 16, 0);
      L.step();
      wait_vm<10>(); lbar();
      *(LAS f32x4*)(smem + L_CSS + lw * 1024 + lane * 16) = sum4(gt);
      asm volatile("" : "+v"(xs));
      if (lw == 1) *(LAS u32x4*)(smem + L_RST + lane * 16) = xs;
      wait_vm<0>(); lbar();
      if (lw == 0) *(LAS f32x4*)(smem + L_CS + lane * 16) = lds_sum4(smem + L_CSS + lane * 16);
      const rsrc_t r1 = mkrsrc(W1, D_ * FF_ * 2);
      load_wtile<224, 0, 4>(smem + L_WT, r1, tm * 64 * (FF_ * 2) + gp * 448, FF_ * 2, 128, lw, lane);
      lbar();
      bar();
      __builtin_amdgcn_s_setprio(1);
      ld4(xk, fk);
      wf[0] = ld_b128(smem + L_WF + hh * 2048 + lane * 16);
      wf[1] = ld_b128(smem + L_WF + hh * 2048 + 1024 + lane * 16);
      ld4(smem + L_XV + lw * 4096 + lane * 16, fv);
    } else {
      {
#pragma unroll
        for (int t = 0; t < 2; ++t) {
          s16x8 w;
#pragma unroll
          for (int j = 0; j < 8; ++j) w[j] = (short)WPHI[head * 1024 + (16 * (j >> 2) + 4 * g + (j & 3)) * 32 + 16 * t + r16];
          wf[t] = __builtin_bit_cast(bf16x8, w);
        }
        {
          const int id = loc * 4 + wid;
          const unsigned blk = tm * 16 + gp;
          const unsigned o0 = wid == 0 ? WS_O + blk * 16384u : wid == 1 ? WS_H + blk * 16384u : wid == 2 ? WS_KVP + (gp * 16 + tm) * KVP_BYTES : WS_HST + blk * 1024u;
          const char* a0 = WS + o0;
          const char* a1 = (const char*)W1 + (size_t)id * 57344;
          const char* a2 = (const char*)W2 + (size_t)id * 57344;
          const char* a3 = (const char*)OUT + (size_t)id * 32768;
          const char* a4 = (const char*)WO + (size_t)id * 16384;
          const char* a5 = WS + (WS_H1 + blk * 57344u + wid * 16384u);
          unsigned d;
          asm volatile("s_load_dword %0, %1, 0x0\n\ts_load_dword %0, %2, 0x0\n\ts_load_dword %0, %3, 0x0\n\ts_load_dword %0, %4, 0x0\n\t"
                       "s_load_dword %0, %5, 0x0\n\ts_load_dword %0, %6, 0x0\n\ts_waitcnt lgkmcnt(0)"
                       : "=&s"(d) : "s"(a0), "s"(a1), "s"(a2), "s"(a3), "s"(a4), "s"(a5) : "memory");
        }
        tag = read_tag(WS);
      }
      if (wid == 0) STAMP(4, 0);
      f32x4 acc[4][6];
      mma_loop<192, NS, NT, 4, 6, false, false>(smem, acc, wm * 64, lane, [&](int j) { return (j >> 1) * 4 + hh * 2 + (j & 1); }, []() {}, [](int) {});
      if (wid == 0) STAMP(0, 2);
      if (wm == 0) {
        *(LAS bf16x8*)(smem + L_WF + hh * 2048 + lane * 16) = wf[0];
        *(LAS bf16x8*)(smem + L_WF + hh * 2048 + 1024 + lane * 16) = wf[1];
      }
      st4(xk, acc[2][2], acc[2][3], acc[3][2], acc[3][3]);
      st4(smem + L_XV + wid * 4096 + lane * 16, acc[2][4], acc[2][5], acc[3][4], acc[3][5]);
      lbar();
      if (wid == 0) STAMP(4, 4);
      st4(smem + L_XQ + wid * 4096 + lane * 16, acc[2][0], acc[2][1], acc[3][0], acc[3][1]);
#pragma unroll
      for (int t = 0; t < 2; ++t)
#pragma unroll
        for (int u = 0; u < 2; ++u) { fq[t][u] = acc[t][u]; fk[t][u] = acc[t][2 + u]; fv[t][u] = acc[t][4 + u]; }
    }
    const int rbase = wm * 64 + mi * 32, q = r16 >> 2, p = lane & 3;
    LAS const float* cs = (LAS const float*)(smem + L_CS) + hh * 32;
    LAS const f32x2* rst = (LAS const f32x2*)(smem + L_RST) + rbase;
    auto feature = [&](const f32x4 (&x)[2][2], int part, f32x4 (&e)[2][2][2]) {
      const f32x4 c0 = *(LAS const f32x4*)(cs + part * 64 + 4 * g), c1 = *(LAS const f32x4*)(cs + part * 64 + 16 + 4 * g);
      f32x4 pp[2][2];
      float sc[2];
#pragma unroll
      for (int t = 0; t < 2; ++t) {
        const f32x2 ms = rst[t * 16 + r16];
        sc[t] = ms[1] * 1.4426950408889634f;
        f32x4 y0, y1;
#pragma unroll
        for (int e_ = 0; e_ < 4; ++e_) { y0[e_] = __builtin_fmaf(-ms[0], c0[e_], x[t][0][e_]); y1[e_] = __builtin_fmaf(-ms[0], c1[e_], x[t][1][e_]); }
        const bf16x8 yb = pack_frag(y0, y1);
        pp[t][0] = MFMA16(wf[0], yb, ((f32x4){0.f, 0.f, 0.f, 0.f}));
        pp[t][1] = MFMA16(wf[1], yb, ((f32x4){0.f, 0.f, 0.f, 0.f}));
      }
      dual_softmax(pp, sc, e);
    };
    if (wid == 0) STAMP(4, 5);
    {
      const float cv0 = cs[128 + r16], cv1 = cs[144 + r16];
#pragma unroll
      for (int t = 0; t < 2; ++t) {
        const f32x4 s01 = *(LAS const f32x4*)(rst + t * 16 + 4 * g), s23 = *(LAS const f32x4*)(rst + t * 16 + 4 * g + 2);
        const float mv[4] = {s01[0], s01[2], s23[0], s23[2]}, rv[4] = {s01[1], s01[3], s23[1], s23[3]};
#pragma unroll
        for (int e_ = 0; e_ < 4; ++e_) {
          fv[t][0][e_] = __builtin_fmaf(-mv[e_], cv0, fv[t][0][e_]) * rv[e_];
          fv[t][1][e_] = __builtin_fmaf(-mv[e_], cv1, fv[t][1][e_]) * rv[e_];
        }
      }
      LAS char* vd = smem + (loader ? L_XV + lw * 4096 : L_VF + lw * 2048) + lane * 16;
      *(LAS bf16x8*)vd = pack_frag(fv[0][0], fv[1][0]);
      *(LAS bf16x8*)(vd + 1024) = pack_frag(fv[0][1], fv[1][1]);
      f32x4 e[2][2][2];
      feature(fk, 1, e);
      LAS char* wr = smem + L_PHI + hh * 17408 + (rbase + r16) * 136 + g * 8;
#pragma unroll
      for (int w = 0; w < 2; ++w)
#pragma unroll
        for (int t = 0; t < 2; ++t)
#pragma unroll
          for (int u = 0; u < 2; ++u)
            *(LAS u32x2*)(wr + t * 16 * 136 + w * 64 + u * 32) = (u32x2){pk2(e[w][t][u][0], e[w][t][u][1]), pk2(e[w][t][u][2], e[w][t][u][3])};
    }
    if (wid == 0) STAMP(4, 7);
    if (wid == 4) STAMP(4, 9);
    lbar();
    __builtin_amdgcn_s_setprio(0);
    if (wid == 0) STAMP(4, 8);
    bf16x8 pf[2][2];
    auto qpath = [&]() {
      f32x4 e[2][2][2];
      feature(fq, 0, e);
#pragma unroll
      for (int t = 0; t < 2; ++t)
#pragma unroll
        for (int w = 0; w < 2; ++w) { pf[t][w] = pack_frag(e[w][t][0], e[w][t][1]); asm volatile("" : "+v"(pf[t][w])); }
    };
    if (loader) {
      ld4(smem + L_XQ + lw * 4096 + lane * 16, fq);
      qpath();
      load_wtile<224, 4, 7>(smem + L_WT, mkrsrc(W1, D_ * FF_ * 2), tm * 64 * (FF_ * 2) + gp * 448, FF_ * 2, 128, lw, lane);
    } else {
      f32x4 k00 = {0.f, 0.f, 0.f, 0.f}, k01 = k00, k10 = k00, k11 = k00, z0 = k00, z1 = k00;
      const s16x8 one_ = {0x3f80, 0x3f80, 0x3f80, 0x3f80, 0x3f80, 0x3f80, 0x3f80, 0x3f80};
      LAS const char* rd = smem + L_PHI + hh * 17408 + (4 * g + q) * 136 + wm * 64 + p * 8;
#pragma unroll
      for (int u = 0; u < 4; ++u) {
        const bf16x8 pa0 = ld_tr2(rd + 32 * u * 136, rd + (32 * u + 16) * 136), pa1 = ld_tr2(rd + 32 * u * 136 + 32, rd + (32 * u + 16) * 136 + 32);
        LAS const char* vp = smem + ((u & 1) ? L_XV + ((u >> 1) * 2 + hh) * 4096 : L_VF + ((u >> 1) * 2 + hh) * 2048) + lane * 16;
        const bf16x8 v0 = ld_b128(vp), v1 = ld_b128(vp + 1024);
        k00 = MFMA16(pa0, v0, k00); k10 = MFMA16(pa1, v0, k10);
        k01 = MFMA16(pa0, v1, k01); k11 = MFMA16(pa1, v1, k11);
        z0 = MFMA16(pa0, __builtin_bit_cast(bf16x8, one_), z0); z1 = MFMA16(pa1, __builtin_bit_cast(bf16x8, one_), z1);
      }
      const int kb = kvp_grp + tm * KVP_BYTES;
      st128(pack_xy(k00, k10), rws, kb + hh * 4096 + wm * 2048 + lane * 16);
      st128(pack_xy(k01, k11), rws, kb + hh * 4096 + wm * 2048 + 1024 + lane * 16);
      st128(__builtin_bit_cast(u32x4, r16 == 0 ? z0 : z1), rws, r16 < 2 ? kb + 8192 + hh * 256 + wm * 128 + g * 32 + r16 * 16 : (int)0x80000000);
      if (wid == 0) STAMP(0, 3);
      wait_vm<0>();
    }
    lbar();
    if (tid == 0) set_flag(ctl, C_KV + gp, tm, tag);
    if (wid == 0) STAMP(0, 4);
    if (loader) {
      Loader<64, 5, 16, false, 1> L2;
      L2.init(smem, rws, mkrsrc(WO, D_ * D_ * 2), WS_O + tm * 128 * D_ * 2, 0, gp * 128, D_ * 2, 128, lw, lane, 0);
      L2.issueB(0); L2.issueB(1); L2.issueB(2); L2.issueB(3); L2.issueB(4);
    } else {
      qpath();
    }
    if (wid == 0) {
      STAMP(0, 5);
      poll(ctl, C_KV + gp, 0, 16, tag, lane);
    }
    bar();
    if (wid == 0) STAMP(0, 6);
    {
      u32x4 tz[16];
      if (wid == 7) {
#pragma unroll
        for (int j = 0; j < 16; ++j) tz[j] = ld128(rws, lane < 32 ? kvp_grp + 8192 + lane * 16 : 0x80000000u, j * KVP_BYTES);
      }
      u32x4 t[16];
#pragma unroll
      for (int j = 0; j < 16; ++j) t[j] = ld128(rws, kvp_grp + tid * 16, j * KVP_BYTES);
      if (wid == 7) {
        f32x4 z = __builtin_bit_cast(f32x4, tz[0]);
#pragma unroll
        for (int j = 1; j < 16; ++j) z += __builtin_bit_cast(f32x4, tz[j]);
        if (lane < 32) *(LAS u32x2*)(smem + L_KVT + 8192 + lane * 8) = (u32x2){pk2(z[0], z[1]), pk2(z[2], z[3])};
      }
      f32x4 x = {0.f, 0.f, 0.f, 0.f}, y = x;
#pragma unroll
      for (int j = 0; j < 16; ++j) add_bf8(x, y, t[j]);
      *(LAS u32x4*)(smem + L_KVT + tid * 16) = pack_xy(x, y);
    }
    lbar();
    if (wid == 0) STAMP(0, 7);
    {
      bf16x8 ka[2][2], za[2];
#pragma unroll
      for (int w = 0; w < 2; ++w) {
        ka[w][0] = ld_b128(smem + L_KVT + hh * 4096 + w * 2048 + lane * 16);
        ka[w][1] = ld_b128(smem + L_KVT + hh * 4096 + w * 2048 + 1024 + lane * 16);
        za[w] = ld_b128(smem + L_KVT + 8192 + hh * 128 + w * 64 + g * 16);
      }
#pragma unroll
      for (int t = 0; t < 2; ++t) {
        f32x4 o0 = {0.f, 0.f, 0.f, 0.f}, o1 = o0, dn = o0;
#pragma unroll
        for (int w = 0; w < 2; ++w) { dn = MFMA16(za[w], pf[t][w], dn); o0 = MFMA16(ka[w][0], pf[t][w], o0); o1 = MFMA16(ka[w][1], pf[t][w], o1); }
        const float rd = 1.0f / (dn[0] + 1e-6f);
#pragma unroll
        for (int e_ = 0; e_ < 4; ++e_) { o0[e_] *= rd; o1[e_] *= rd; }
        swap_pair(o0, o1);
        st128(pack_xy(o0, o1), rws, WS_O + (tm * 32 + head) * 8192 + (rbase + t * 16 + r16) * 64 + cswz);
      }
    }
    wait_vm<0>();
    lbar();
    if (tid == 0) set_flag(ctl, C_O + tm, gp, tag);
    if (wid == 0) STAMP(0, 8);
  }

  const int kh = gp >> 3;
  if (loader) {
    __builtin_amdgcn_s_setprio(3);
    relane();
    Loader<64, 5, 16, false, 1> L2;
    L2.init(smem, rws, mkrsrc(WO, D_ * D_ * 2), WS_O + tm * 128 * D_ * 2, 0, gp * 128, D_ * 2, 128, lw, lane, 0);
    colsum_tile<224>(smem + L_WT, rws, WS_CS3 + (gp * 16 + tm) * 896, lw, lane);
    poll(ctl, C_O + tm, 0, 16, tag, lane);
    if (wid == 4) STAMP(1, 0);
    L2.issueA(0); L2.issueA(1); L2.issueA(2); L2.issueA(3); L2.issueA(4);
    if (bid == 0 && wid == 4 && lane == 0) __hip_atomic_store(ctl + C_EPOCH * 64, tag, __ATOMIC_RELAXED, __HIP_MEMORY_SCOPE_AGENT);
    wait_vm<16>(); lbar();
    wait_vm<12>(); lbar();
    L2.tn = 5;
    L2.step<8>();
    L2.step<4 + 6>();
    L2.steps(9);
    L2.finish();
    if (lane == 0) set_flag(ctl, C_CS3 + gp, tm * 4 + lw, tag);
    const rsrc_t r2 = mkrsrc(W2, FF_ * D_ * 2);
    {
      relane();
      Loader<224, 3, 16, false> L;
      L.init(smem, rws, mkrsrc(W1, D_ * FF_ * 2), WS_H + tm * 128 * D_ * 2, 0, gp * 448, FF_ * 2, 128, lw, lane, gp);
      bar();
      L.issueB(0); L.issueB(1);
      wait_vm<7>(); bar();
      L.issueB(2);
      bar();
      if (wid == 4 && lane == 0) set_flag(ctl, C_H + tm, gp, tag);
      poll(ctl, C_H + tm, 0, 16, tag, lane);
      if (wid == 4) STAMP(2, 0);
      L.issueA(1); L.issueA(2);
      u32x4 gt[4], hs[4];
#pragma unroll
      for (int j = 0; j < 4; ++j) hs[j] = ld128(rws, WS_HST + tm * 16384 + lw * 4096 + lane * 16, j * 1024);
      const unsigned seen = peek(ctl, C_CS3 + gp, lane);
      L.tn = 3;
      L.step<4 + 5>();
      L.step<5 + 11>();
      confirm(seen, ctl, C_CS3 + gp, tag, lane);
#pragma unroll
      for (int j = 0; j < 4; ++j) gt[j] = ld128(rws, WS_CS3 + (gp * 16 + lw * 4) * 896 + lane * 16, j * 896);
      *(LAS f32x4*)(smem + L_CSS + 4096 + lw * 1024 + lane * 16) = sum4(hs);
      L.step();
      if (lw == 1) {
        const f32x4 sum = lds_sum4(smem + L_CSS + 4096 + lane * 16);
        const float m0 = sum[0] * (1.0f / D_), m1 = sum[2] * (1.0f / D_);
        const float v0 = __builtin_fmaxf(sum[1] * (1.0f / D_) - m0 * m0, 0.f), v1 = __builtin_fmaxf(sum[3] * (1.0f / D_) - m1 * m1, 0.f);
        *(LAS f32x4*)(smem + L_RST + lane * 16) = (f32x4){m0, 1.0f / __builtin_sqrtf(v0 + LN_EPS), m1, 1.0f / __builtin_sqrtf(v1 + LN_EPS)};
      }
      L.step();
      *(LAS f32x4*)(smem + L_CSS + lw * 1024 + lane * 16) = sum4(gt);
      L.step();
      if (lw == 0) *(LAS f32x4*)(smem + L_CS + lane * 16) = lds_sum4(smem + L_CSS + lane * 16);
      const int stag = (loc & 7) < 6 ? (loc & 7) : 6;
      L.steps(stag);
      touch_lines(smem, r2, bid * 28672, lane < 56 ? lw * 56 + lane : 4096, 224);
      L.step();
      L.step<12>();
      L.steps(6 - stag);
      L.finish();
    }
    {
      relane();
      Loader<128, 4, 31, false, 1> L;
      L.kh = kh;
      L.init(smem, rws, r2, WS_H1 + tm * 112 * 8192, 0, gp * 128, D_ * 2, kh ? -1024 : 1024, lw, lane, (7 * (gp & 7) + 1) >> 1);
      lbar();
      L.issueB(0); L.issueB(1); L.issueB(2); L.issueB(3);
      wait_vm<12>(); bar();
      wait_vm<8>(); bar();
      wait_vm<4>(); bar();
      if (wid == 4 && lane == 0) set_flag(ctl, C_H1 + tm, gp, tag);
      L.issueB(4);
      poll(ctl, C_H1 + tm, kh * 8, 8, tag, lane);
      if (wid == 4) STAMP(3, 0);
      L.issueA(3); L.issueA(4);
      L.tn = 5;
      L.step<4>();
      L.steps(12);
      const unsigned seen = peek(ctl, C_H1 + tm, lane, 16);
      L.steps(4);
      confirm(seen, ctl, C_H1 + tm, tag, lane, 16);
      L.steps(3);
      L.init_half(gp * 128, D_ * 2, lw, lane);
      wait_vm<8>(); bar(); L.issueH(25);
      wait_vm<6>(); bar(); L.issueH(26);
#pragma unroll 1
      for (int t = 27; t <= 30; ++t) { wait_vm<6>(); bar(); L.issueH(t); }
      wait_vm<6>(); bar();
      wait_vm<0>(); bar();
      bar();
    }
  } else {
    relane();
    {
      const int row = tm * 128 + wid * 32 + r16, col = gp * 64 + cpair;
      u32x4 res[2][2];
#pragma unroll
      for (int i = 0; i < 2; ++i)
#pragma unroll
        for (int j = 0; j < 2; ++j) res[i][j] = *(const u32x4*)(X + (size_t)(row + i * 16) * D_ + col + j * 32);
      f32x4 acc[2][4];
      mma_loop<64, 5, 16, 2, 4, true, false>(smem, acc, wid * 32, lane, [](int j) { return j; }, []() {}, [](int) {});
      if (wid == 0) STAMP(1, 2);
#pragma unroll
      for (int i = 0; i < 2; ++i) {
        float s = 0.f, qq = 0.f;
#pragma unroll
        for (int j = 0; j < 2; ++j) {
          f32x4 x = acc[i][2 * j], y = acc[i][2 * j + 1];
          swap_pair(x, y);
          add_bf8(x, y, res[i][j]);
          const u32x4 o = pack_xy(x, y);
          st128(o, rws, WS_H + (tm * 32 + gp * 2 + j) * 8192 + (wid * 32 + i * 16 + r16) * 64 + cswz);
          *(LAS u32x4*)(smem + j * 8192 + (wid * 32 + i * 16 + r16) * 64 + cswz) = o;
#pragma unroll
          for (int e = 0; e < 4; ++e) { const float lo = bflo(o[e]), hi = bfhi(o[e]); s += lo + hi; qq = __builtin_fmaf(lo, lo, __builtin_fmaf(hi, hi, qq)); }
        }
        s = x32_sum(x16_sum(s)); qq = x32_sum(x16_sum(qq));
        if (g == 0) __builtin_amdgcn_raw_buffer_store_b64(__builtin_bit_cast(u32x2, (f32x2){s, qq}), rws, WS_HST + ((tm * 16 + gp) * 128 + wid * 32 + i * 16 + r16) * 8, 0, SC1);
      }
      if (wid == 0) STAMP(1, 3);
    }
    const int wm = wid >> 1, wn = wid & 1;
    relane();
    {
      f32x4 acc[4][7];
      mma_loop<224, 3, 16, 4, 7, false, true>(smem, acc, wm * 64, lane, [&](int j) { return j < 6 ? wn * 8 + j : 6 + wn; }, []() { wait_vm<0>(); }, [](int) {});
      if (wid == 0) STAMP(2, 2);
      relane();
      f32x4 cs[7];
#pragma unroll
      for (int j = 0; j < 7; ++j) cs[j] = *(LAS const f32x4*)(smem + L_CS + ((j < 6 ? wn * 8 + j : 6 + wn) * 16 + g * 4) * 4);
      const int par = gp & 1, hb = WS_H1 + (tm * 112 + gp * 7) * 8192;
#pragma unroll
      for (int i = 0; i < 4; ++i) {
        const f32x2 ms = *(LAS const f32x2*)(smem + L_RST + (wm * 64 + i * 16 + r16) * 8);
        const float a = ms[1], b = -ms[0] * ms[1];
#pragma unroll
        for (int j = 0; j < 7; ++j)
#pragma unroll
          for (int e = 0; e < 4; ++e) acc[i][j][e] = __builtin_fmaxf(__builtin_fmaf(acc[i][j][e], a, b * cs[j][e]), 0.f);
        const int ro = (wm * 64 + i * 16 + r16) * 64 + cswz;
#pragma unroll
        for (int j = 0; j < 3; ++j) {
          f32x4 x = acc[i][2 * j], y = acc[i][2 * j + 1];
          swap_pair(x, y);
          const u32x4 o = pack_xy(x, y);
          const int sl = wn * 4 + j;
          st128(o, rws, hb + sl * 8192 + ro);
          if ((unsigned)(sl - par) < 6u) *(LAS u32x4*)(smem + (sl - par) * 8192 + ro) = o;
        }
        if (i & 1) {
          f32x4 x = acc[i - 1][6], y = acc[i][6];
          swap_pair(x, y);
          const u32x4 o = pack_xy(x, y);
          const int r3 = (wm * 64 + (i - 1 + (g & 1)) * 16 + r16) * 64 + (((2 * wn + (g >> 1)) ^ aswz(r16)) * 16);
          st128(o, rws, hb + 3 * 8192 + r3);
          *(LAS u32x4*)(smem + (3 - par) * 8192 + r3) = o;
        }
      }
      if (wid == 0) STAMP(2, 3);
    }
    relane();
    {
      const int row = tm * 128 + wm * 64 + r16, col = gp * 64 + wn * 32 + cpair;
      u32x4 res[4], t[4];
      unsigned seen = 0;
      f32x4 acc[4][4];
      mma_loop<128, 4, 31, 4, 4, true, true>(smem, acc, wm * 64, lane, [&](int j) { return (j >> 1) * 4 + wn * 2 + (j & 1); }, [&]() {
        wait_vm<0>();
        if (wid == 0) STAMP(2, 4);
#pragma unroll
        for (int i = 0; i < 4; ++i) res[i] = ld128(rws, WS_H + (tm * 32 + gp * 2 + wn) * 8192 + (wm * 64 + i * 16 + r16) * 64 + cswz, 0);
      }, [&](int what) {
        if (what == 0) {
          const int pb = WS_P4 + (tm * 16 + gp) * 16384 + wid * 4096 + lane * 16;
#pragma unroll
          for (int i = 0; i < 4; ++i) st128(pack_xy(acc[i][2], acc[i][3]), rws, pb + i * 1024);
        } else if (what == 1) {
          wait_vm<0>();
          if (lane == 0) set_flag(ctl, C_P4 + tm, gp * 4 + wid, tag);
          if (wid == 0) STAMP(3, 3);
        } else if (what == 2) {
          seen = __hip_atomic_load(ctl + (C_P4 + tm) * 64 + (gp ^ 8) * 4 + wid, __ATOMIC_RELAXED, __HIP_MEMORY_SCOPE_AGENT);
        } else {
          asm volatile("" : "+v"(seen));
          if (!wave_all(seen == tag)) poll(ctl, C_P4 + tm, (gp ^ 8) * 4 + wid, 1, tag, lane);
          acquire();
          if (wid == 0) STAMP(3, 4);
          const int qb = WS_P4 + (tm * 16 + (gp ^ 8)) * 16384 + wid * 4096 + lane * 16;
#pragma unroll
          for (int i = 0; i < 4; ++i) t[i] = ld128(rws, qb + i * 1024, 0);
        }
      });
      if (wid == 0) STAMP(3, 2);
#pragma unroll
      for (int i = 0; i < 4; ++i) {
        f32x4 x = acc[i][0], y = acc[i][1];
        add_bf8(x, y, t[i]);
        swap_pair(x, y);
        add_bf8(x, y, res[i]);
        *(u32x4*)(OUT + (size_t)(row + i * 16) * D_ + col) = pack_xy(x, y);
      }
      if (wid == 0) STAMP(3, 5);
    }
  }
#endif
}
#if defined(__HIP_DEVICE_COMPILE__)
#pragma clang attribute pop
#endif

extern "C" void kernel_launch(void* const* d_in, const int* in_sizes, int n_in, void* d_out, int out_size, void* d_ws, size_t ws_size,
                              hipStream_t stream) {
  (void)hipFuncSetAttribute((const void*)block_kernel, hipFuncAttributeMaxDynamicSharedMemorySize, LDS_BYTES);
  block_kernel<<<256, 512, LDS_BYTES, stream>>>((const u16*)d_in[0], (const u16*)d_in[1], (const u16*)d_in[2], (const u16*)d_in[3],
                                                (const u16*)d_in[5], (const u16*)d_in[6], (u16*)d_out, (char*)d_ws);
}
```

```cpp
#include <hip/hip_runtime.h>
#include <hip/hip_bf16.h>
#include <type_traits>

#define LAS __attribute__((address_space(3)))
#define GAS __attribute__((address_space(1)))
#define DI __device__ __forceinline__
typedef __bf16 bf16x8 __attribute__((ext_vector_type(8)));
typedef __bf16 bf16x2 __attribute__((ext_vector_type(2)));
typedef short s16x4 __attribute__((ext_vector_type(4)));
typedef short s16x8 __attribute__((ext_vector_type(8)));
typedef float f32x2 __attribute__((ext_vector_type(2)));
typedef float f32x4 __attribute__((ext_vector_type(4)));
typedef unsigned u32x2 __attribute__((ext_vector_type(2)));
typedef unsigned u32x4 __attribute__((ext_vector_type(4)));
typedef unsigned short u16;

constexpr int S_ = 2048, D_ = 1024, FF_ = 3584;
constexpr float LN_EPS = 1e-5f;

constexpr int C_EPOCH = 0, C_TMO = 1, C_ST = 2, C_CS1 = 18, C_CS3 = 34, C_KV = 50, C_O = 66, C_H = 82, C_H1 = 98, C_P4 = 114, C_H1B = 130, C_LINES = 146;
constexpr unsigned WS_XST = 40960;
constexpr unsigned WS_CS1 = WS_XST + S_ * 8;
constexpr unsigned WS_CS3 = WS_CS1 + 16 * 16 * 192 * 4;
constexpr unsigned WS_HST = WS_CS3 + 16 * 16 * 224 * 4;
constexpr unsigned KVP_BYTES = 8192 + 512;
constexpr unsigned WS_KVP = WS_HST + S_ * 16 * 8;
#define BLOCKED 1
constexpr unsigned WS_O = WS_KVP + 256 * KVP_BYTES;
constexpr unsigned WS_H = WS_O + S_ * D_ * 2;
constexpr unsigned WS_H1 = WS_H + S_ * D_ * 2;
constexpr unsigned WS_P4 = WS_O, WS_END = WS_H1 + S_ * FF_ * 2;
static_assert(C_LINES * 256 <= (int)WS_XST && WS_O % 256 == 0 && WS_KVP % 256 == 0, "");

constexpr int L_WT = 120 * 1024;
constexpr int L_TAB = 149 * 1024;
constexpr int L_DUMMY = L_TAB;
constexpr int L_RST = L_TAB + 256;
constexpr int L_CS = L_RST + 1024;
constexpr int L_CSS = L_CS + 1024;
constexpr int L_GO = L_CSS + 8192;
constexpr int LDS_BYTES = L_GO + 16;
static_assert(LDS_BYTES <= 160 * 1024, "");

#define STAMP(k, i) do {} while (0)
#define TS_DECL
#define TS(t)
#define TS_OUT(k)

#if defined(__HIP_DEVICE_COMPILE__)
#pragma clang attribute push(__attribute__((target("no-packed-fp32-ops"))), apply_to = function)
typedef __amdgpu_buffer_rsrc_t rsrc_t;
DI float u2f(unsigned u) { return __builtin_bit_cast(float, u); }
DI unsigned f2u(float f) { return __builtin_bit_cast(unsigned, f); }
DI bool wave_all(bool p) { return __builtin_amdgcn_ballot_w64(p) == ~0ull; }
#define CODE_AHEAD 36864
#define PSLEEP "2"
constexpr int SC1 = 16;
template <int N> DI void wait_vm() { asm volatile("s_waitcnt vmcnt(%0)" ::"n"(N) : "memory"); }
DI void wait_lgkm0() { asm volatile("s_waitcnt lgkmcnt(0)" ::: "memory"); }
DI void bar() { asm volatile("" ::: "memory"); __builtin_amdgcn_s_barrier(); asm volatile("" ::: "memory"); }
DI void lbar() { wait_lgkm0(); bar(); }
DI unsigned pk2(float a, float b) { f32x2 v = {a, b}; return __builtin_bit_cast(unsigned, __builtin_convertvector(v, bf16x2)); }
DI float bflo(unsigned u) { return u2f(u << 16); }
DI float bfhi(unsigned u) { return u2f(u & 0xffff0000u); }
DI bf16x8 ld_b128(LAS const char* p) { return *(LAS const bf16x8*)p; }
DI bf16x8 ld_tr2(LAS const char* p0, LAS const char* p1) {
  s16x4 a = __builtin_amdgcn_ds_read_tr16_b64_v4i16((LAS s16x4*)p0);
  s16x4 b = __builtin_amdgcn_ds_read_tr16_b64_v4i16((LAS s16x4*)p1);
  return __builtin_bit_cast(bf16x8, __builtin_shufflevector(a, b, 0, 1, 2, 3, 4, 5, 6, 7));
}
DI float x32_sum(float v) { auto r = __builtin_amdgcn_permlane32_swap(f2u(v), f2u(v), false, false); return u2f(r[0]) + u2f(r[1]); }
DI float x16_sum(float v) { auto r = __builtin_amdgcn_permlane16_swap(f2u(v), f2u(v), false, false); return u2f(r[0]) + u2f(r[1]); }
template <int CTRL> DI float dpp_add(float v) { return v + u2f(__builtin_amdgcn_update_dpp(0, (int)f2u(v), CTRL, 0xf, 0xf, true)); }
DI float half_sum(float v) {
  v = dpp_add<0xB1>(v);
  v = dpp_add<0x4E>(v);
  v = dpp_add<0x141>(v);
  v = dpp_add<0x140>(v);
  return x16_sum(v);
}
#define MFMA16(a, b, c) __builtin_amdgcn_mfma_f32_16x16x32_bf16((a), (b), (c), 0, 0, 0)
DI rsrc_t mkrsrc(const void* p, unsigned bytes) { return __builtin_amdgcn_make_buffer_rsrc((void*)p, 0, bytes, 0x00020000); }
DI void st128(u32x4 v, rsrc_t r, int off) { __builtin_amdgcn_raw_buffer_store_b128(v, r, off, 0, SC1); }
DI u32x4 ld128(rsrc_t r, int off, int soff) { return __builtin_amdgcn_raw_buffer_load_b128(r, off, soff, SC1); }

DI unsigned read_tag(const char* ws) {
  unsigned e;
  asm volatile("s_load_dword %0, %1, 0x0\n\ts_waitcnt lgkmcnt(0)" : "=s"(e) : "s"(ws) : "memory");
  return e + 1u;
}
DI void set_flag(GAS unsigned* ctl, int line, int word, unsigned tag) { __hip_atomic_store(ctl + line * 64 + word, tag, __ATOMIC_RELAXED, __HIP_MEMORY_SCOPE_AGENT); }
DI void acquire() {
  asm volatile("" ::: "memory");
}
DI void poll(GAS unsigned* ctl, int line, int lo, int n, unsigned tag, int lane) {
  GAS unsigned* p = ctl + line * 64 + lo + (lane < n ? lane : 0);
  unsigned spins = 0;
  while (!wave_all(__hip_atomic_load(p, __ATOMIC_RELAXED, __HIP_MEMORY_SCOPE_AGENT) == tag)) {
    __builtin_amdgcn_s_sleep(1);
    if (++spins > (1u << 17)) { if (lane == 0) __hip_atomic_store(ctl + C_TMO * 64, (unsigned)line, __ATOMIC_RELAXED, __HIP_MEMORY_SCOPE_AGENT); break; }
  }
  acquire();
}
struct FastPoll {
  unsigned a, b, c, d;
  DI void run(GAS unsigned* ctl, int line, int lo, int n, unsigned tag, int lane) {
    GAS unsigned* p = ctl + line * 64 + lo + (lane < n ? lane : 0);
    unsigned cnt = 1u << 17;
#define FP_LD(r) "global_load_dword %" #r ", %5, off sc1\n\ts_sleep " PSLEEP "\n\t"
#define FP_CK(r) "s_waitcnt vmcnt(3)\n\tv_cmp_eq_u32 vcc, %6, %" #r "\n\ts_cmp_eq_u64 vcc, -1\n\ts_cbranch_scc1 2f\n\t" FP_LD(r)
    asm volatile(FP_LD(0) FP_LD(1) FP_LD(2) FP_LD(3) "1:\n\t" FP_CK(0) FP_CK(1) FP_CK(2) FP_CK(3)
                 "s_sub_u32 %4, %4, 1\n\ts_cbranch_scc0 1b\n\t2:"
                 : "=&v"(a), "=&v"(b), "=&v"(c), "=&v"(d), "+s"(cnt) : "v"(p), "s"(tag) : "vcc", "scc", "memory");
#undef FP_LD
#undef FP_CK
  }
  DI void keep() const { asm volatile("" :: "v"(a), "v"(b), "v"(c), "v"(d)); }
};
DI void poll4(FastPoll& fp, LAS char* smem, unsigned go, int lw, GAS unsigned* ctl, int line, int lo, int n, unsigned tag, int lane) {
  LAS volatile unsigned* w = (LAS volatile unsigned*)(smem + L_GO);
  if (lw == 0) {
    fp.run(ctl, line, lo, n, tag, lane);
    *w = go;
  } else {
    fp.a = fp.b = fp.c = fp.d = 0u;
    unsigned spins = 0;
    while (__builtin_amdgcn_readfirstlane(*w) != go && ++spins < (1u << 24)) __builtin_amdgcn_s_sleep(1);
  }
  asm volatile("" ::: "memory");
}

template <int BN> DI constexpr int bswz(int k) {
  return BN == 224 ? (((k >> 3) & 1) << 1)
       : BN == 128 ? (((k & 3) | (((k >> 3) & 1) << 2)) << 1)
                   : ((((k >> 1) & 1) | (((k >> 3) & 1) << 1)) << 1);
}

DI constexpr int aswz(int r) { return (-(r >> 2)) & 3; }

template <int BN, int NS, int NT, bool AROW, int LAG = 0>
struct Loader {
  static constexpr int PA = 4, PB = BN / 32, PW = PA + PB, BST = BN * 128, CPR = BN / 8;
  static_assert(NS * (16384 + BST) <= L_TAB, "");
  LAS char* dst0; rsrc_t ra, rb; int va[PA], vb[PB], vh[2]; int ldb64, own0, tn;
  static constexpr int NOWN = BN == 128 ? 3 : BN == 224 ? 1 : 0;
  int kh;
  DI int ktile(int t) const {
    if (AROW) return (t + own0) & (NT - 1);
    if (BN != 128) return t < NOWN ? own0 + t : (t - NOWN < own0 ? t - NOWN : t);
    const int pi = t < 17 ? (t - 3) >> 1 : t - 17, p = t < 3 ? own0 : pi + (pi >= own0 ? 1 : 0);
    const int k = t >= 24 ? 7 * ((t >= 28 ? t - 3 : t) - 24) + 3 : ((7 * p + 1) >> 1) + (t < 3 ? t : t < 17 ? 2 * ((t - 3) & 1) : 1);
    return k + ((t >= 28) != (kh != 0) ? 28 : 0);
  }
  DI void issueA(int t) {
    const int kk = ktile(t), slot = t % NS;
#pragma unroll
    for (int i = 0; i < PA; ++i)
      __builtin_amdgcn_raw_ptr_buffer_load_lds(ra, dst0 + slot * 16384 + i * 4096, 16, va[i], kk * (AROW ? 128 : 16384), 0, AROW ? 0 : SC1);
  }
  DI void issueB(int t) {
    const int kk = ktile(t), slot = t % NS;
#pragma unroll
    for (int i = 0; i < PB; ++i) __builtin_amdgcn_raw_ptr_buffer_load_lds(rb, dst0 + NS * 16384 + slot * BST + i * 4096, 16, vb[i], kk * ldb64, 0, 0);
  }
  DI void issueH(int t) {
    issueA(t);
    const int kk = ktile(t), slot = t % NS;
#pragma unroll
    for (int i = 0; i < 2; ++i) __builtin_amdgcn_raw_ptr_buffer_load_lds(rb, dst0 + NS * 16384 + slot * BST + i * 4096, 16, vh[i], kk * ldb64, 0, 0);
  }
  DI void init(LAS char* smem, rsrc_t ra_, rsrc_t rb_, int a_off, int lda_b, int b_off, int ldb_b, int segb, int lw, int lane, int own) {
    dst0 = smem + lw * 1024; ra = ra_; rb = rb_; ldb64 = 64 * ldb_b; own0 = own; tn = NS - 1;
#pragma unroll
    for (int i = 0; i < PA; ++i) {
      const int ins = lw + i * 4, r = ins * 8 + (lane >> 3), c = (lane & 7) ^ ((r >> 1) & 7);
      va[i] = AROW ? a_off + r * lda_b + c * 16 : a_off + ins * 1024 + lane * 16;
    }
#pragma unroll
    for (int i = 0; i < PB; ++i) {
      const int ch = (lw + i * 4) * 64 + lane, k = ch / CPR, c = (ch % CPR) ^ bswz<BN>(k);
      vb[i] = b_off + k * ldb_b + (c >> 3) * segb + (c & 7) * 16;
    }
  }
  DI void init_half(int b_off, int ldb_b, int lw, int lane) {
#pragma unroll
    for (int i = 0; i < 2; ++i) { const int k = (lw + i * 4) * 8 + (lane >> 3); vh[i] = b_off + k * ldb_b + (((lane & 7) ^ bswz<64>(k)) * 16); }
  }
  template <int ALLOW = (NS - 2 - LAG) * PW> DI void step() { wait_vm<ALLOW>(); lbar(); issueA(tn); issueB(tn); ++tn; }
  DI void steps(int n) {
#pragma unroll 1
    for (int t = 0; t < n; ++t) step();
  }
  DI void finish() {
    if constexpr (NS - LAG >= 5) { wait_vm<3 * PW>(); lbar(); }
    if constexpr (NS - LAG >= 4) { wait_vm<2 * PW>(); lbar(); }
    if constexpr (NS - LAG >= 3) { wait_vm<1 * PW>(); lbar(); }
    wait_vm<0>(); lbar();
  }
};
template <int BN, int I0, int I1> DI void load_wtile(LAS char* dst, rsrc_t r, int off, int ldb_b, int segb, int lw, int lane) {
#pragma unroll
  for (int i = I0; i < I1; ++i) {
    const int ch = (lw + i * 4) * 64 + lane, k = ch / (BN / 8), c = (ch % (BN / 8)) ^ bswz<BN>(k);
    __builtin_amdgcn_raw_ptr_buffer_load_lds(r, dst + (lw + i * 4) * 1024, 16, off + k * ldb_b + (c >> 3) * segb + (c & 7) * 16, 0, 0, 0);
  }
}
DI int opaque(int v) { asm volatile("" : "+v"(v)); return v; }

DI void touch_lines(LAS char* smem, rsrc_t r, int base, int line, int nlines) {
  __builtin_amdgcn_raw_ptr_buffer_load_lds(r, smem + L_DUMMY, 4, line < nlines ? base + line * 128 : (int)0x80000000, 0, 0, 0);
}

template <int BN> DI void colsum_tile(LAS char* img, rsrc_t rws, unsigned off, int lw, int lane) {
  constexpr int RS = BN * 2;
  const int r16 = lane & 15, g = lane >> 4, q = (lane & 15) >> 2, p = lane & 3;
  const s16x8 one_ = {0x3f80, 0x3f80, 0x3f80, 0x3f80, 0x3f80, 0x3f80, 0x3f80, 0x3f80};
  LAS const char* rd[4];
#pragma unroll
  for (int i = 0; i < 4; ++i) {
    const int nt = lw + 4 * i < BN / 16 ? lw + 4 * i : lw;
    rd[i] = img + (8 * g + q) * RS + (((2 * nt + (p >> 1)) ^ bswz<BN>(8 * g + q)) * 16) + 8 * (p & 1);
  }
  s16x4 v[4][4];
#define TR4(a, b, c, d, r) "ds_read_b64_tr_b16 %" #a ", %" #r "\n\tds_read_b64_tr_b16 %" #b ", %" #r " offset:%c20\n\t" \
                           "ds_read_b64_tr_b16 %" #c ", %" #r " offset:%c21\n\tds_read_b64_tr_b16 %" #d ", %" #r " offset:%c22\n\t"
  asm volatile(TR4(0, 1, 2, 3, 16) TR4(4, 5, 6, 7, 17) TR4(8, 9, 10, 11, 18) TR4(12, 13, 14, 15, 19) "s_waitcnt lgkmcnt(0)"
               : "=&v"(v[0][0]), "=&v"(v[0][1]), "=&v"(v[0][2]), "=&v"(v[0][3]), "=&v"(v[1][0]), "=&v"(v[1][1]), "=&v"(v[1][2]), "=&v"(v[1][3]),
                 "=&v"(v[2][0]), "=&v"(v[2][1]), "=&v"(v[2][2]), "=&v"(v[2][3]), "=&v"(v[3][0]), "=&v"(v[3][1]), "=&v"(v[3][2]), "=&v"(v[3][3])
               : "v"(rd[0]), "v"(rd[1]), "v"(rd[2]), "v"(rd[3]), "i"(4 * RS), "i"(32 * RS), "i"(36 * RS) : "memory");
#undef TR4
  f32x4 c[4];
#pragma unroll
  for (int i = 0; i < 4; ++i) {
    c[i] = (f32x4){0.f, 0.f, 0.f, 0.f};
    c[i] = MFMA16(__builtin_bit_cast(bf16x8, __builtin_shufflevector(v[i][0], v[i][1], 0, 1, 2, 3, 4, 5, 6, 7)), __builtin_bit_cast(bf16x8, one_), c[i]);
    c[i] = MFMA16(__builtin_bit_cast(bf16x8, __builtin_shufflevector(v[i][2], v[i][3], 0, 1, 2, 3, 4, 5, 6, 7)), __builtin_bit_cast(bf16x8, one_), c[i]);
  }
  const f32x4 out = r16 == 0 ? c[0] : r16 == 1 ? c[1] : r16 == 2 ? c[2] : c[3];
  st128(__builtin_bit_cast(u32x4, out), rws, lw + 4 * r16 < BN / 16 ? off + (lw + 4 * r16) * 64 + g * 16 : 0x80000000u);
}
DI f32x4 sum4(u32x4 (&t)[4]) {
  asm volatile("" : "+v"(t[0]), "+v"(t[1]), "+v"(t[2]), "+v"(t[3]));
  return (__builtin_bit_cast(f32x4, t[0]) + __builtin_bit_cast(f32x4, t[1])) + (__builtin_bit_cast(f32x4, t[2]) + __builtin_bit_cast(f32x4, t[3]));
}
DI f32x4 lds_sum4(LAS const char* p) {
  return (*(LAS const f32x4*)p + *(LAS const f32x4*)(p + 1024)) + (*(LAS const f32x4*)(p + 2048) + *(LAS const f32x4*)(p + 3072));
}
DI unsigned peek(GAS unsigned* ctl, int line, int lane, int n = 64) { return __hip_atomic_load(ctl + line * 64 + (lane & (n - 1)), __ATOMIC_RELAXED, __HIP_MEMORY_SCOPE_AGENT); }
DI void confirm(unsigned seen, GAS unsigned* ctl, int line, unsigned tag, int lane, int n = 64) {
  asm volatile("" : "+v"(seen));
  if (!wave_all(seen == tag)) poll(ctl, line, 0, n, tag, lane);
  else acquire();
}

DI void mfma16(f32x4& c, const bf16x8& a, const bf16x8& b) { asm volatile("v_mfma_f32_16x16x32_bf16 %0, %1, %2, %0" : "+v"(c) : "v"(a), "v"(b)); }
DI void mfma_drain() { asm volatile("s_nop 7\n\ts_nop 7" ::: "memory"); }
template <int TM, int TN> struct Frag { s16x4 b[TN][2]; bf16x8 a[TM]; };
template <int BN, int NS, int NT, int TM, int TN, bool DEEP, bool OWN, class NMap, class Mid, class Send>
DI void mma_loop(LAS char* smem, f32x4 (&acc)[TM][TN], int row0, int lane, NMap&& nt, Mid&& mid, Send&& send) {
  constexpr int RS = BN * 2, BST = BN * 128, NL = TM + 2 * TN, NM = TM * TN;
  typedef Frag<TM, TN> F;
  const int r16 = lane & 15, g = lane >> 4, q = (lane & 15) >> 2, p = lane & 3;
  constexpr bool AROW = BN == 192;
  const int aoff = AROW ? (row0 + r16) * 128 + ((g ^ (r16 >> 1)) * 16) : (row0 + r16) * 64 + ((g ^ aswz(r16)) * 16);
  const int aoff1 = AROW ? aoff ^ 64 : aoff + 8192;
  int boff[TN];
#pragma unroll
  for (int j = 0; j < TN; ++j) boff[j] = NS * 16384 + (8 * g + q) * RS + (((2 * nt(j) + (p >> 1)) ^ bswz<BN>(8 * g + q)) * 16) + 8 * (p & 1);
#pragma unroll
  for (int i = 0; i < TM; ++i)
#pragma unroll
    for (int j = 0; j < TN; ++j) { acc[i][j] = (f32x4){0.f, 0.f, 0.f, 0.f}; asm volatile("" : "+v"(acc[i][j])); }
  int boffh[2];
  auto ld1 = [&](F& f, int slot, int s, int k, int tn = TN) {
    if (k == 0 || k > 2 * tn) { const int i = k == 0 ? 0 : k - 2 * tn; f.a[i] = ld_b128(smem + slot * 16384 + (s ? aoff1 : aoff) + i * (AROW ? 2048 : 1024)); }
    else {
      const int j = (k - 1) >> 1, u = (k - 1) & 1;
      f.b[j][u] = __builtin_amdgcn_ds_read_tr16_b64_v4i16((LAS s16x4*)(smem + slot * BST + (tn == TN ? boff[j] + (32 * s + 4 * u) * RS : boffh[j & 1] + (32 * s + 4 * u) * 128)));
    }
  };
  auto mm1 = [&](const F& f, int m, int tn = TN) {
    const int i = m / tn, j = m % tn;
    const bf16x8 b = __builtin_bit_cast(bf16x8, __builtin_shufflevector(f.b[j][0], f.b[j][1], 0, 1, 2, 3, 4, 5, 6, 7));
    if (AROW && j >= 4) mfma16(acc[i][j], f.a[i], b); else mfma16(acc[i][j], b, f.a[i]);
  };
  auto ldall = [&](F& f, int slot, int s) {
#pragma unroll
    for (int k = 0; k < NL; ++k) ld1(f, slot, s, k);
  };
  auto mmall = [&](const F& f) {
#pragma unroll
    for (int m = 0; m < NM; ++m) mm1(f, m);
  };
  auto sub = [&](const F& x, F& u, int slot, int s) {
    constexpr int SP = DEEP ? NM : (NL < NM ? NL : NM - 2);
#pragma unroll
    for (int m = 0; m < NM; ++m) {
      mm1(x, m);
#pragma unroll
      for (int k = (m * NL + SP - 1) / SP; k < NL && k < ((m + 1) * NL + SP - 1) / SP; ++k) ld1(u, slot, s, k);
      __builtin_amdgcn_sched_barrier(0);
    }
  };
  auto subh = [&](const F& x, F& u, int slot, int s, auto tx, auto tu) {
    constexpr int TX = decltype(tx)::value, TU = decltype(tu)::value, M = TM * TX, LN = TM + 2 * TU;
#pragma unroll
    for (int m = 0; m < M; ++m) {
      mm1(x, m, TX);
#pragma unroll
      for (int k = (m * LN + M - 1) / M; k < LN && k < ((m + 1) * LN + M - 1) / M; ++k) ld1(u, slot, s, k, TU);
      __builtin_amdgcn_sched_barrier(0);
    }
  };
  auto nxt = [](int s) { return s == NS - 1 ? 0 : s + 1; };
  lbar();
  if (threadIdx.x < 64) STAMP(BN == 192 ? 0 : BN == 64 ? 1 : BN == 224 ? 2 : 3, 1);
  TS_DECL; TS(0);
  int slot = 0;
  if constexpr (!DEEP) {
    F f0, f1;
    ldall(f0, 0, 0);
    if constexpr (OWN) {
      sub(f0, f1, 0, 1);
      mid();
      bar();
      mmall(f1);
      __builtin_amdgcn_sched_barrier(0);
      TS(32);
      lbar();
      TS(1);
      slot = 1;
      ldall(f0, 1, 0);
    }
#pragma unroll 1
    for (int t = OWN ? 1 : 0; t < NT - 1; ++t) {
      sub(f0, f1, slot, 1);
      TS(32 + t);
      lbar();
      TS(t + 1);
      slot = nxt(slot);
      sub(f1, f0, slot, 0);
    }
    sub(f0, f1, slot, 1);
    lbar();
    TS(NT); TS_OUT(BN == 192 ? 0 : BN == 64 ? 1 : BN == 224 ? 2 : 3);
    mmall(f1);
  } else {
    F f0, f1, g0, g1;
    auto tile = [&](const F& x0, const F& x1, F& u0, F& u1, int t) {
      TS(32 + t);
      bar();
      TS(t + 1);
      slot = nxt(slot);
      sub(x0, u0, slot, 0);
      sub(x1, u1, slot, 1);
    };
    ldall(f0, 0, 0); ldall(f1, 0, 1);
    int t0 = 0;
    if constexpr (OWN) {
      tile(f0, f1, g0, g1, 0);
      tile(g0, g1, f0, f1, 1);
      mmall(f0); mmall(f1);
      __builtin_amdgcn_sched_barrier(0);
      TS(34);
      lbar();
      TS(3);
      slot = 3;
      ldall(g0, 3, 0); ldall(g1, 3, 1);
      tile(g0, g1, f0, f1, 3);
      t0 = 4;
    }
    if constexpr (!OWN) {
#pragma unroll 1
      for (int t = t0; t < NT - 2; t += 2) {
        tile(f0, f1, g0, g1, t);
        tile(g0, g1, f0, f1, t + 1);
      }
      tile(f0, f1, g0, g1, NT - 2);
      lbar();
      TS(NT); TS_OUT(BN == 192 ? 0 : BN == 64 ? 1 : BN == 224 ? 2 : 3);
      mmall(g0); mmall(g1);
    } else {
#pragma unroll 1
      for (int t = t0; t < 24; t += 2) {
        tile(f0, f1, g0, g1, t);
        if (t == 4) mid();
        tile(g0, g1, f0, f1, t + 1);
      }
      typedef std::integral_constant<int, TN> Full;
      typedef std::integral_constant<int, TN / 2> Half;
      auto tileh = [&](const F& x0, const F& x1, F& u0, F& u1, auto tx) {
        bar();
        slot = nxt(slot);
        subh(x0, u0, slot, 0, tx, Half{});
        subh(x1, u1, slot, 1, tx, Half{});
      };
#pragma unroll
      for (int j = 0; j < 2; ++j) {
        const int l2 = opaque(lane), k = 8 * (l2 >> 4) + ((l2 & 15) >> 2);
        boffh[j] = NS * 16384 + k * 128 + (((2 * nt(j) + ((l2 & 3) >> 1)) ^ bswz<64>(k)) * 16) + 8 * (l2 & 1);
      }
      tileh(f0, f1, g0, g1, Full{});
      mfma_drain();
#pragma unroll
      for (int i = 0; i < TM; ++i)
#pragma unroll
        for (int j = TN / 2; j < TN; ++j) asm volatile("" : "+v"(acc[i][j]));
      send(0);
      __builtin_amdgcn_sched_barrier(0);
      tileh(g0, g1, f0, f1, Half{});
      tileh(f0, f1, g0, g1, Half{});
      send(1);
      tileh(g0, g1, f0, f1, Half{});
      tileh(f0, f1, g0, g1, Half{});
      send(2);
      tileh(g0, g1, f0, f1, Half{});
      send(3);
      lbar();
      TS(28); TS_OUT(3);
#pragma unroll
      for (int m = 0; m < TM * TN / 2; ++m) mm1(f0, m, TN / 2);
#pragma unroll
      for (int m = 0; m < TM * TN / 2; ++m) mm1(f1, m, TN / 2);
    }
  }
  mfma_drain();
#pragma unroll
  for (int i = 0; i < TM; ++i)
#pragma unroll
    for (int j = 0; j < TN; ++j) asm volatile("" : "+v"(acc[i][j]));
}
DI void swap_pair(f32x4& x, f32x4& y) {
#pragma unroll
  for (int e = 0; e < 4; ++e) {
    auto r = __builtin_amdgcn_permlane16_swap(f2u(x[e]), f2u(y[e]), false, false);
    x[e] = u2f(r[0]); y[e] = u2f(r[1]);
  }
}
DI void add_bf8(f32x4& x, f32x4& y, const u32x4& rv) {
  x[0] += bflo(rv[0]); x[1] += bfhi(rv[0]); x[2] += bflo(rv[1]); x[3] += bfhi(rv[1]);
  y[0] += bflo(rv[2]); y[1] += bfhi(rv[2]); y[2] += bflo(rv[3]); y[3] += bfhi(rv[3]);
}
DI u32x4 pack_xy(const f32x4& x, const f32x4& y) { return (u32x4){pk2(x[0], x[1]), pk2(x[2], x[3]), pk2(y[0], y[1]), pk2(y[2], y[3])}; }

template <class Op> DI void red2(float& a, float& b, Op&& op) {
  auto r = __builtin_amdgcn_permlane16_swap(f2u(a), f2u(b), false, false);
  const float c = op(u2f(r[0]), u2f(r[1]));
  auto t = __builtin_amdgcn_permlane32_swap(f2u(c), f2u(c), false, false);
  const float d = op(u2f(t[0]), u2f(t[1]));
  auto u = __builtin_amdgcn_permlane16_swap(f2u(d), f2u(d), false, false);
  a = u2f(u[0]); b = u2f(u[1]);
}
DI void dual_softmax(const f32x4 (&p)[2][2], const float (&sc)[2], f32x4 (&e)[2][2][2]) {
  float mx[2], mn[2], s1[2], s2[2];
#pragma unroll
  for (int t = 0; t < 2; ++t) {
    mx[t] = p[t][0][0]; mn[t] = p[t][0][0];
#pragma unroll
    for (int r = 1; r < 8; ++r) { mx[t] = __builtin_fmaxf(mx[t], p[t][r >> 2][r & 3]); mn[t] = __builtin_fminf(mn[t], p[t][r >> 2][r & 3]); }
  }
  red2(mx[0], mx[1], [](float x, float y) { return __builtin_fmaxf(x, y); });
  red2(mn[0], mn[1], [](float x, float y) { return __builtin_fminf(x, y); });
#pragma unroll
  for (int t = 0; t < 2; ++t) {
    const float a = -mx[t] * sc[t], b = mn[t] * sc[t];
    s1[t] = 0.f; s2[t] = 0.f;
#pragma unroll
    for (int r = 0; r < 8; ++r) {
      const float x = __builtin_amdgcn_exp2f(__builtin_fmaf(p[t][r >> 2][r & 3], sc[t], a));
      const float y = __builtin_amdgcn_exp2f(__builtin_fmaf(p[t][r >> 2][r & 3], -sc[t], b));
      e[0][t][r >> 2][r & 3] = x; e[1][t][r >> 2][r & 3] = y;
      s1[t] += x; s2[t] += y;
    }
  }
  red2(s1[0], s1[1], [](float x, float y) { return x + y; });
  red2(s2[0], s2[1], [](float x, float y) { return x + y; });
#pragma unroll
  for (int t = 0; t < 2; ++t) {
    const float r1 = __builtin_amdgcn_rcpf(s1[t]), r2 = __builtin_amdgcn_rcpf(s2[t]);
#pragma unroll
    for (int r = 0; r < 8; ++r) { e[0][t][r >> 2][r & 3] *= r1; e[1][t][r >> 2][r & 3] *= r2; }
  }
}
DI bf16x8 pack_frag(const f32x4& x, const f32x4& y) { return __builtin_bit_cast(bf16x8, pack_xy(x, y)); }
DI void st4(LAS char* p, const f32x4& a, const f32x4& b, const f32x4& c, const f32x4& d) {
  *(LAS f32x4*)p = a; *(LAS f32x4*)(p + 1024) = b; *(LAS f32x4*)(p + 2048) = c; *(LAS f32x4*)(p + 3072) = d;
}
DI void ld4(LAS const char* p, f32x4 (&v)[2][2]) {
  v[0][0] = *(LAS const f32x4*)p; v[0][1] = *(LAS const f32x4*)(p + 1024); v[1][0] = *(LAS const f32x4*)(p + 2048); v[1][1] = *(LAS const f32x4*)(p + 3072);
}
#endif

__global__ __launch_bounds__(512, 2) void block_kernel(const u16* __restrict__ X, const u16* __restrict__ WQKV, const u16* __restrict__ WPHI,
                                                        const u16* __restrict__ WO, const u16* __restrict__ W1, const u16* __restrict__ W2,
                                                        u16* __restrict__ OUT, char* WS) {
#if defined(__HIP_DEVICE_COMPILE__)
  extern __shared__ __attribute__((aligned(16))) char smem_[];
  LAS char* smem = (LAS char*)smem_;
  const int tid = threadIdx.x, wid = __builtin_amdgcn_readfirstlane(tid >> 6);
  int lane = tid & 63;
  const int bid = blockIdx.x, xcd = bid & 7, loc = bid >> 3;
  const int tm = (xcd & 1) * 8 + (loc & 7), gp = (xcd >> 1) * 4 + (loc >> 3);
  const bool loader = wid >= 4;
  const int lw = wid & 3;
  GAS unsigned* ctl = (GAS unsigned*)WS;
  const rsrc_t rws = mkrsrc(WS, WS_END);
  if (wid == 0) STAMP(0, 0);
  unsigned tag = 0;

  int r16, g, cpair, cswz;
  auto relane = [&]() {
    lane = opaque(lane);
    r16 = lane & 15; g = lane >> 4;
    cpair = (g & 1) * 16 + (g >> 1) * 8;
    cswz = ((cpair >> 3) ^ aswz(r16)) * 16;
  };
  {
    constexpr int NT = 16, NS = 3;
    constexpr int L_PHI = 0;
    constexpr int L_XV = 2 * 17408;
    constexpr int L_VF = L_XV + 16384;
    constexpr int L_XQ = L_VF + 8192;
    constexpr int L_KVT = L_XQ;
    constexpr int L_WF = L_XQ + 16384;
    static_assert(L_WF + 4096 <= 5 * 16384, "phase 2's B ring is filled during this epilogue");
    relane();
    const int wm = (wid >> 1) & 1, hh = wid & 1, mi = wid >> 2;
    const int head = gp * 2 + hh;
    const unsigned kvp_grp = WS_KVP + gp * 16 * KVP_BYTES;
    LAS char* const xk = smem + L_PHI + hh * 17408 + (wm * 64 + 32) * 136 + lane * 16;
    bf16x8 wf[2];
    f32x4 fq[2][2], fk[2][2], fv[2][2];
    if (loader) {
      const rsrc_t rb = mkrsrc(WQKV, D_ * 3 * D_ * 2), ro = mkrsrc(WO, D_ * D_ * 2);
      Loader<192, NS, NT, true> L;
      const rsrc_t rx = mkrsrc(X, S_ * D_ * 2);
      L.init(smem, rx, rb, tm * 128 * D_ * 2, D_ * 2, gp * 128, 3 * D_ * 2, 2048, lw, lane, (tm & 3) * 4);
      L.issueA(0); L.issueB(0); L.issueA(1); L.issueB(1);
      if (lw == 0) {
        unsigned long long pc;
        asm volatile("s_getpc_b64 %0" : "=s"(pc));
        touch_lines(smem, mkrsrc((const void*)(pc & ~127ull), CODE_AHEAD), (loc * 16) * 128, lane < 16 ? lane : 4096, 16);
      }
      if (lw == 0) *(LAS volatile unsigned*)(smem + L_GO) = 0u;
      tag = read_tag(WS);
      __builtin_amdgcn_s_setprio(3);
      if (wid == 4) STAMP(4, 1);
      auto cs1 = [&](int t) { if ((tm >> 2) == t) colsum_tile<192>(smem + NS * 16384 + (t % NS) * 24576, rws, WS_CS1 + (gp * 16 + tm) * 768, lw, lane); };
      L.step();
      cs1(0);
      const int xrow = tm * 128 + gp * 8 + lw * 2 + (lane >> 5);
      u32x4 xv[4];
#pragma unroll
      for (int j = 0; j < 4; ++j) xv[j] = __builtin_amdgcn_raw_buffer_load_b128(rx, xrow * (D_ * 2) + (lane & 31) * 16, j * 512, 0);
      L.step<14>();
      cs1(1);
      L.step<14>();
      cs1(2);
      L.step();
      cs1(3);
      {
        asm volatile("" : "+v"(xv[0]), "+v"(xv[1]), "+v"(xv[2]), "+v"(xv[3]));
        float f[32], sum = 0.f, qq = 0.f;
#pragma unroll
        for (int i = 0; i < 16; ++i) { f[2 * i] = bflo(xv[i >> 2][i & 3]); f[2 * i + 1] = bfhi(xv[i >> 2][i & 3]); sum += f[2 * i] + f[2 * i + 1]; }
        const float mean = half_sum(sum) * (1.0f / D_);
#pragma unroll
        for (int i = 0; i < 32; ++i) { const float d = f[i] - mean; qq = __builtin_fmaf(d, d, qq); }
        const float rstd = 1.0f / __builtin_sqrtf(half_sum(qq) * (1.0f / D_) + LN_EPS);
        __builtin_amdgcn_raw_buffer_store_b64(__builtin_bit_cast(u32x2, (f32x2){mean, rstd}), rws, (lane & 31) == 0 ? WS_XST + xrow * 8 : 0x80000000u, 0, SC1);
      }
      L.step();
      L.step<0>();
      if (lane == 0) { set_flag(ctl, C_CS1 + gp, tm * 4 + lw, tag); set_flag(ctl, C_ST + tm, gp * 4 + lw, tag); }
      touch_lines(smem, ro, bid * 8192, lane < 16 ? lw * 16 + lane : 4096, 64);
      L.step();
      L.step<13>();
      L.steps(3);
      const unsigned seen = peek(ctl, C_CS1 + gp, lane), seen2 = peek(ctl, C_ST + tm, lane);
      L.steps(2);
      confirm(seen, ctl, C_CS1 + gp, tag, lane);
      confirm(seen2, ctl, C_ST + tm, tag, lane);
      u32x4 gt[4];
#pragma unroll
      for (int j = 0; j < 4; ++j) gt[j] = ld128(rws, WS_CS1 + (gp * 16 + lw * 4) * 768 + lane * 16, j * 768);
      u32x4 xs = ld128(rws, WS_XST + tm * 1024 + lane * 16, 0);
      L.step();
      wait_vm<10>(); lbar();
      *(LAS f32x4*)(smem + L_CSS + lw * 1024 + lane * 16) = sum4(gt);
      asm volatile("" : "+v"(xs));
      if (lw == 1) *(LAS u32x4*)(smem + L_RST + lane * 16) = xs;
      wait_vm<0>(); lbar();
      if (lw == 0) *(LAS f32x4*)(smem + L_CS + lane * 16) = lds_sum4(smem + L_CSS + lane * 16);
      const rsrc_t r1 = mkrsrc(W1, D_ * FF_ * 2);
      load_wtile<224, 0, 4>(smem + L_WT, r1, tm * 64 * (FF_ * 2) + gp * 448, FF_ * 2, 128, lw, lane);
      lbar();
      bar();
      __builtin_amdgcn_s_setprio(1);
      ld4(xk, fk);
      wf[0] = ld_b128(smem + L_WF + hh * 2048 + lane * 16);
      wf[1] = ld_b128(smem + L_WF + hh * 2048 + 1024 + lane * 16);
      ld4(smem + L_XV + lw * 4096 + lane * 16, fv);
    } else {
      {
#pragma unroll
        for (int t = 0; t < 2; ++t) {
          s16x8 w;
#pragma unroll
          for (int j = 0; j < 8; ++j) w[j] = (short)WPHI[head * 1024 + (16 * (j >> 2) + 4 * g + (j & 3)) * 32 + 16 * t + r16];
          wf[t] = __builtin_bit_cast(bf16x8, w);
        }
        {
          const int id = loc * 4 + wid;
          const unsigned blk = tm * 16 + gp;
          const unsigned o0 = wid == 0 ? WS_O + blk * 16384u : wid == 1 ? WS_H + blk * 16384u : wid == 2 ? WS_KVP + (gp * 16 + tm) * KVP_BYTES : WS_HST + blk * 1024u;
          const char* a0 = WS + o0;
          const char* a1 = (const char*)W1 + (size_t)id * 57344;
          const char* a2 = (const char*)W2 + (size_t)id * 57344;
          const char* a3 = (const char*)OUT + (size_t)id * 32768;
          const char* a4 = (const char*)WO + (size_t)id * 16384;
          const char* a5 = WS + (WS_H1 + blk * 57344u + wid * 16384u);
          unsigned d;
          asm volatile("s_load_dword %0, %1, 0x0\n\ts_load_dword %0, %2, 0x0\n\ts_load_dword %0, %3, 0x0\n\ts_load_dword %0, %4, 0x0\n\t"
                       "s_load_dword %0, %5, 0x0\n\ts_load_dword %0, %6, 0x0\n\ts_waitcnt lgkmcnt(0)"
                       : "=&s"(d) : "s"(a0), "s"(a1), "s"(a2), "s"(a3), "s"(a4), "s"(a5) : "memory");
        }
        tag = read_tag(WS);
      }
      if (wid == 0) STAMP(4, 0);
      f32x4 acc[4][6];
      mma_loop<192, NS, NT, 4, 6, false, false>(smem, acc, wm * 64, lane, [&](int j) { return (j >> 1) * 4 + hh * 2 + (j & 1); }, []() {}, [](int) {});
      if (wid == 0) STAMP(0, 2);
      if (wm == 0) {
        *(LAS bf16x8*)(smem + L_WF + hh * 2048 + lane * 16) = wf[0];
        *(LAS bf16x8*)(smem + L_WF + hh * 2048 + 1024 + lane * 16) = wf[1];
      }
      st4(xk, acc[2][2], acc[2][3], acc[3][2], acc[3][3]);
      st4(smem + L_XV + wid * 4096 + lane * 16, acc[2][4], acc[2][5], acc[3][4], acc[3][5]);
      lbar();
      if (wid == 0) STAMP(4, 4);
      st4(smem + L_XQ + wid * 4096 + lane * 16, acc[2][0], acc[2][1], acc[3][0], acc[3][1]);
#pragma unroll
      for (int t = 0; t < 2; ++t)
#pragma unroll
        for (int u = 0; u < 2; ++u) { fq[t][u] = acc[t][u]; fk[t][u] = acc[t][2 + u]; fv[t][u] = acc[t][4 + u]; }
    }
    const int rbase = wm * 64 + mi * 32, q = r16 >> 2, p = lane & 3;
    LAS const float* cs = (LAS const float*)(smem + L_CS) + hh * 32;
    LAS const f32x2* rst = (LAS const f32x2*)(smem + L_RST) + rbase;
    auto feature = [&](const f32x4 (&x)[2][2], int part, f32x4 (&e)[2][2][2]) {
      const f32x4 c0 = *(LAS const f32x4*)(cs + part * 64 + 4 * g), c1 = *(LAS const f32x4*)(cs + part * 64 + 16 + 4 * g);
      f32x4 pp[2][2];
      float sc[2];
#pragma unroll
      for (int t = 0; t < 2; ++t) {
        const f32x2 ms = rst[t * 16 + r16];
        sc[t] = ms[1] * 1.4426950408889634f;
        f32x4 y0, y1;
#pragma unroll
        for (int e_ = 0; e_ < 4; ++e_) { y0[e_] = __builtin_fmaf(-ms[0], c0[e_], x[t][0][e_]); y1[e_] = __builtin_fmaf(-ms[0], c1[e_], x[t][1][e_]); }
        const bf16x8 yb = pack_frag(y0, y1);
        pp[t][0] = MFMA16(wf[0], yb, ((f32x4){0.f, 0.f, 0.f, 0.f}));
        pp[t][1] = MFMA16(wf[1], yb, ((f32x4){0.f, 0.f, 0.f, 0.f}));
      }
      dual_softmax(pp, sc, e);
    };
    if (wid == 0) STAMP(4, 5);
    {
      const float cv0 = cs[128 + r16], cv1 = cs[144 + r16];
#pragma unroll
      for (int t = 0; t < 2; ++t) {
        const f32x4 s01 = *(LAS const f32x4*)(rst + t * 16 + 4 * g), s23 = *(LAS const f32x4*)(rst + t * 16 + 4 * g + 2);
        const float mv[4] = {s01[0], s01[2], s23[0], s23[2]}, rv[4] = {s01[1], s01[3], s23[1], s23[3]};
#pragma unroll
        for (int e_ = 0; e_ < 4; ++e_) {
          fv[t][0][e_] = __builtin_fmaf(-mv[e_], cv0, fv[t][0][e_]) * rv[e_];
          fv[t][1][e_] = __builtin_fmaf(-mv[e_], cv1, fv[t][1][e_]) * rv[e_];
        }
      }
      LAS char* vd = smem + (loader ? L_XV + lw * 4096 : L_VF + lw * 2048) + lane * 16;
      *(LAS bf16x8*)vd = pack_frag(fv[0][0], fv[1][0]);
      *(LAS bf16x8*)(vd + 1024) = pack_frag(fv[0][1], fv[1][1]);
      f32x4 e[2][2][2];
      feature(fk, 1, e);
      LAS char* wr = smem + L_PHI + hh * 17408 + (rbase + r16) * 136 + g * 8;
#pragma unroll
      for (int w = 0; w < 2; ++w)
#pragma unroll
        for (int t = 0; t < 2; ++t)
#pragma unroll
          for (int u = 0; u < 2; ++u)
            *(LAS u32x2*)(wr + t * 16 * 136 + w * 64 + u * 32) = (u32x2){pk2(e[w][t][u][0], e[w][t][u][1]), pk2(e[w][t][u][2], e[w][t][u][3])};
    }
    if (wid == 0) STAMP(4, 7);
    if (wid == 4) STAMP(4, 9);
    lbar();
    __builtin_amdgcn_s_setprio(0);
    if (wid == 0) STAMP(4, 8);
    bf16x8 pf[2][2];
    auto qpath = [&]() {
      f32x4 e[2][2][2];
      feature(fq, 0, e);
#pragma unroll
      for (int t = 0; t < 2; ++t)
#pragma unroll
        for (int w = 0; w < 2; ++w) { pf[t][w] = pack_frag(e[w][t][0], e[w][t][1]); asm volatile("" : "+v"(pf[t][w])); }
    };
    if (loader) {
      ld4(smem + L_XQ + lw * 4096 + lane * 16, fq);
      qpath();
      load_wtile<224, 4, 7>(smem + L_WT, mkrsrc(W1, D_ * FF_ * 2), tm * 64 * (FF_ * 2) + gp * 448, FF_ * 2, 128, lw, lane);
    } else {
      f32x4 k00 = {0.f, 0.f, 0.f, 0.f}, k01 = k00, k10 = k00, k11 = k00, z0 = k00, z1 = k00;
      const s16x8 one_ = {0x3f80, 0x3f80, 0x3f80, 0x3f80, 0x3f80, 0x3f80, 0x3f80, 0x3f80};
      LAS const char* rd = smem + L_PHI + hh * 17408 + (4 * g + q) * 136 + wm * 64 + p * 8;
#pragma unroll
      for (int u = 0; u < 4; ++u) {
        const bf16x8 pa0 = ld_tr2(rd + 32 * u * 136, rd + (32 * u + 16) * 136), pa1 = ld_tr2(rd + 32 * u * 136 + 32, rd + (32 * u + 16) * 136 + 32);
        LAS const char* vp = smem + ((u & 1) ? L_XV + ((u >> 1) * 2 + hh) * 4096 : L_VF + ((u >> 1) * 2 + hh) * 2048) + lane * 16;
        const bf16x8 v0 = ld_b128(vp), v1 = ld_b128(vp + 1024);
        k00 = MFMA16(pa0, v0, k00); k10 = MFMA16(pa1, v0, k10);
        k01 = MFMA16(pa0, v1, k01); k11 = MFMA16(pa1, v1, k11);
        z0 = MFMA16(pa0, __builtin_bit_cast(bf16x8, one_), z0); z1 = MFMA16(pa1, __builtin_bit_cast(bf16x8, one_), z1);
      }
      const int kb = kvp_grp + tm * KVP_BYTES;
      st128(pack_xy(k00, k10), rws, kb + hh * 4096 + wm * 2048 + lane * 16);
      st128(pack_xy(k01, k11), rws, kb + hh * 4096 + wm * 2048 + 1024 + lane * 16);
      st128(__builtin_bit_cast(u32x4, r16 == 0 ? z0 : z1), rws, r16 < 2 ? kb + 8192 + hh * 256 + wm * 128 + g * 32 + r16 * 16 : (int)0x80000000);
      if (wid == 0) STAMP(0, 3);
      wait_vm<0>();
    }
    lbar();
    if (tid == 0) set_flag(ctl, C_KV + gp, tm, tag);
    if (wid == 0) STAMP(0, 4);
    if (loader) {
      Loader<64, 5, 16, false, 1> L2;
      L2.init(smem, rws, mkrsrc(WO, D_ * D_ * 2), WS_O + tm * 128 * D_ * 2, 0, gp * 128, D_ * 2, 128, lw, lane, 0);
      L2.issueB(0); L2.issueB(1); L2.issueB(2); L2.issueB(3); L2.issueB(4);
    } else {
      qpath();
    }
    FastPoll fpk = {0u, 0u, 0u, 0u};
    if (wid == 0) {
      STAMP(0, 5);
      fpk.run(ctl, C_KV + gp, 0, 16, tag, lane);
    }
    bar();
    if (wid == 0) STAMP(0, 6);
    {
      u32x4 tz[16];
      if (wid == 7) {
#pragma unroll
        for (int j = 0; j < 16; ++j) tz[j] = ld128(rws, lane < 32 ? kvp_grp + 8192 + lane * 16 : 0x80000000u, j * KVP_BYTES);
      }
      u32x4 t[16];
#pragma unroll
      for (int j = 0; j < 16; ++j) t[j] = ld128(rws, kvp_grp + tid * 16, j * KVP_BYTES);
      if (wid == 7) {
        f32x4 z = __builtin_bit_cast(f32x4, tz[0]);
#pragma unroll
        for (int j = 1; j < 16; ++j) z += __builtin_bit_cast(f32x4, tz[j]);
        if (lane < 32) *(LAS u32x2*)(smem + L_KVT + 8192 + lane * 8) = (u32x2){pk2(z[0], z[1]), pk2(z[2], z[3])};
      }
      f32x4 x = {0.f, 0.f, 0.f, 0.f}, y = x;
#pragma unroll
      for (int j = 0; j < 16; ++j) add_bf8(x, y, t[j]);
      *(LAS u32x4*)(smem + L_KVT + tid * 16) = pack_xy(x, y);
    }
    fpk.keep();
    lbar();
    if (wid == 0) STAMP(0, 7);
    {
      bf16x8 ka[2][2], za[2];
#pragma unroll
      for (int w = 0; w < 2; ++w) {
        ka[w][0] = ld_b128(smem + L_KVT + hh * 4096 + w * 2048 + lane * 16);
        ka[w][1] = ld_b128(smem + L_KVT + hh * 4096 + w * 2048 + 1024 + lane * 16);
        za[w] = ld_b128(smem + L_KVT + 8192 + hh * 128 + w * 64 + g * 16);
      }
#pragma unroll
      for (int t = 0; t < 2; ++t) {
        f32x4 o0 = {0.f, 0.f, 0.f, 0.f}, o1 = o0, dn = o0;
#pragma unroll
        for (int w = 0; w < 2; ++w) { dn = MFMA16(za[w], pf[t][w], dn); o0 = MFMA16(ka[w][0], pf[t][w], o0); o1 = MFMA16(ka[w][1], pf[t][w], o1); }
        const float rd = 1.0f / (dn[0] + 1e-6f);
#pragma unroll
        for (int e_ = 0; e_ < 4; ++e_) { o0[e_] *= rd; o1[e_] *= rd; }
        swap_pair(o0, o1);
        st128(pack_xy(o0, o1), rws, WS_O + (tm * 32 + head) * 8192 + (rbase + t * 16 + r16) * 64 + cswz);
      }
    }
    wait_vm<0>();
    lbar();
    if (tid == 0) set_flag(ctl, C_O + tm, gp, tag);
    if (wid == 0) STAMP(0, 8);
  }

  const int kh = gp >> 3;
  if (loader) {
    __builtin_amdgcn_s_setprio(3);
    relane();
    Loader<64, 5, 16, false, 1> L2;
    L2.init(smem, rws, mkrsrc(WO, D_ * D_ * 2), WS_O + tm * 128 * D_ * 2, 0, gp * 128, D_ * 2, 128, lw, lane, 0);
    colsum_tile<224>(smem + L_WT, rws, WS_CS3 + (gp * 16 + tm) * 896, lw, lane);
    FastPoll fp;
    poll4(fp, smem, 1, lw, ctl, C_O + tm, 0, 16, tag, lane);
    if (wid == 4) STAMP(1, 0);
    L2.issueA(0); L2.issueA(1); L2.issueA(2); L2.issueA(3); L2.issueA(4);
    if (bid == 0 && wid == 4 && lane == 0) __hip_atomic_store(ctl + C_EPOCH * 64, tag, __ATOMIC_RELAXED, __HIP_MEMORY_SCOPE_AGENT);
    wait_vm<16>(); fp.keep(); lbar();
    wait_vm<12>(); lbar();
    L2.tn = 5;
    L2.step<8>();
    L2.step<4 + 6>();
    L2.steps(9);
    L2.finish();
    if (lane == 0) set_flag(ctl, C_CS3 + gp, tm * 4 + lw, tag);
    const rsrc_t r2 = mkrsrc(W2, FF_ * D_ * 2);
    {
      relane();
      Loader<224, 3, 16, false> L;
      L.init(smem, rws, mkrsrc(W1, D_ * FF_ * 2), WS_H + tm * 128 * D_ * 2, 0, gp * 448, FF_ * 2, 128, lw, lane, gp);
      bar();
      L.issueB(0); L.issueB(1);
      wait_vm<7>(); bar();
      L.issueB(2);
      bar();
      if (wid == 4 && lane == 0) set_flag(ctl, C_H + tm, gp, tag);
      poll4(fp, smem, 2, lw, ctl, C_H + tm, 0, 16, tag, lane);
      if (wid == 4) STAMP(2, 0);
      L.issueA(1); L.issueA(2);
      u32x4 gt[4], hs[4];
#pragma unroll
      for (int j = 0; j < 4; ++j) hs[j] = ld128(rws, WS_HST + tm * 16384 + lw * 4096 + lane * 16, j * 1024);
      const unsigned seen = peek(ctl, C_CS3 + gp, lane);
      L.tn = 3;
      L.step<4 + 5>();
      fp.keep();
      L.step<5 + 11>();
      confirm(seen, ctl, C_CS3 + gp, tag, lane);
#pragma unroll
      for (int j = 0; j < 4; ++j) gt[j] = ld128(rws, WS_CS3 + (gp * 16 + lw * 4) * 896 + lane * 16, j * 896);
      *(LAS f32x4*)(smem + L_CSS + 4096 + lw * 1024 + lane * 16) = sum4(hs);
      L.step();
      if (lw == 1) {
        const f32x4 sum = lds_sum4(smem + L_CSS + 4096 + lane * 16);
        const float m0 = sum[0] * (1.0f / D_), m1 = sum[2] * (1.0f / D_);
        const float v0 = __builtin_fmaxf(sum[1] * (1.0f / D_) - m0 * m0, 0.f), v1 = __builtin_fmaxf(sum[3] * (1.0f / D_) - m1 * m1, 0.f);
        *(LAS f32x4*)(smem + L_RST + lane * 16) = (f32x4){m0, 1.0f / __builtin_sqrtf(v0 + LN_EPS), m1, 1.0f / __builtin_sqrtf(v1 + LN_EPS)};
      }
      L.step();
      *(LAS f32x4*)(smem + L_CSS + lw * 1024 + lane * 16) = sum4(gt);
      L.step();
      if (lw == 0) *(LAS f32x4*)(smem + L_CS + lane * 16) = lds_sum4(smem + L_CSS + lane * 16);
      const int stag = (loc & 7) < 6 ? (loc & 7) : 6;
      L.steps(stag);
      touch_lines(smem, r2, bid * 28672, lane < 56 ? lw * 56 + lane : 4096, 224);
      L.step();
      L.step<12>();
      L.steps(6 - stag);
      L.finish();
    }
    {
      relane();
      Loader<128, 4, 31, false, 1> L;
      L.kh = kh;
      L.init(smem, rws, r2, WS_H1 + tm * 112 * 8192, 0, gp * 128, D_ * 2, kh ? -1024 : 1024, lw, lane, gp & 7);
      lbar();
      L.issueB(0); L.issueB(1); L.issueB(2); L.issueB(3);
      wait_vm<12>(); bar();
      if (wid == 4 && lane == 0) set_flag(ctl, C_H1 + tm, gp, tag);
      wait_vm<8>(); bar();
      wait_vm<4>(); bar();
      L.issueB(4);
      poll4(fp, smem, 3, lw, ctl, C_H1 + tm, kh * 8, 8, tag, lane);
      if (wid == 4) STAMP(3, 0);
      L.issueA(3); L.issueA(4);
      L.tn = 5;
      L.step<4>();
      fp.keep();
      L.steps(3);
      if (wid == 4 && lane == 0) set_flag(ctl, C_H1B + tm, gp, tag);
      L.steps(5);
      const unsigned seen = peek(ctl, C_H1B + tm, lane, 16);
      L.steps(3);
      confirm(seen, ctl, C_H1B + tm, tag, lane, 16);
      L.steps(8);
      L.init_half(gp * 128, D_ * 2, lw, lane);
      wait_vm<8>(); bar(); L.issueH(25);
      wait_vm<6>(); bar(); L.issueH(26);
#pragma unroll 1
      for (int t = 27; t <= 30; ++t) { wait_vm<6>(); bar(); L.issueH(t); }
      wait_vm<6>(); bar();
      wait_vm<0>(); bar();
      bar();
    }
  } else {
    relane();
    {
      const int row = tm * 128 + wid * 32 + r16, col = gp * 64 + cpair;
      u32x4 res[2][2];
#pragma unroll
      for (int i = 0; i < 2; ++i)
#pragma unroll
        for (int j = 0; j < 2; ++j) res[i][j] = *(const u32x4*)(X + (size_t)(row + i * 16) * D_ + col + j * 32);
      f32x4 acc[2][4];
      mma_loop<64, 5, 16, 2, 4, true, false>(smem, acc, wid * 32, lane, [](int j) { return j; }, []() {}, [](int) {});
      if (wid == 0) STAMP(1, 2);
#pragma unroll
      for (int i = 0; i < 2; ++i) {
        float s = 0.f, qq = 0.f;
#pragma unroll
        for (int j = 0; j < 2; ++j) {
          f32x4 x = acc[i][2 * j], y = acc[i][2 * j + 1];
          swap_pair(x, y);
          add_bf8(x, y, res[i][j]);
          const u32x4 o = pack_xy(x, y);
          st128(o, rws, WS_H + (tm * 32 + gp * 2 + j) * 8192 + (wid * 32 + i * 16 + r16) * 64 + cswz);
          *(LAS u32x4*)(smem + j * 8192 + (wid * 32 + i * 16 + r16) * 64 + cswz) = o;
#pragma unroll
          for (int e = 0; e < 4; ++e) { const float lo = bflo(o[e]), hi = bfhi(o[e]); s += lo + hi; qq = __builtin_fmaf(lo, lo, __builtin_fmaf(hi, hi, qq)); }
        }
        s = x32_sum(x16_sum(s)); qq = x32_sum(x16_sum(qq));
        if (g == 0) __builtin_amdgcn_raw_buffer_store_b64(__builtin_bit_cast(u32x2, (f32x2){s, qq}), rws, WS_HST + ((tm * 16 + gp) * 128 + wid * 32 + i * 16 + r16) * 8, 0, SC1);
      }
      if (wid == 0) STAMP(1, 3);
    }
    const int wm = wid >> 1, wn = wid & 1;
    relane();
    {
      const int par = gp & 1;
      auto slab3 = [&](int a) { return wn * 4 + (a + par == 3 ? 0 : a + par); };
      auto ntile3 = [&](int j) { return j < 6 ? slab3(j >> 1) * 2 + (j & 1) : 6 + wn; };
      f32x4 acc[4][7];
      mma_loop<224, 3, 16, 4, 7, false, true>(smem, acc, wm * 64, lane, ntile3, []() { wait_vm<0>(); }, [](int) {});
      if (wid == 0) STAMP(2, 2);
      relane();
      f32x4 cs[7];
#pragma unroll
      for (int j = 0; j < 7; ++j) cs[j] = *(LAS const f32x4*)(smem + L_CS + (ntile3(j) * 16 + g * 4) * 4);
      float fa[4], fb[4];
#pragma unroll
      for (int i = 0; i < 4; ++i) {
        const f32x2 ms = *(LAS const f32x2*)(smem + L_RST + (wm * 64 + i * 16 + r16) * 8);
        fa[i] = ms[1]; fb[i] = -ms[0] * ms[1];
      }
      auto act = [&](int i, int j) {
        f32x4 y;
#pragma unroll
        for (int e = 0; e < 4; ++e) y[e] = __builtin_fmaxf(__builtin_fmaf(acc[i][j][e], fa[i], fb[i] * cs[j][e]), 0.f);
        return y;
      };
      const int hb = WS_H1 + (tm * 112 + gp * 7) * 8192;
#pragma unroll
      for (int a = 0; a < 3; ++a) {
        const int sl = slab3(a);
#pragma unroll
        for (int i = 0; i < 4; ++i) {
          f32x4 x = act(i, 2 * a), y = act(i, 2 * a + 1);
          swap_pair(x, y);
          const u32x4 o = pack_xy(x, y);
          const int ro = (wm * 64 + i * 16 + r16) * 64 + cswz;
          st128(o, rws, hb + sl * 8192 + ro);
          if (a < 2 || (unsigned)(sl - par) < 6u) *(LAS u32x4*)(smem + (sl - par) * 8192 + ro) = o;
        }
        asm volatile("" ::: "memory");
      }
#pragma unroll
      for (int i = 1; i < 4; i += 2) {
        f32x4 x = act(i - 1, 6), y = act(i, 6);
        swap_pair(x, y);
        const u32x4 o = pack_xy(x, y);
        const int r3 = (wm * 64 + (i - 1 + (g & 1)) * 16 + r16) * 64 + (((2 * wn + (g >> 1)) ^ aswz(r16)) * 16);
        st128(o, rws, hb + 3 * 8192 + r3);
        *(LAS u32x4*)(smem + (3 - par) * 8192 + r3) = o;
      }
      if (wid == 0) STAMP(2, 5);
      wait_vm<6>();
      if (wid == 0) STAMP(2, 3);
    }
    relane();
    {
      const int row = tm * 128 + wm * 64 + r16, col = gp * 64 + wn * 32 + cpair;
      u32x4 res[4], t[4];
      unsigned seen = 0;
      f32x4 acc[4][4];
      mma_loop<128, 4, 31, 4, 4, true, true>(smem, acc, wm * 64, lane, [&](int j) { return (j >> 1) * 4 + wn * 2 + (j & 1); }, [&]() {
        wait_vm<0>();
        if (wid == 0) STAMP(2, 4);
#pragma unroll
        for (int i = 0; i < 4; ++i) res[i] = ld128(rws, WS_H + (tm * 32 + gp * 2 + wn) * 8192 + (wm * 64 + i * 16 + r16) * 64 + cswz, 0);
      }, [&](int what) {
        if (what == 0) {
          const int pb = WS_P4 + (tm * 16 + gp) * 16384 + wid * 4096 + lane * 16;
#pragma unroll
          for (int i = 0; i < 4; ++i) st128(pack_xy(acc[i][2], acc[i][3]), rws, pb + i * 1024);
        } else if (what == 1) {
          wait_vm<0>();
          if (lane == 0) set_flag(ctl, C_P4 + tm, gp * 4 + wid, tag);
          if (wid == 0) STAMP(3, 3);
        } else if (what == 2) {
          seen = __hip_atomic_load(ctl + (C_P4 + tm) * 64 + (gp ^ 8) * 4 + wid, __ATOMIC_RELAXED, __HIP_MEMORY_SCOPE_AGENT);
        } else {
          asm volatile("" : "+v"(seen));
          if (!wave_all(seen == tag)) poll(ctl, C_P4 + tm, (gp ^ 8) * 4 + wid, 1, tag, lane);
          acquire();
          if (wid == 0) STAMP(3, 4);
          const int qb = WS_P4 + (tm * 16 + (gp ^ 8)) * 16384 + wid * 4096 + lane * 16;
#pragma unroll
          for (int i = 0; i < 4; ++i) t[i] = ld128(rws, qb + i * 1024, 0);
        }
      });
      if (wid == 0) STAMP(3, 2);
#pragma unroll
      for (int i = 0; i < 4; ++i) {
        f32x4 x = acc[i][0], y = acc[i][1];
        add_bf8(x, y, t[i]);
        swap_pair(x, y);
        add_bf8(x, y, res[i]);
        *(u32x4*)(OUT + (size_t)(row + i * 16) * D_ + col) = pack_xy(x, y);
      }
      if (wid == 0) STAMP(3, 5);
    }
  }
#endif
}
#if defined(__HIP_DEVICE_COMPILE__)
#pragma clang attribute pop
#endif

extern "C" void kernel_launch(void* const* d_in, const int* in_sizes, int n_in, void* d_out, int out_size, void* d_ws, size_t ws_size,
                              hipStream_t stream) {
  (void)hipFuncSetAttribute((const void*)block_kernel, hipFuncAttributeMaxDynamicSharedMemorySize, LDS_BYTES);
  block_kernel<<<256, 512, LDS_BYTES, stream>>>((const u16*)d_in[0], (const u16*)d_in[1], (const u16*)d_in[2], (const u16*)d_in[3],
                                                (const u16*)d_in[5], (const u16*)d_in[6], (u16*)d_out, (char*)d_ws);
}
```

```cpp
#include <hip/hip_runtime.h>
#include <hip/hip_bf16.h>
#include <type_traits>

#define LAS __attribute__((address_space(3)))
#define GAS __attribute__((address_space(1)))
#define DI __device__ __forceinline__
typedef __bf16 bf16x8 __attribute__((ext_vector_type(8)));
typedef __bf16 bf16x2 __attribute__((ext_vector_type(2)));
typedef short s16x4 __attribute__((ext_vector_type(4)));
typedef short s16x8 __attribute__((ext_vector_type(8)));
typedef float f32x2 __attribute__((ext_vector_type(2)));
typedef float f32x4 __attribute__((ext_vector_type(4)));
typedef unsigned u32x2 __attribute__((ext_vector_type(2)));
typedef unsigned u32x4 __attribute__((ext_vector_type(4)));
typedef unsigned short u16;

constexpr int S_ = 2048, D_ = 1024, FF_ = 3584;
constexpr float LN_EPS = 1e-5f;

constexpr int C_EPOCH = 0, C_TMO = 1, C_ST = 2, C_CS1 = 18, C_CS3 = 34, C_KV = 50, C_O = 66, C_H = 82, C_H1 = 98, C_P4 = 114, C_H1B = 130, C_LINES = 146;
constexpr unsigned WS_XST = 40960;
constexpr unsigned WS_CS1 = WS_XST + S_ * 8;
constexpr unsigned WS_CS3 = WS_CS1 + 16 * 16 * 192 * 4;
constexpr unsigned WS_HST = WS_CS3 + 16 * 16 * 224 * 4;
constexpr unsigned KVP_BYTES = 8192 + 512;
constexpr unsigned WS_KVP = WS_HST + S_ * 16 * 8;
#define BLOCKED 2
constexpr unsigned WS_O = WS_KVP + 256 * KVP_BYTES;
constexpr unsigned WS_H = WS_O + S_ * D_ * 2;
constexpr unsigned WS_H1 = WS_H + S_ * D_ * 2;
constexpr unsigned WS_P4 = WS_O, WS_END = WS_H1 + S_ * FF_ * 2;
static_assert(C_LINES * 256 <= (int)WS_XST && WS_O % 256 == 0 && WS_KVP % 256 == 0, "");

constexpr int L_WT = 120 * 1024;
constexpr int L_TAB = 149 * 1024;
constexpr int L_DUMMY = L_TAB;
constexpr int L_RST = L_TAB + 256;
constexpr int L_CS = L_RST + 1024;
constexpr int L_CSS = L_CS + 1024;
constexpr int L_GO = L_CSS + 8192;
constexpr int LDS_BYTES = L_GO + 16;
static_assert(LDS_BYTES <= 160 * 1024, "");

#define STAMP(k, i) do {} while (0)
#define TS_DECL
#define TS(t)
#define TS_OUT(k)

#if defined(__HIP_DEVICE_COMPILE__)
#pragma clang attribute push(__attribute__((target("no-packed-fp32-ops"))), apply_to = function)
typedef __amdgpu_buffer_rsrc_t rsrc_t;
DI float u2f(unsigned u) { return __builtin_bit_cast(float, u); }
DI unsigned f2u(float f) { return __builtin_bit_cast(unsigned, f); }
DI bool wave_all(bool p) { return __builtin_amdgcn_ballot_w64(p) == ~0ull; }
#define CODE_AHEAD 48640
#define PSLEEP "2"
#define WO1 9
#define BDEPTH 3
#define PFD 8
#define B3DELAY 48
constexpr int SC1 = 16;
template <int N> DI void wait_vm() { asm volatile("s_waitcnt vmcnt(%0)" ::"n"(N) : "memory"); }
DI void wait_lgkm0() { asm volatile("s_waitcnt lgkmcnt(0)" ::: "memory"); }
DI void bar() { asm volatile("" ::: "memory"); __builtin_amdgcn_s_barrier(); asm volatile("" ::: "memory"); }
DI void lbar() { wait_lgkm0(); bar(); }
DI unsigned pk2(float a, float b) { f32x2 v = {a, b}; return __builtin_bit_cast(unsigned, __builtin_convertvector(v, bf16x2)); }
DI float bflo(unsigned u) { return u2f(u << 16); }
DI float bfhi(unsigned u) { return u2f(u & 0xffff0000u); }
DI bf16x8 ld_b128(LAS const char* p) { return *(LAS const bf16x8*)p; }
DI bf16x8 ld_tr2(LAS const char* p0, LAS const char* p1) {
  s16x4 a = __builtin_amdgcn_ds_read_tr16_b64_v4i16((LAS s16x4*)p0);
  s16x4 b = __builtin_amdgcn_ds_read_tr16_b64_v4i16((LAS s16x4*)p1);
  return __builtin_bit_cast(bf16x8, __builtin_shufflevector(a, b, 0, 1, 2, 3, 4, 5, 6, 7));
}
DI float x32_sum(float v) { auto r = __builtin_amdgcn_permlane32_swap(f2u(v), f2u(v), false, false); return u2f(r[0]) + u2f(r[1]); }
DI float x16_sum(float v) { auto r = __builtin_amdgcn_permlane16_swap(f2u(v), f2u(v), false, false); return u2f(r[0]) + u2f(r[1]); }
template <int CTRL> DI float dpp_add(float v) { return v + u2f(__builtin_amdgcn_update_dpp(0, (int)f2u(v), CTRL, 0xf, 0xf, true)); }
DI float half_sum(float v) {
  v = dpp_add<0xB1>(v);
  v = dpp_add<0x4E>(v);
  v = dpp_add<0x141>(v);
  v = dpp_add<0x140>(v);
  return x16_sum(v);
}
#define MFMA16(a, b, c) __builtin_amdgcn_mfma_f32_16x16x32_bf16((a), (b), (c), 0, 0, 0)
DI rsrc_t mkrsrc(const void* p, unsigned bytes) { return __builtin_amdgcn_make_buffer_rsrc((void*)p, 0, bytes, 0x00020000); }
DI void st128(u32x4 v, rsrc_t r, int off) { __builtin_amdgcn_raw_buffer_store_b128(v, r, off, 0, SC1); }
DI u32x4 ld128(rsrc_t r, int off, int soff) { return __builtin_amdgcn_raw_buffer_load_b128(r, off, soff, SC1); }

DI unsigned read_tag(const char* ws) {
  unsigned e;
  asm volatile("s_load_dword %0, %1, 0x0\n\ts_waitcnt lgkmcnt(0)" : "=s"(e) : "s"(ws) : "memory");
  return e + 1u;
}
DI void set_flag(GAS unsigned* ctl, int line, int word, unsigned tag) { __hip_atomic_store(ctl + line * 64 + word, tag, __ATOMIC_RELAXED, __HIP_MEMORY_SCOPE_AGENT); }
DI void acquire() {
  asm volatile("" ::: "memory");
}
DI void poll(GAS unsigned* ctl, int line, int lo, int n, unsigned tag, int lane) {
  GAS unsigned* p = ctl + line * 64 + lo + (lane < n ? lane : 0);
  unsigned spins = 0;
  while (!wave_all(__hip_atomic_load(p, __ATOMIC_RELAXED, __HIP_MEMORY_SCOPE_AGENT) == tag)) {
    __builtin_amdgcn_s_sleep(1);
    if (++spins > (1u << 17)) { if (lane == 0) __hip_atomic_store(ctl + C_TMO * 64, (unsigned)line, __ATOMIC_RELAXED, __HIP_MEMORY_SCOPE_AGENT); break; }
  }
  acquire();
}
struct FastPoll {
  unsigned a, b, c, d;
  DI void run(GAS unsigned* ctl, int line, int lo, int n, unsigned tag, int lane) {
    GAS unsigned* p = ctl + line * 64 + lo + (lane < n ? lane : 0);
    unsigned cnt = 1u << 17;
#define FP_LD(r) "global_load_dword %" #r ", %5, off sc1\n\ts_sleep " PSLEEP "\n\t"
#define FP_CK(r) "s_waitcnt vmcnt(3)\n\tv_cmp_eq_u32 vcc, %6, %" #r "\n\ts_cmp_eq_u64 vcc, -1\n\ts_cbranch_scc1 2f\n\t" FP_LD(r)
    asm volatile(FP_LD(0) FP_LD(1) FP_LD(2) FP_LD(3) "1:\n\t" FP_CK(0) FP_CK(1) FP_CK(2) FP_CK(3)
                 "s_sub_u32 %4, %4, 1\n\ts_cbranch_scc0 1b\n\t2:"
                 : "=&v"(a), "=&v"(b), "=&v"(c), "=&v"(d), "+s"(cnt) : "v"(p), "s"(tag) : "vcc", "scc", "memory");
#undef FP_LD
#undef FP_CK
  }
  DI void keep() const { asm volatile("" :: "v"(a), "v"(b), "v"(c), "v"(d)); }
};
DI void poll4(FastPoll& fp, LAS char* smem, unsigned go, int lw, GAS unsigned* ctl, int line, int lo, int n, unsigned tag, int lane) {
  LAS volatile unsigned* w = (LAS volatile unsigned*)(smem + L_GO);
  if (lw == 0) {
    fp.run(ctl, line, lo, n, tag, lane);
    *w = go;
  } else {
    fp.a = fp.b = fp.c = fp.d = 0u;
    unsigned spins = 0;
    while (__builtin_amdgcn_readfirstlane(*w) != go && ++spins < (1u << 24)) __builtin_amdgcn_s_sleep(1);
  }
  asm volatile("" ::: "memory");
}

template <int BN> DI constexpr int bswz(int k) {
  return BN == 224 ? (((k >> 3) & 1) << 1)
       : BN == 128 ? (((k & 3) | (((k >> 3) & 1) << 2)) << 1)
                   : ((((k >> 1) & 1) | (((k >> 3) & 1) << 1)) << 1);
}

template <int BN, int NS> DI int bslot(int s) {
  if (BN == 224) return ((0x4c3078 >> (8 * s)) & 255) << 10;
  if (BN == 128) return ((0x78504068 >> (8 * s)) & 255) << 10;
  return NS * 16384 + s * BN * 128;
}
DI constexpr int wo_slot(int t) { return t < 5 ? 81920 + t * 8192 : t < 15 ? (t - 5) * 8192 : L_CSS; }

template <int BN, int NS, int NT, bool AROW, int LAG = 0>
struct Loader {
  static constexpr int PA = 4, PB = BN / 32, PW = PA + PB, BST = BN * 128, CPR = BN / 8;
  static_assert(NS * (16384 + BST) <= L_TAB, "");
  LAS char* dst0; rsrc_t ra, rb; int va[PA], vb[PB], vh[2]; int ldb64, own0, tn;
  static constexpr int NOWN = BN == 128 ? 3 : BN == 224 ? 1 : 0;
  int kh;
  DI int ktile(int t) const {
    if (AROW) return (t + own0) & (NT - 1);
    if (BN != 128) return t < NOWN ? own0 + t : (t - NOWN < own0 ? t - NOWN : t);
    const int pi = t < 17 ? (t - 3) >> 1 : t - 17, p = t < 3 ? own0 : pi + (pi >= own0 ? 1 : 0);
    const int k = t >= 24 ? 7 * ((t >= 28 ? t - 3 : t) - 24) + 3 : ((7 * p + 1) >> 1) + (t < 3 ? t : t < 17 ? 2 * ((t - 3) & 1) : 1);
    return k + ((t >= 28) != (kh != 0) ? 28 : 0);
  }
  DI void issueA(int t) {
    const int kk = ktile(t), slot = t % NS;
#pragma unroll
    for (int i = 0; i < PA; ++i)
      __builtin_amdgcn_raw_ptr_buffer_load_lds(ra, dst0 + slot * 16384 + i * 4096, 16, va[i], kk * (AROW ? 128 : 16384), 0, AROW ? 0 : SC1);
  }
  DI void issueB(int t) {
    const int kk = ktile(t), slot = t % NS;
#pragma unroll
    for (int i = 0; i < PB; ++i) __builtin_amdgcn_raw_ptr_buffer_load_lds(rb, dst0 + bslot<BN, NS>(slot) + i * 4096, 16, vb[i], kk * ldb64, 0, 0);
  }
  DI void issueH(int t) {
    issueA(t);
    const int kk = ktile(t), slot = t % NS;
#pragma unroll
    for (int i = 0; i < 2; ++i) __builtin_amdgcn_raw_ptr_buffer_load_lds(rb, dst0 + bslot<BN, NS>(slot) + i * 4096, 16, vh[i], kk * ldb64, 0, 0);
  }
  DI void init(LAS char* smem, rsrc_t ra_, rsrc_t rb_, int a_off, int lda_b, int b_off, int ldb_b, int segb, int lw, int lane, int own) {
    dst0 = smem + lw * 1024; ra = ra_; rb = rb_; ldb64 = 64 * ldb_b; own0 = own; tn = NS - 1;
#pragma unroll
    for (int i = 0; i < PA; ++i) {
      const int ins = lw + i * 4, r = ins * 8 + (lane >> 3), c = (lane & 7) ^ ((r >> 1) & 7);
      va[i] = AROW ? a_off + r * lda_b + c * 16 : a_off + ins * 1024 + lane * 16;
    }
#pragma unroll
    for (int i = 0; i < PB; ++i) {
      const int ch = (lw + i * 4) * 64 + lane, k = ch / CPR, c = (ch % CPR) ^ bswz<BN>(k);
      vb[i] = b_off + k * ldb_b + (c >> 3) * segb + (c & 7) * 16;
    }
  }
  DI void init_half(int b_off, int ldb_b, int lw, int lane) {
#pragma unroll
    for (int i = 0; i < 2; ++i) { const int k = (lw + i * 4) * 8 + (lane >> 3); vh[i] = b_off + k * ldb_b + (((lane & 7) ^ bswz<64>(k)) * 16); }
  }
  template <int ALLOW = (NS - 2 - LAG) * PW> DI void step() { wait_vm<ALLOW>(); lbar(); issueA(tn); issueB(tn); ++tn; }
  DI void steps(int n) {
#pragma unroll 1
    for (int t = 0; t < n; ++t) step();
  }
  DI void finish() {
    if constexpr (NS - LAG >= 5) { wait_vm<3 * PW>(); lbar(); }
    if constexpr (NS - LAG >= 4) { wait_vm<2 * PW>(); lbar(); }
    if constexpr (NS - LAG >= 3) { wait_vm<1 * PW>(); lbar(); }
    wait_vm<0>(); lbar();
  }
};
template <int BN, int I0, int I1> DI void load_wtile(LAS char* dst, rsrc_t r, int off, int ldb_b, int segb, int lw, int lane) {
#pragma unroll
  for (int i = I0; i < I1; ++i) {
    const int ch = (lw + i * 4) * 64 + lane, k = ch / (BN / 8), c = (ch % (BN / 8)) ^ bswz<BN>(k);
    __builtin_amdgcn_raw_ptr_buffer_load_lds(r, dst + (lw + i * 4) * 1024, 16, off + k * ldb_b + (c >> 3) * segb + (c & 7) * 16, 0, 0, 0);
  }
}
template <int T0, int T1> DI void load_wo(LAS char* smem, rsrc_t ro, int gp, int lw, int lane) {
  int vo[2];
#pragma unroll
  for (int i = 0; i < 2; ++i) { const int k = (lw + i * 4) * 8 + (lane >> 3); vo[i] = gp * 128 + k * (D_ * 2) + (((lane & 7) ^ bswz<64>(k)) * 16); }
#pragma unroll
  for (int t = T0; t < T1; ++t)
#pragma unroll
    for (int i = 0; i < 2; ++i) __builtin_amdgcn_raw_ptr_buffer_load_lds(ro, smem + wo_slot(t) + (lw + i * 4) * 1024, 16, vo[i], t * 64 * (D_ * 2), 0, 0);
}
DI int opaque(int v) { asm volatile("" : "+v"(v)); return v; }

DI void touch_lines(LAS char* smem, rsrc_t r, int base, int line, int nlines) {
  __builtin_amdgcn_raw_ptr_buffer_load_lds(r, smem + L_DUMMY, 4, line < nlines ? base + line * 128 : (int)0x80000000, 0, 0, 0);
}

template <int BN> DI void colsum_tile(LAS char* img, rsrc_t rws, unsigned off, int lw, int lane) {
  constexpr int RS = BN * 2;
  const int r16 = lane & 15, g = lane >> 4, q = (lane & 15) >> 2, p = lane & 3;
  const s16x8 one_ = {0x3f80, 0x3f80, 0x3f80, 0x3f80, 0x3f80, 0x3f80, 0x3f80, 0x3f80};
  LAS const char* rd[4];
#pragma unroll
  for (int i = 0; i < 4; ++i) {
    const int nt = lw + 4 * i < BN / 16 ? lw + 4 * i : lw;
    rd[i] = img + (8 * g + q) * RS + (((2 * nt + (p >> 1)) ^ bswz<BN>(8 * g + q)) * 16) + 8 * (p & 1);
  }
  s16x4 v[4][4];
#define TR4(a, b, c, d, r) "ds_read_b64_tr_b16 %" #a ", %" #r "\n\tds_read_b64_tr_b16 %" #b ", %" #r " offset:%c20\n\t" \
                           "ds_read_b64_tr_b16 %" #c ", %" #r " offset:%c21\n\tds_read_b64_tr_b16 %" #d ", %" #r " offset:%c22\n\t"
  asm volatile(TR4(0, 1, 2, 3, 16) TR4(4, 5, 6, 7, 17) TR4(8, 9, 10, 11, 18) TR4(12, 13, 14, 15, 19) "s_waitcnt lgkmcnt(0)"
               : "=&v"(v[0][0]), "=&v"(v[0][1]), "=&v"(v[0][2]), "=&v"(v[0][3]), "=&v"(v[1][0]), "=&v"(v[1][1]), "=&v"(v[1][2]), "=&v"(v[1][3]),
                 "=&v"(v[2][0]), "=&v"(v[2][1]), "=&v"(v[2][2]), "=&v"(v[2][3]), "=&v"(v[3][0]), "=&v"(v[3][1]), "=&v"(v[3][2]), "=&v"(v[3][3])
               : "v"(rd[0]), "v"(rd[1]), "v"(rd[2]), "v"(rd[3]), "i"(4 * RS), "i"(32 * RS), "i"(36 * RS) : "memory");
#undef TR4
  f32x4 c[4];
#pragma unroll
  for (int i = 0; i < 4; ++i) {
    c[i] = (f32x4){0.f, 0.f, 0.f, 0.f};
    c[i] = MFMA16(__builtin_bit_cast(bf16x8, __builtin_shufflevector(v[i][0], v[i][1], 0, 1, 2, 3, 4, 5, 6, 7)), __builtin_bit_cast(bf16x8, one_), c[i]);
    c[i] = MFMA16(__builtin_bit_cast(bf16x8, __builtin_shufflevector(v[i][2], v[i][3], 0, 1, 2, 3, 4, 5, 6, 7)), __builtin_bit_cast(bf16x8, one_), c[i]);
  }
  const f32x4 out = r16 == 0 ? c[0] : r16 == 1 ? c[1] : r16 == 2 ? c[2] : c[3];
  st128(__builtin_bit_cast(u32x4, out), rws, lw + 4 * r16 < BN / 16 ? off + (lw + 4 * r16) * 64 + g * 16 : 0x80000000u);
}
DI f32x4 sum4(u32x4 (&t)[4]) {
  asm volatile("" : "+v"(t[0]), "+v"(t[1]), "+v"(t[2]), "+v"(t[3]));
  return (__builtin_bit_cast(f32x4, t[0]) + __builtin_bit_cast(f32x4, t[1])) + (__builtin_bit_cast(f32x4, t[2]) + __builtin_bit_cast(f32x4, t[3]));
}
DI f32x4 lds_sum4(LAS const char* p) {
  return (*(LAS const f32x4*)p + *(LAS const f32x4*)(p + 1024)) + (*(LAS const f32x4*)(p + 2048) + *(LAS const f32x4*)(p + 3072));
}
DI unsigned peek(GAS unsigned* ctl, int line, int lane, int n = 64) { return __hip_atomic_load(ctl + line * 64 + (lane & (n - 1)), __ATOMIC_RELAXED, __HIP_MEMORY_SCOPE_AGENT); }
DI void confirm(unsigned seen, GAS unsigned* ctl, int line, unsigned tag, int lane, int n = 64) {
  asm volatile("" : "+v"(seen));
  if (!wave_all(seen == tag)) poll(ctl, line, 0, n, tag, lane);
  else acquire();
}

DI void mfma16(f32x4& c, const bf16x8& a, const bf16x8& b) { asm volatile("v_mfma_f32_16x16x32_bf16 %0, %1, %2, %0" : "+v"(c) : "v"(a), "v"(b)); }
DI void mfma_drain() { asm volatile("s_nop 7\n\ts_nop 7" ::: "memory"); }
template <int TM, int TN> struct Frag { s16x4 b[TN][2]; bf16x8 a[TM]; };
template <int BN, int NS, int NT, int TM, int TN, bool DEEP, bool OWN, class NMap, class Mid, class Send>
DI void mma_loop(LAS char* smem, f32x4 (&acc)[TM][TN], int row0, int lane, NMap&& nt, Mid&& mid, Send&& send) {
  constexpr int RS = BN * 2, BST = BN * 128, NL = TM + 2 * TN, NM = TM * TN;
  typedef Frag<TM, TN> F;
  const int r16 = lane & 15, g = lane >> 4, q = (lane & 15) >> 2, p = lane & 3;
  constexpr bool AROW = BN == 192;
  const int aoff = AROW ? (row0 + r16) * 128 + ((g ^ (r16 >> 1)) * 16) : row0 * 64 + lane * 16;
  const int aoff1 = AROW ? aoff ^ 64 : aoff + 8192;
  int boff[TN];
#pragma unroll
  for (int j = 0; j < TN; ++j) boff[j] = (8 * g + q) * RS + (((2 * nt(j) + (p >> 1)) ^ bswz<BN>(8 * g + q)) * 16) + 8 * (p & 1);
#pragma unroll
  for (int i = 0; i < TM; ++i)
#pragma unroll
    for (int j = 0; j < TN; ++j) { acc[i][j] = (f32x4){0.f, 0.f, 0.f, 0.f}; asm volatile("" : "+v"(acc[i][j])); }
  int boffh[2];
  auto ld1 = [&](F& f, int slot, int s, int k, int tn = TN) {
    if (k == 0 || k > 2 * tn) { const int i = k == 0 ? 0 : k - 2 * tn; f.a[i] = ld_b128(smem + slot * 16384 + (s ? aoff1 : aoff) + i * (AROW ? 2048 : 1024)); }
    else {
      const int j = (k - 1) >> 1, u = (k - 1) & 1;
      f.b[j][u] = __builtin_amdgcn_ds_read_tr16_b64_v4i16((LAS s16x4*)(smem + bslot<BN, NS>(slot) + (tn == TN ? boff[j] + (32 * s + 4 * u) * RS : boffh[j & 1] + (32 * s + 4 * u) * 128)));
    }
  };
  auto mm1 = [&](const F& f, int m, int tn = TN) {
    const int i = m / tn, j = m % tn;
    const bf16x8 b = __builtin_bit_cast(bf16x8, __builtin_shufflevector(f.b[j][0], f.b[j][1], 0, 1, 2, 3, 4, 5, 6, 7));
    if (AROW && j >= 4) mfma16(acc[i][j], f.a[i], b); else mfma16(acc[i][j], b, f.a[i]);
  };
  auto ldall = [&](F& f, int slot, int s) {
#pragma unroll
    for (int k = 0; k < NL; ++k) ld1(f, slot, s, k);
  };
  auto mmall = [&](const F& f) {
#pragma unroll
    for (int m = 0; m < NM; ++m) mm1(f, m);
  };
  auto sub = [&](const F& x, F& u, int slot, int s) {
    constexpr int SP = DEEP ? NM : (NL < NM ? NL : NM - 2);
#pragma unroll
    for (int m = 0; m < NM; ++m) {
      mm1(x, m);
#pragma unroll
      for (int k = (m * NL + SP - 1) / SP; k < NL && k < ((m + 1) * NL + SP - 1) / SP; ++k) ld1(u, slot, s, k);
      __builtin_amdgcn_sched_barrier(0);
    }
  };
  auto subh = [&](const F& x, F& u, int slot, int s, auto tx, auto tu) {
    constexpr int TX = decltype(tx)::value, TU = decltype(tu)::value, M = TM * TX, LN = TM + 2 * TU;
#pragma unroll
    for (int m = 0; m < M; ++m) {
      mm1(x, m, TX);
#pragma unroll
      for (int k = (m * LN + M - 1) / M; k < LN && k < ((m + 1) * LN + M - 1) / M; ++k) ld1(u, slot, s, k, TU);
      __builtin_amdgcn_sched_barrier(0);
    }
  };
  auto nxt = [](int s) { return s == NS - 1 ? 0 : s + 1; };
  lbar();
  if (threadIdx.x < 64) STAMP(BN == 192 ? 0 : BN == 64 ? 1 : BN == 224 ? 2 : 3, 1);
  TS_DECL; TS(0);
  int slot = 0;
  if constexpr (!DEEP) {
    F f0, f1;
    ldall(f0, 0, 0);
    if constexpr (OWN) {
      sub(f0, f1, 0, 1);
      mmall(f1);
      __builtin_amdgcn_sched_barrier(0);
      TS(32);
      lbar();
      TS(1);
      slot = 1;
      ldall(f0, 1, 0);
    }
#pragma unroll 1
    for (int t = OWN ? 1 : 0; t < NT - 1; ++t) {
      sub(f0, f1, slot, 1);
      TS(32 + t);
      lbar();
      TS(t + 1);
      slot = nxt(slot);
      sub(f1, f0, slot, 0);
    }
    sub(f0, f1, slot, 1);
    lbar();
    TS(NT); TS_OUT(BN == 192 ? 0 : BN == 64 ? 1 : BN == 224 ? 2 : 3);
    mmall(f1);
  } else {
    F f0, f1, g0, g1;
    auto tile = [&](const F& x0, const F& x1, F& u0, F& u1, int t) {
      TS(32 + t);
      bar();
      TS(t + 1);
      slot = nxt(slot);
      sub(x0, u0, slot, 0);
      sub(x1, u1, slot, 1);
    };
    ldall(f0, 0, 0); ldall(f1, 0, 1);
    int t0 = 0;
    if constexpr (OWN) {
      tile(f0, f1, g0, g1, 0);
      tile(g0, g1, f0, f1, 1);
      mmall(f0); mmall(f1);
      __builtin_amdgcn_sched_barrier(0);
      TS(34);
      lbar();
      TS(3);
      slot = 3;
      ldall(g0, 3, 0); ldall(g1, 3, 1);
      tile(g0, g1, f0, f1, 3);
      t0 = 4;
    }
    if constexpr (!OWN) {
#pragma unroll 1
      for (int t = t0; t < NT - 2; t += 2) {
        tile(f0, f1, g0, g1, t);
        tile(g0, g1, f0, f1, t + 1);
      }
      tile(f0, f1, g0, g1, NT - 2);
      lbar();
      TS(NT); TS_OUT(BN == 192 ? 0 : BN == 64 ? 1 : BN == 224 ? 2 : 3);
      mmall(g0); mmall(g1);
    } else {
#pragma unroll 1
      for (int t = t0; t < 24; t += 2) {
        tile(f0, f1, g0, g1, t);
        if (t == 4) mid();
        tile(g0, g1, f0, f1, t + 1);
      }
      typedef std::integral_constant<int, TN> Full;
      typedef std::integral_constant<int, TN / 2> Half;
      auto tileh = [&](const F& x0, const F& x1, F& u0, F& u1, auto tx) {
        bar();
        slot = nxt(slot);
        subh(x0, u0, slot, 0, tx, Half{});
        subh(x1, u1, slot, 1, tx, Half{});
      };
#pragma unroll
      for (int j = 0; j < 2; ++j) {
        const int l2 = opaque(lane), k = 8 * (l2 >> 4) + ((l2 & 15) >> 2);
        boffh[j] = k * 128 + (((2 * nt(j) + ((l2 & 3) >> 1)) ^ bswz<64>(k)) * 16) + 8 * (l2 & 1);
      }
      tileh(f0, f1, g0, g1, Full{});
      mfma_drain();
#pragma unroll
      for (int i = 0; i < TM; ++i)
#pragma unroll
        for (int j = TN / 2; j < TN; ++j) asm volatile("" : "+v"(acc[i][j]));
      send(0);
      __builtin_amdgcn_sched_barrier(0);
      tileh(g0, g1, f0, f1, Half{});
      tileh(f0, f1, g0, g1, Half{});
      send(1);
      tileh(g0, g1, f0, f1, Half{});
      tileh(f0, f1, g0, g1, Half{});
      send(2);
      tileh(g0, g1, f0, f1, Half{});
      send(3);
      lbar();
      TS(28); TS_OUT(3);
#pragma unroll
      for (int m = 0; m < TM * TN / 2; ++m) mm1(f0, m, TN / 2);
#pragma unroll
      for (int m = 0; m < TM * TN / 2; ++m) mm1(f1, m, TN / 2);
    }
  }
  mfma_drain();
#pragma unroll
  for (int i = 0; i < TM; ++i)
#pragma unroll
    for (int j = 0; j < TN; ++j) asm volatile("" : "+v"(acc[i][j]));
}
DI void swap_pair(f32x4& x, f32x4& y) {
#pragma unroll
  for (int e = 0; e < 4; ++e) {
    auto r = __builtin_amdgcn_permlane16_swap(f2u(x[e]), f2u(y[e]), false, false);
    x[e] = u2f(r[0]); y[e] = u2f(r[1]);
  }
}
DI void add_bf8(f32x4& x, f32x4& y, const u32x4& rv) {
  x[0] += bflo(rv[0]); x[1] += bfhi(rv[0]); x[2] += bflo(rv[1]); x[3] += bfhi(rv[1]);
  y[0] += bflo(rv[2]); y[1] += bfhi(rv[2]); y[2] += bflo(rv[3]); y[3] += bfhi(rv[3]);
}
DI u32x4 pack_xy(const f32x4& x, const f32x4& y) { return (u32x4){pk2(x[0], x[1]), pk2(x[2], x[3]), pk2(y[0], y[1]), pk2(y[2], y[3])}; }

template <class Op> DI void red2(float& a, float& b, Op&& op) {
  auto r = __builtin_amdgcn_permlane16_swap(f2u(a), f2u(b), false, false);
  const float c = op(u2f(r[0]), u2f(r[1]));
  auto t = __builtin_amdgcn_permlane32_swap(f2u(c), f2u(c), false, false);
  const float d = op(u2f(t[0]), u2f(t[1]));
  auto u = __builtin_amdgcn_permlane16_swap(f2u(d), f2u(d), false, false);
  a = u2f(u[0]); b = u2f(u[1]);
}
DI void dual_softmax(const f32x4 (&p)[2][2], const float (&sc)[2], f32x4 (&e)[2][2][2]) {
  float mx[2], mn[2], s1[2], s2[2];
#pragma unroll
  for (int t = 0; t < 2; ++t) {
    mx[t] = p[t][0][0]; mn[t] = p[t][0][0];
#pragma unroll
    for (int r = 1; r < 8; ++r) { mx[t] = __builtin_fmaxf(mx[t], p[t][r >> 2][r & 3]); mn[t] = __builtin_fminf(mn[t], p[t][r >> 2][r & 3]); }
  }
  red2(mx[0], mx[1], [](float x, float y) { return __builtin_fmaxf(x, y); });
  red2(mn[0], mn[1], [](float x, float y) { return __builtin_fminf(x, y); });
#pragma unroll
  for (int t = 0; t < 2; ++t) {
    const float a = -mx[t] * sc[t], b = mn[t] * sc[t];
    s1[t] = 0.f; s2[t] = 0.f;
#pragma unroll
    for (int r = 0; r < 8; ++r) {
      const float x = __builtin_amdgcn_exp2f(__builtin_fmaf(p[t][r >> 2][r & 3], sc[t], a));
      const float y = __builtin_amdgcn_exp2f(__builtin_fmaf(p[t][r >> 2][r & 3], -sc[t], b));
      e[0][t][r >> 2][r & 3] = x; e[1][t][r >> 2][r & 3] = y;
      s1[t] += x; s2[t] += y;
    }
  }
  red2(s1[0], s1[1], [](float x, float y) { return x + y; });
  red2(s2[0], s2[1], [](float x, float y) { return x + y; });
#pragma unroll
  for (int t = 0; t < 2; ++t) {
    const float r1 = __builtin_amdgcn_rcpf(s1[t]), r2 = __builtin_amdgcn_rcpf(s2[t]);
#pragma unroll
    for (int r = 0; r < 8; ++r) { e[0][t][r >> 2][r & 3] *= r1; e[1][t][r >> 2][r & 3] *= r2; }
  }
}
DI bf16x8 pack_frag(const f32x4& x, const f32x4& y) { return __builtin_bit_cast(bf16x8, pack_xy(x, y)); }
DI void st4(LAS char* p, const f32x4& a, const f32x4& b, const f32x4& c, const f32x4& d) {
  *(LAS f32x4*)p = a; *(LAS f32x4*)(p + 1024) = b; *(LAS f32x4*)(p + 2048) = c; *(LAS f32x4*)(p + 3072) = d;
}
DI void ld4(LAS const char* p, f32x4 (&v)[2][2]) {
  v[0][0] = *(LAS const f32x4*)p; v[0][1] = *(LAS const f32x4*)(p + 1024); v[1][0] = *(LAS const f32x4*)(p + 2048); v[1][1] = *(LAS const f32x4*)(p + 3072);
}
#endif

__global__ __launch_bounds__(512, 2) void block_kernel(const u16* __restrict__ X, const u16* __restrict__ WQKV, const u16* __restrict__ WPHI,
                                                        const u16* __restrict__ WO, const u16* __restrict__ W1, const u16* __restrict__ W2,
                                                        u16* __restrict__ OUT, char* WS) {
#if defined(__HIP_DEVICE_COMPILE__)
  extern __shared__ __attribute__((aligned(16))) char smem_[];
  LAS char* smem = (LAS char*)smem_;
  const int tid = threadIdx.x, wid = __builtin_amdgcn_readfirstlane(tid >> 6);
  int lane = tid & 63;
  const int bid = blockIdx.x, xcd = bid & 7, loc = bid >> 3;
  const int tm = (xcd & 1) * 8 + (loc & 7), gp = (xcd >> 1) * 4 + (loc >> 3);
  const bool loader = wid >= 4;
  const int lw = wid & 3;
  GAS unsigned* ctl = (GAS unsigned*)WS;
  const rsrc_t rws = mkrsrc(WS, WS_END);
  if (wid == 0) STAMP(0, 0);
  unsigned tag = 0;

  int r16, g, cpair, cswz;
  auto relane = [&]() {
    lane = opaque(lane);
    r16 = lane & 15; g = lane >> 4;
    cpair = (g & 1) * 16 + (g >> 1) * 8;
    cswz = (cpair >> 3) * 256 + r16 * 16;
  };
  {
    constexpr int NT = 16, NS = 3;
    constexpr int L_PHI = 0;
    constexpr int L_XV = 2 * 17408;
    constexpr int L_VF = L_XV + 16384;
    constexpr int L_XQ = L_VF + 8192;
    constexpr int L_KVT = L_XQ;
    constexpr int L_WF = L_XQ + 16384;
    static_assert(L_WF + 4096 <= 5 * 16384, "phase 2's B ring is filled during this epilogue");
    relane();
    const int wm = (wid >> 1) & 1, hh = wid & 1, mi = wid >> 2;
    const int head = gp * 2 + hh;
    const unsigned kvp_grp = WS_KVP + gp * 16 * KVP_BYTES;
    LAS char* const xk = smem + L_PHI + hh * 17408 + (wm * 64 + 32) * 136 + lane * 16;
    bf16x8 wf[2];
    f32x4 fq[2][2], fk[2][2], fv[2][2];
    if (loader) {
      const rsrc_t rb = mkrsrc(WQKV, D_ * 3 * D_ * 2), ro = mkrsrc(WO, D_ * D_ * 2);
      Loader<192, NS, NT, true> L;
      const rsrc_t rx = mkrsrc(X, S_ * D_ * 2);
      L.init(smem, rx, rb, tm * 128 * D_ * 2, D_ * 2, gp * 128, 3 * D_ * 2, 2048, lw, lane, (tm & 3) * 4);
      L.issueA(0); L.issueB(0); L.issueA(1); L.issueB(1);
      if (lw == 0) {
        unsigned long long pc;
        asm volatile("s_getpc_b64 %0" : "=s"(pc));
        touch_lines(smem, mkrsrc((const void*)(pc & ~127ull), CODE_AHEAD), (loc * 16) * 128, lane < 16 ? lane : 4096, 16);
      }
      if (lw == 0) *(LAS volatile unsigned*)(smem + L_GO) = 0u;
      tag = read_tag(WS);
      __builtin_amdgcn_s_setprio(3);
      if (wid == 4) STAMP(4, 1);
      auto cs1 = [&](int t) { if ((tm >> 2) == t) colsum_tile<192>(smem + NS * 16384 + (t % NS) * 24576, rws, WS_CS1 + (gp * 16 + tm) * 768, lw, lane); };
      L.step();
      cs1(0);
      const int xrow = tm * 128 + gp * 8 + lw * 2 + (lane >> 5);
      u32x4 xv[4];
#pragma unroll
      for (int j = 0; j < 4; ++j) xv[j] = __builtin_amdgcn_raw_buffer_load_b128(rx, xrow * (D_ * 2) + (lane & 31) * 16, j * 512, 0);
      L.step<14>();
      cs1(1);
      L.step<14>();
      cs1(2);
      L.step();
      cs1(3);
      {
        asm volatile("" : "+v"(xv[0]), "+v"(xv[1]), "+v"(xv[2]), "+v"(xv[3]));
        float f[32], sum = 0.f, qq = 0.f;
#pragma unroll
        for (int i = 0; i < 16; ++i) { f[2 * i] = bflo(xv[i >> 2][i & 3]); f[2 * i + 1] = bfhi(xv[i >> 2][i & 3]); sum += f[2 * i] + f[2 * i + 1]; }
        const float mean = half_sum(sum) * (1.0f / D_);
#pragma unroll
        for (int i = 0; i < 32; ++i) { const float d = f[i] - mean; qq = __builtin_fmaf(d, d, qq); }
        const float rstd = 1.0f / __builtin_sqrtf(half_sum(qq) * (1.0f / D_) + LN_EPS);
        __builtin_amdgcn_raw_buffer_store_b64(__builtin_bit_cast(u32x2, (f32x2){mean, rstd}), rws, (lane & 31) == 0 ? WS_XST + xrow * 8 : 0x80000000u, 0, SC1);
      }
      L.step();
      L.step<0>();
      if (lane == 0) { set_flag(ctl, C_CS1 + gp, tm * 4 + lw, tag); set_flag(ctl, C_ST + tm, gp * 4 + lw, tag); }
      touch_lines(smem, ro, bid * 8192, lane < 16 ? lw * 16 + lane : 4096, 64);
      L.step();
      L.step<13>();
      L.steps(3);
      const unsigned seen = peek(ctl, C_CS1 + gp, lane), seen2 = peek(ctl, C_ST + tm, lane);
      L.steps(2);
      confirm(seen, ctl, C_CS1 + gp, tag, lane);
      confirm(seen2, ctl, C_ST + tm, tag, lane);
      u32x4 gt[4];
#pragma unroll
      for (int j = 0; j < 4; ++j) gt[j] = ld128(rws, WS_CS1 + (gp * 16 + lw * 4) * 768 + lane * 16, j * 768);
      u32x4 xs = ld128(rws, WS_XST + tm * 1024 + lane * 16, 0);
      L.step();
      wait_vm<10>(); lbar();
      *(LAS f32x4*)(smem + L_CSS + lw * 1024 + lane * 16) = sum4(gt);
      asm volatile("" : "+v"(xs));
      if (lw == 1) *(LAS u32x4*)(smem + L_RST + lane * 16) = xs;
      wait_vm<0>(); lbar();
      if (lw == 0) *(LAS f32x4*)(smem + L_CS + lane * 16) = lds_sum4(smem + L_CSS + lane * 16);
      const rsrc_t r1 = mkrsrc(W1, D_ * FF_ * 2);
      load_wtile<224, 0, 4>(smem + L_WT, r1, tm * 64 * (FF_ * 2) + gp * 448, FF_ * 2, 128, lw, lane);
      lbar();
      bar();
      __builtin_amdgcn_s_setprio(1);
      ld4(xk, fk);
      wf[0] = ld_b128(smem + L_WF + hh * 2048 + lane * 16);
      wf[1] = ld_b128(smem + L_WF + hh * 2048 + 1024 + lane * 16);
      ld4(smem + L_XV + lw * 4096 + lane * 16, fv);
    } else {
      {
#pragma unroll
        for (int t = 0; t < 2; ++t) {
          s16x8 w;
#pragma unroll
          for (int j = 0; j < 8; ++j) w[j] = (short)WPHI[head * 1024 + (16 * (j >> 2) + 4 * g + (j & 3)) * 32 + 16 * t + r16];
          wf[t] = __builtin_bit_cast(bf16x8, w);
        }
        tag = read_tag(WS);
      }
      if (wid == 0) STAMP(4, 0);
      f32x4 acc[4][6];
      mma_loop<192, NS, NT, 4, 6, false, false>(smem, acc, wm * 64, lane, [&](int j) { return (j >> 1) * 4 + hh * 2 + (j & 1); }, []() {}, [](int) {});
      if (wid == 0) STAMP(0, 2);
      if (wm == 0) {
        *(LAS bf16x8*)(smem + L_WF + hh * 2048 + lane * 16) = wf[0];
        *(LAS bf16x8*)(smem + L_WF + hh * 2048 + 1024 + lane * 16) = wf[1];
      }
      st4(xk, acc[2][2], acc[2][3], acc[3][2], acc[3][3]);
      st4(smem + L_XV + wid * 4096 + lane * 16, acc[2][4], acc[2][5], acc[3][4], acc[3][5]);
      lbar();
      if (wid == 0) STAMP(4, 4);
      st4(smem + L_XQ + wid * 4096 + lane * 16, acc[2][0], acc[2][1], acc[3][0], acc[3][1]);
#pragma unroll
      for (int t = 0; t < 2; ++t)
#pragma unroll
        for (int u = 0; u < 2; ++u) { fq[t][u] = acc[t][u]; fk[t][u] = acc[t][2 + u]; fv[t][u] = acc[t][4 + u]; }
    }
    const int rbase = wm * 64 + mi * 32, q = r16 >> 2, p = lane & 3;
    LAS const float* cs = (LAS const float*)(smem + L_CS) + hh * 32;
    LAS const f32x2* rst = (LAS const f32x2*)(smem + L_RST) + rbase;
    auto feature = [&](const f32x4 (&x)[2][2], int part, f32x4 (&e)[2][2][2]) {
      const f32x4 c0 = *(LAS const f32x4*)(cs + part * 64 + 4 * g), c1 = *(LAS const f32x4*)(cs + part * 64 + 16 + 4 * g);
      f32x4 pp[2][2];
      float sc[2];
#pragma unroll
      for (int t = 0; t < 2; ++t) {
        const f32x2 ms = rst[t * 16 + r16];
        sc[t] = ms[1] * 1.4426950408889634f;
        f32x4 y0, y1;
#pragma unroll
        for (int e_ = 0; e_ < 4; ++e_) { y0[e_] = __builtin_fmaf(-ms[0], c0[e_], x[t][0][e_]); y1[e_] = __builtin_fmaf(-ms[0], c1[e_], x[t][1][e_]); }
        const bf16x8 yb = pack_frag(y0, y1);
        pp[t][0] = MFMA16(wf[0], yb, ((f32x4){0.f, 0.f, 0.f, 0.f}));
        pp[t][1] = MFMA16(wf[1], yb, ((f32x4){0.f, 0.f, 0.f, 0.f}));
      }
      dual_softmax(pp, sc, e);
    };
    if (wid == 0) STAMP(4, 5);
    {
      const float cv0 = cs[128 + r16], cv1 = cs[144 + r16];
#pragma unroll
      for (int t = 0; t < 2; ++t) {
        const f32x4 s01 = *(LAS const f32x4*)(rst + t * 16 + 4 * g), s23 = *(LAS const f32x4*)(rst + t * 16 + 4 * g + 2);
        const float mv[4] = {s01[0], s01[2], s23[0], s23[2]}, rv[4] = {s01[1], s01[3], s23[1], s23[3]};
#pragma unroll
        for (int e_ = 0; e_ < 4; ++e_) {
          fv[t][0][e_] = __builtin_fmaf(-mv[e_], cv0, fv[t][0][e_]) * rv[e_];
          fv[t][1][e_] = __builtin_fmaf(-mv[e_], cv1, fv[t][1][e_]) * rv[e_];
        }
      }
      LAS char* vd = smem + (loader ? L_XV + lw * 4096 : L_VF + lw * 2048) + lane * 16;
      *(LAS bf16x8*)vd = pack_frag(fv[0][0], fv[1][0]);
      *(LAS bf16x8*)(vd + 1024) = pack_frag(fv[0][1], fv[1][1]);
      f32x4 e[2][2][2];
      feature(fk, 1, e);
      LAS char* wr = smem + L_PHI + hh * 17408 + (rbase + r16) * 136 + g * 8;
#pragma unroll
      for (int w = 0; w < 2; ++w)
#pragma unroll
        for (int t = 0; t < 2; ++t)
#pragma unroll
          for (int u = 0; u < 2; ++u)
            *(LAS u32x2*)(wr + t * 16 * 136 + w * 64 + u * 32) = (u32x2){pk2(e[w][t][u][0], e[w][t][u][1]), pk2(e[w][t][u][2], e[w][t][u][3])};
    }
    if (wid == 0) STAMP(4, 7);
    if (wid == 4) STAMP(4, 9);
    lbar();
    __builtin_amdgcn_s_setprio(0);
    if (wid == 0) STAMP(4, 8);
    bf16x8 pf[2][2];
    auto qpath = [&]() {
      f32x4 e[2][2][2];
      feature(fq, 0, e);
#pragma unroll
      for (int t = 0; t < 2; ++t)
#pragma unroll
        for (int w = 0; w < 2; ++w) { pf[t][w] = pack_frag(e[w][t][0], e[w][t][1]); asm volatile("" : "+v"(pf[t][w])); }
    };
    if (loader) {
      ld4(smem + L_XQ + lw * 4096 + lane * 16, fq);
      qpath();
      load_wtile<224, 4, 7>(smem + L_WT, mkrsrc(W1, D_ * FF_ * 2), tm * 64 * (FF_ * 2) + gp * 448, FF_ * 2, 128, lw, lane);
    } else {
      f32x4 k00 = {0.f, 0.f, 0.f, 0.f}, k01 = k00, k10 = k00, k11 = k00, z0 = k00, z1 = k00;
      const s16x8 one_ = {0x3f80, 0x3f80, 0x3f80, 0x3f80, 0x3f80, 0x3f80, 0x3f80, 0x3f80};
      LAS const char* rd = smem + L_PHI + hh * 17408 + (4 * g + q) * 136 + wm * 64 + p * 8;
#pragma unroll
      for (int u = 0; u < 4; ++u) {
        const bf16x8 pa0 = ld_tr2(rd + 32 * u * 136, rd + (32 * u + 16) * 136), pa1 = ld_tr2(rd + 32 * u * 136 + 32, rd + (32 * u + 16) * 136 + 32);
        LAS const char* vp = smem + ((u & 1) ? L_XV + ((u >> 1) * 2 + hh) * 4096 : L_VF + ((u >> 1) * 2 + hh) * 2048) + lane * 16;
        const bf16x8 v0 = ld_b128(vp), v1 = ld_b128(vp + 1024);
        k00 = MFMA16(pa0, v0, k00); k10 = MFMA16(pa1, v0, k10);
        k01 = MFMA16(pa0, v1, k01); k11 = MFMA16(pa1, v1, k11);
        z0 = MFMA16(pa0, __builtin_bit_cast(bf16x8, one_), z0); z1 = MFMA16(pa1, __builtin_bit_cast(bf16x8, one_), z1);
      }
      const int kb = kvp_grp + tm * KVP_BYTES;
      st128(pack_xy(k00, k10), rws, kb + hh * 4096 + wm * 2048 + lane * 16);
      st128(pack_xy(k01, k11), rws, kb + hh * 4096 + wm * 2048 + 1024 + lane * 16);
      st128(__builtin_bit_cast(u32x4, r16 == 0 ? z0 : z1), rws, r16 < 2 ? kb + 8192 + hh * 256 + wm * 128 + g * 32 + r16 * 16 : (int)0x80000000);
      if (wid == 0) STAMP(0, 3);
      wait_vm<0>();
    }
    lbar();
    if (tid == 0) set_flag(ctl, C_KV + gp, tm, tag);
    if (wid == 0) STAMP(0, 4);
    if (loader) {
      load_wo<0, WO1>(smem, mkrsrc(WO, D_ * D_ * 2), gp, lw, lane);
    } else {
      qpath();
    }
    FastPoll fpk = {0u, 0u, 0u, 0u};
    if (wid == 0) {
      STAMP(0, 5);
      fpk.run(ctl, C_KV + gp, 0, 16, tag, lane);
    }
    bar();
    if (wid == 0) STAMP(0, 6);
    {
      u32x4 tz[16];
      if (wid == 7) {
#pragma unroll
        for (int j = 0; j < 16; ++j) tz[j] = ld128(rws, lane < 32 ? kvp_grp + 8192 + lane * 16 : 0x80000000u, j * KVP_BYTES);
      }
      u32x4 t[16];
#pragma unroll
      for (int j = 0; j < 16; ++j) t[j] = ld128(rws, kvp_grp + tid * 16, j * KVP_BYTES);
      if (wid == 7) {
        f32x4 z = __builtin_bit_cast(f32x4, tz[0]);
#pragma unroll
        for (int j = 1; j < 16; ++j) z += __builtin_bit_cast(f32x4, tz[j]);
        if (lane < 32) *(LAS u32x2*)(smem + L_KVT + 8192 + lane * 8) = (u32x2){pk2(z[0], z[1]), pk2(z[2], z[3])};
      }
      f32x4 x = {0.f, 0.f, 0.f, 0.f}, y = x;
#pragma unroll
      for (int j = 0; j < 16; ++j) add_bf8(x, y, t[j]);
      *(LAS u32x4*)(smem + L_KVT + tid * 16) = pack_xy(x, y);
    }
    fpk.keep();
    lbar();
    if (wid == 0) STAMP(0, 7);
    {
      bf16x8 ka[2][2], za[2];
#pragma unroll
      for (int w = 0; w < 2; ++w) {
        ka[w][0] = ld_b128(smem + L_KVT + hh * 4096 + w * 2048 + lane * 16);
        ka[w][1] = ld_b128(smem + L_KVT + hh * 4096 + w * 2048 + 1024 + lane * 16);
        za[w] = ld_b128(smem + L_KVT + 8192 + hh * 128 + w * 64 + g * 16);
      }
#pragma unroll
      for (int t = 0; t < 2; ++t) {
        f32x4 o0 = {0.f, 0.f, 0.f, 0.f}, o1 = o0, dn = o0;
#pragma unroll
        for (int w = 0; w < 2; ++w) { dn = MFMA16(za[w], pf[t][w], dn); o0 = MFMA16(ka[w][0], pf[t][w], o0); o1 = MFMA16(ka[w][1], pf[t][w], o1); }
        const float rd = 1.0f / (dn[0] + 1e-6f);
#pragma unroll
        for (int e_ = 0; e_ < 4; ++e_) { o0[e_] *= rd; o1[e_] *= rd; }
        swap_pair(o0, o1);
        st128(pack_xy(o0, o1), rws, WS_O + (tm * 32 + head) * 8192 + (rbase + t * 16) * 64 + cswz);
      }
    }
    wait_vm<0>();
    lbar();
    if (tid == 0) set_flag(ctl, C_O + tm, gp, tag);
    if (wid == 0) STAMP(0, 8);
  }

  relane();
  {
    FastPoll fp = {0u, 0u, 0u, 0u};
    if (loader) {
      load_wo<WO1, 16>(smem, mkrsrc(WO, D_ * D_ * 2), gp, lw, lane);
      colsum_tile<224>(smem + L_WT, rws, WS_CS3 + (gp * 16 + tm) * 896, lw, lane);
    }
    if (wid == 0) {
      fp.run(ctl, C_O + tm, 0, 16, tag, lane);
      STAMP(1, 0);
      if (bid == 0 && lane == 0) __hip_atomic_store(ctl + C_EPOCH * 64, tag, __ATOMIC_RELAXED, __HIP_MEMORY_SCOPE_AGENT);
    }
    if (loader) {
      wait_vm<0>();
      bar();
      if (lane == 0) set_flag(ctl, C_CS3 + gp, tm * 4 + lw, tag);
    } else {
      const int row = tm * 128 + wid * 32 + r16, col = gp * 64 + cpair;
      u32x4 res[2][2];
#pragma unroll
      for (int i = 0; i < 2; ++i)
#pragma unroll
        for (int j = 0; j < 2; ++j) res[i][j] = *(const u32x4*)(X + (size_t)(row + i * 16) * D_ + col + j * 32);
      f32x4 acc[2][4];
#pragma unroll
      for (int j = 0; j < 8; ++j) { acc[j >> 2][j & 3] = (f32x4){0.f, 0.f, 0.f, 0.f}; asm volatile("" : "+v"(acc[j >> 2][j & 3])); }
      constexpr int PF = PFD, BD = BDEPTH;
      const int q = r16 >> 2, p = lane & 3;
      int bo[4];
#pragma unroll
      for (int j = 0; j < 4; ++j) bo[j] = (8 * g + q) * 128 + (((2 * j + (p >> 1)) ^ bswz<64>(8 * g + q)) * 16) + 8 * (p & 1);
      const int ao = WS_O + tm * 262144 + wid * 2048 + lane * 16;
      u32x4 af[PF][2];
      s16x4 bf[BD][4][2];
      auto ldb = [&](int sl, int k) {
        bf[sl % BD][k >> 1][k & 1] = __builtin_amdgcn_ds_read_tr16_b64_v4i16((LAS s16x4*)(smem + wo_slot(sl >> 1) + bo[k >> 1] + (32 * (sl & 1) + 4 * (k & 1)) * 128));
      };
      bar();
      if (wid == 0) STAMP(1, 1);
#pragma unroll
      for (int sl = 0; sl < PF; ++sl) { af[sl][0] = ld128(rws, ao, sl * 8192); af[sl][1] = ld128(rws, ao + 1024, sl * 8192); }
#pragma unroll
      for (int k = 0; k < 8 * (BD - 1); ++k) ldb(k >> 3, k & 7);
      __builtin_amdgcn_sched_barrier(0);
#pragma unroll
      for (int sl = 0; sl < 32; ++sl) {
#pragma unroll
        for (int m = 0; m < 8; ++m) {
          const int i = m >> 2, j = m & 3;
          mfma16(acc[i][j], __builtin_bit_cast(bf16x8, __builtin_shufflevector(bf[sl % BD][j][0], bf[sl % BD][j][1], 0, 1, 2, 3, 4, 5, 6, 7)),
                 __builtin_bit_cast(bf16x8, af[sl % PF][i]));
          if (sl + BD - 1 < 32) ldb(sl + BD - 1, m);
          if (j == 3 && sl + PF < 32) af[sl % PF][i] = ld128(rws, ao + i * 1024, (sl + PF) * 8192);
          __builtin_amdgcn_sched_barrier(0);
        }
        if (sl == 30) lbar();
      }
      fp.keep();
      mfma_drain();
#pragma unroll
      for (int j = 0; j < 8; ++j) asm volatile("" : "+v"(acc[j >> 2][j & 3]));
      if (wid == 0) STAMP(1, 2);
#pragma unroll
      for (int i = 0; i < 2; ++i) {
        float s = 0.f, qq = 0.f;
#pragma unroll
        for (int j = 0; j < 2; ++j) {
          f32x4 x = acc[i][2 * j], y = acc[i][2 * j + 1];
          swap_pair(x, y);
          add_bf8(x, y, res[i][j]);
          const u32x4 o = pack_xy(x, y);
          st128(o, rws, WS_H + (tm * 32 + gp * 2 + j) * 8192 + (wid * 32 + i * 16) * 64 + cswz);
          *(LAS u32x4*)(smem + j * 8192 + (wid * 32 + i * 16) * 64 + cswz) = o;
#pragma unroll
          for (int e = 0; e < 4; ++e) { const float lo = bflo(o[e]), hi = bfhi(o[e]); s += lo + hi; qq = __builtin_fmaf(lo, lo, __builtin_fmaf(hi, hi, qq)); }
        }
        red2(s, qq, [](float x, float y) { return x + y; });
        __builtin_amdgcn_raw_buffer_store_b64(__builtin_bit_cast(u32x2, (f32x2){s, qq}), rws,
                                              g == 0 ? WS_HST + ((tm * 16 + gp) * 128 + wid * 32 + i * 16 + r16) * 8 : 0x80000000u, 0, SC1);
      }
      if (wid == 0) STAMP(1, 3);
    }
  }

  const int kh = gp >> 3;
  if (loader) {
    __builtin_amdgcn_s_setprio(3);
    relane();
    const rsrc_t r2 = mkrsrc(W2, FF_ * D_ * 2);
    FastPoll fp;
    {
      Loader<224, 3, 16, false> L;
      L.init(smem, rws, mkrsrc(W1, D_ * FF_ * 2), WS_H + tm * 128 * D_ * 2, 0, gp * 448, FF_ * 2, 128, lw, lane, gp);
      __builtin_amdgcn_s_sleep(B3DELAY);
      L.issueB(0);
      bar();
      bar();
      if (wid == 4 && lane == 0) set_flag(ctl, C_H + tm, gp, tag);
      wait_vm<0>(); bar();
      L.issueB(1); L.issueB(2);
      poll4(fp, smem, 2, lw, ctl, C_H + tm, 0, 16, tag, lane);
      if (wid == 4) STAMP(2, 0);
      L.issueA(1); L.issueA(2);
      u32x4 gt[4], hs[4];
#pragma unroll
      for (int j = 0; j < 4; ++j) hs[j] = ld128(rws, WS_HST + tm * 16384 + lw * 4096 + lane * 16, j * 1024);
      const unsigned seen = peek(ctl, C_CS3 + gp, lane);
      L.tn = 3;
      L.step<4 + 5>();
      fp.keep();
      L.step<5 + 11>();
      confirm(seen, ctl, C_CS3 + gp, tag, lane);
#pragma unroll
      for (int j = 0; j < 4; ++j) gt[j] = ld128(rws, WS_CS3 + (gp * 16 + lw * 4) * 896 + lane * 16, j * 896);
      *(LAS f32x4*)(smem + L_CSS + 4096 + lw * 1024 + lane * 16) = sum4(hs);
      L.step();
      if (lw == 1) {
        const f32x4 sum = lds_sum4(smem + L_CSS + 4096 + lane * 16);
        const float m0 = sum[0] * (1.0f / D_), m1 = sum[2] * (1.0f / D_);
        const float v0 = __builtin_fmaxf(sum[1] * (1.0f / D_) - m0 * m0, 0.f), v1 = __builtin_fmaxf(sum[3] * (1.0f / D_) - m1 * m1, 0.f);
        *(LAS f32x4*)(smem + L_RST + lane * 16) = (f32x4){m0, 1.0f / __builtin_sqrtf(v0 + LN_EPS), m1, 1.0f / __builtin_sqrtf(v1 + LN_EPS)};
      }
      L.step();
      *(LAS f32x4*)(smem + L_CSS + lw * 1024 + lane * 16) = sum4(gt);
      L.step();
      if (lw == 0) *(LAS f32x4*)(smem + L_CS + lane * 16) = lds_sum4(smem + L_CSS + lane * 16);
      const int stag = (loc & 7) < 6 ? (loc & 7) : 6;
      L.steps(stag);
      touch_lines(smem, r2, bid * 28672, lane < 56 ? lw * 56 + lane : 4096, 224);
      L.step();
      L.step<12>();
      L.steps(6 - stag);
    }
    {
      relane();
      Loader<128, 4, 31, false, 1> L;
      L.kh = kh;
      L.init(smem, rws, r2, WS_H1 + tm * 112 * 8192, 0, gp * 128, D_ * 2, kh ? -1024 : 1024, lw, lane, gp & 7);
      L.issueB(0);
      wait_vm<11 + 4>(); lbar(); wait_vm<4>(); bar();
      bar();
      L.issueB(1); L.issueB(2); L.issueB(3);
      wait_vm<12>(); bar();
      if (wid == 4 && lane == 0) set_flag(ctl, C_H1 + tm, gp, tag);
      wait_vm<8>(); bar();
      wait_vm<4>(); bar();
      L.issueB(4);
      poll4(fp, smem, 3, lw, ctl, C_H1 + tm, kh * 8, 8, tag, lane);
      if (wid == 4) STAMP(3, 0);
      L.issueA(3); L.issueA(4);
      L.tn = 5;
      L.step<4>();
      fp.keep();
      L.steps(3);
      if (wid == 4 && lane == 0) set_flag(ctl, C_H1B + tm, gp, tag);
      L.steps(5);
      const unsigned seen = peek(ctl, C_H1B + tm, lane, 16);
      L.steps(3);
      confirm(seen, ctl, C_H1B + tm, tag, lane, 16);
      L.steps(8);
      L.init_half(gp * 128, D_ * 2, lw, lane);
      wait_vm<8>(); bar(); L.issueH(25);
      wait_vm<6>(); bar(); L.issueH(26);
#pragma unroll 1
      for (int t = 27; t <= 30; ++t) { wait_vm<6>(); bar(); L.issueH(t); }
      wait_vm<6>(); bar();
      wait_vm<0>(); bar();
      bar();
    }
  } else {
    const int wm = wid >> 1, wn = wid & 1;
    relane();
    wait_vm<0>();
    bar();
    {
      const int par = gp & 1;
      auto slab3 = [&](int a) { return wn * 4 + (a + par == 3 ? 0 : a + par); };
      auto ntile3 = [&](int j) { return j < 6 ? slab3(j >> 1) * 2 + (j & 1) : 6 + wn; };
      f32x4 acc[4][7];
      mma_loop<224, 3, 16, 4, 7, false, true>(smem, acc, wm * 64, lane, ntile3, []() {}, [](int) {});
      if (wid == 0) STAMP(2, 2);
      relane();
      f32x4 cs[7];
#pragma unroll
      for (int j = 0; j < 7; ++j) cs[j] = *(LAS const f32x4*)(smem + L_CS + (ntile3(j) * 16 + g * 4) * 4);
      float fa[4], fb[4];
#pragma unroll
      for (int i = 0; i < 4; ++i) {
        const f32x2 ms = *(LAS const f32x2*)(smem + L_RST + (wm * 64 + i * 16 + r16) * 8);
        fa[i] = ms[1]; fb[i] = -ms[0] * ms[1];
      }
      auto act = [&](int i, int j) {
        f32x4 y;
#pragma unroll
        for (int e = 0; e < 4; ++e) y[e] = __builtin_fmaxf(__builtin_fmaf(acc[i][j][e], fa[i], fb[i] * cs[j][e]), 0.f);
        return y;
      };
      const int hb = WS_H1 + (tm * 112 + gp * 7) * 8192;
#pragma unroll
      for (int a = 0; a < 3; ++a) {
        const int sl = slab3(a);
#pragma unroll
        for (int i = 0; i < 4; ++i) {
          f32x4 x = act(i, 2 * a), y = act(i, 2 * a + 1);
          swap_pair(x, y);
          const u32x4 o = pack_xy(x, y);
          const int ro = (wm * 64 + i * 16) * 64 + cswz;
          st128(o, rws, hb + sl * 8192 + ro);
          if (a < 2 || (unsigned)(sl - par) < 6u) *(LAS u32x4*)(smem + (sl - par) * 8192 + ro) = o;
        }
        asm volatile("" ::: "memory");
      }
#pragma unroll
      for (int i = 1; i < 4; i += 2) {
        f32x4 x = act(i - 1, 6), y = act(i, 6);
        swap_pair(x, y);
        const u32x4 o = pack_xy(x, y);
        const int r3 = (wm * 64 + (i - 1 + (g & 1)) * 16) * 64 + (2 * wn + (g >> 1)) * 256 + r16 * 16;
        st128(o, rws, hb + 3 * 8192 + r3);
        *(LAS u32x4*)(smem + (3 - par) * 8192 + r3) = o;
      }
      if (wid == 0) STAMP(2, 5);
      wait_vm<6>();
      if (wid == 0) STAMP(2, 3);
    }
    relane();
    {
      const int row = tm * 128 + wm * 64 + r16, col = gp * 64 + wn * 32 + cpair;
      u32x4 res[4], t[4];
      unsigned seen = 0;
      f32x4 acc[4][4];
      mma_loop<128, 4, 31, 4, 4, true, true>(smem, acc, wm * 64, lane, [&](int j) { return (j >> 1) * 4 + wn * 2 + (j & 1); }, [&]() {
        wait_vm<0>();
        if (wid == 0) STAMP(2, 4);
#pragma unroll
        for (int i = 0; i < 4; ++i) res[i] = ld128(rws, WS_H + (tm * 32 + gp * 2 + wn) * 8192 + (wm * 64 + i * 16) * 64 + cswz, 0);
      }, [&](int what) {
        if (what == 0) {
          const int pb = WS_P4 + (tm * 16 + gp) * 16384 + wid * 4096 + lane * 16;
#pragma unroll
          for (int i = 0; i < 4; ++i) st128(pack_xy(acc[i][2], acc[i][3]), rws, pb + i * 1024);
        } else if (what == 1) {
          wait_vm<0>();
          if (lane == 0) set_flag(ctl, C_P4 + tm, gp * 4 + wid, tag);
          if (wid == 0) STAMP(3, 3);
        } else if (what == 2) {
          seen = __hip_atomic_load(ctl + (C_P4 + tm) * 64 + (gp ^ 8) * 4 + wid, __ATOMIC_RELAXED, __HIP_MEMORY_SCOPE_AGENT);
        } else {
          asm volatile("" : "+v"(seen));
          if (!wave_all(seen == tag)) poll(ctl, C_P4 + tm, (gp ^ 8) * 4 + wid, 1, tag, lane);
          acquire();
          if (wid == 0) STAMP(3, 4);
          const int qb = WS_P4 + (tm * 16 + (gp ^ 8)) * 16384 + wid * 4096 + lane * 16;
#pragma unroll
          for (int i = 0; i < 4; ++i) t[i] = ld128(rws, qb + i * 1024, 0);
        }
      });
      if (wid == 0) STAMP(3, 2);
#pragma unroll
      for (int i = 0; i < 4; ++i) {
        f32x4 x = acc[i][0], y = acc[i][1];
        add_bf8(x, y, t[i]);
        swap_pair(x, y);
        add_bf8(x, y, res[i]);
        *(u32x4*)(OUT + (size_t)(row + i * 16) * D_ + col) = pack_xy(x, y);
      }
      if (wid == 0) STAMP(3, 5);
    }
  }
#endif
}
#if defined(__HIP_DEVICE_COMPILE__)
#pragma clang attribute pop
#endif

extern "C" void kernel_launch(void* const* d_in, const int* in_sizes, int n_in, void* d_out, int out_size, void* d_ws, size_t ws_size,
                              hipStream_t stream) {
  (void)hipFuncSetAttribute((const void*)block_kernel, hipFuncAttributeMaxDynamicSharedMemorySize, LDS_BYTES);
  block_kernel<<<256, 512, LDS_BYTES, stream>>>((const u16*)d_in[0], (const u16*)d_in[1], (const u16*)d_in[2], (const u16*)d_in[3],
                                                (const u16*)d_in[5], (const u16*)d_in[6], (u16*)d_out, (char*)d_ws);
}
```

```cpp
#include <hip/hip_runtime.h>
#include <hip/hip_bf16.h>
#include <type_traits>

#define LAS __attribute__((address_space(3)))
#define GAS __attribute__((address_space(1)))
#define DI __device__ __forceinline__
typedef __bf16 bf16x8 __attribute__((ext_vector_type(8)));
typedef __bf16 bf16x2 __attribute__((ext_vector_type(2)));
typedef short s16x4 __attribute__((ext_vector_type(4)));
typedef short s16x8 __attribute__((ext_vector_type(8)));
typedef float f32x2 __attribute__((ext_vector_type(2)));
typedef float f32x4 __attribute__((ext_vector_type(4)));
typedef unsigned u32x2 __attribute__((ext_vector_type(2)));
typedef unsigned u32x4 __attribute__((ext_vector_type(4)));
typedef unsigned short u16;

constexpr int S_ = 2048, D_ = 1024, FF_ = 3584;
constexpr float LN_EPS = 1e-5f;

constexpr int C_EPOCH = 0, C_TMO = 1, C_ST = 2, C_CS1 = 18, C_CS3 = 34, C_KV = 50, C_O = 66, C_H = 82, C_H1 = 98, C_P4 = 114, C_H1B = 130, C_LINES = 146;
constexpr unsigned WS_XST = 40960;
constexpr unsigned WS_CS1 = WS_XST + S_ * 8;
constexpr unsigned WS_CS3 = WS_CS1 + 16 * 16 * 192 * 4;
constexpr unsigned WS_HST = WS_CS3 + 16 * 16 * 224 * 4;
constexpr unsigned KVP_BYTES = 8192 + 512;
constexpr unsigned WS_KVP = WS_HST + S_ * 16 * 8;
#define BLOCKED 2
constexpr unsigned WS_O = WS_KVP + 256 * KVP_BYTES;
constexpr unsigned WS_H = WS_O + S_ * D_ * 2;
constexpr unsigned WS_H1 = WS_H + S_ * D_ * 2;
constexpr unsigned WS_P4 = WS_O, WS_END = WS_H1 + S_ * FF_ * 2;
static_assert(C_LINES * 256 <= (int)WS_XST && WS_O % 256 == 0 && WS_KVP % 256 == 0, "");

constexpr int L_WT = 120 * 1024;
constexpr int L_TAB = 149 * 1024;
constexpr int L_DUMMY = L_TAB;
constexpr int L_RST = L_TAB + 256;
constexpr int L_CS = L_RST + 1024;
constexpr int L_CSS = L_CS + 1024;
constexpr int L_GO = L_CSS + 8192;
constexpr int LDS_BYTES = L_GO + 16;
static_assert(LDS_BYTES <= 160 * 1024, "");

#define STAMP(k, i) do {} while (0)
#define TS_DECL
#define TS(t)
#define TS_OUT(k)

#if defined(__HIP_DEVICE_COMPILE__)
#pragma clang attribute push(__attribute__((target("no-packed-fp32-ops"))), apply_to = function)
typedef __amdgpu_buffer_rsrc_t rsrc_t;
DI float u2f(unsigned u) { return __builtin_bit_cast(float, u); }
DI unsigned f2u(float f) { return __builtin_bit_cast(unsigned, f); }
DI bool wave_all(bool p) { return __builtin_amdgcn_ballot_w64(p) == ~0ull; }
#define CODE_AHEAD 53248
#define PSLEEP "2"
#define WO1 9
#define BDEPTH 3
#define PFD 8
#define B3DELAY 48
#define CPW 0
constexpr int SC1 = 16;
template <int N> DI void wait_vm() { asm volatile("s_waitcnt vmcnt(%0)" ::"n"(N) : "memory"); }
DI void wait_lgkm0() { asm volatile("" ::: "memory"); __builtin_amdgcn_s_waitcnt(0xC07F); asm volatile("" ::: "memory"); }
DI void bar() { asm volatile("" ::: "memory"); __builtin_amdgcn_s_barrier(); asm volatile("" ::: "memory"); }
DI void lbar() { wait_lgkm0(); bar(); }
DI unsigned pk2(float a, float b) { f32x2 v = {a, b}; return __builtin_bit_cast(unsigned, __builtin_convertvector(v, bf16x2)); }
DI float bflo(unsigned u) { return u2f(u << 16); }
DI float bfhi(unsigned u) { return u2f(u & 0xffff0000u); }
DI bf16x8 ld_b128(LAS const char* p) { return *(LAS const bf16x8*)p; }
DI bf16x8 ld_tr2(LAS const char* p0, LAS const char* p1) {
  s16x4 a = __builtin_amdgcn_ds_read_tr16_b64_v4i16((LAS s16x4*)p0);
  s16x4 b = __builtin_amdgcn_ds_read_tr16_b64_v4i16((LAS s16x4*)p1);
  return __builtin_bit_cast(bf16x8, __builtin_shufflevector(a, b, 0, 1, 2, 3, 4, 5, 6, 7));
}
DI float x32_sum(float v) { auto r = __builtin_amdgcn_permlane32_swap(f2u(v), f2u(v), false, false); return u2f(r[0]) + u2f(r[1]); }
DI float x16_sum(float v) { auto r = __builtin_amdgcn_permlane16_swap(f2u(v), f2u(v), false, false); return u2f(r[0]) + u2f(r[1]); }
template <int CTRL> DI float dpp_add(float v) { return v + u2f(__builtin_amdgcn_update_dpp(0, (int)f2u(v), CTRL, 0xf, 0xf, true)); }
DI float half_sum(float v) {
  v = dpp_add<0xB1>(v);
  v = dpp_add<0x4E>(v);
  v = dpp_add<0x141>(v);
  v = dpp_add<0x140>(v);
  return x16_sum(v);
}
#define MFMA16(a, b, c) __builtin_amdgcn_mfma_f32_16x16x32_bf16((a), (b), (c), 0, 0, 0)
DI rsrc_t mkrsrc(const void* p, unsigned bytes) { return __builtin_amdgcn_make_buffer_rsrc((void*)p, 0, bytes, 0x00020000); }
DI void st128(u32x4 v, rsrc_t r, int off) { __builtin_amdgcn_raw_buffer_store_b128(v, r, off, 0, SC1); }
DI u32x4 ld128(rsrc_t r, int off, int soff) { return __builtin_amdgcn_raw_buffer_load_b128(r, off, soff, SC1); }

DI unsigned read_tag(const char* ws) {
  unsigned e;
  asm volatile("s_load_dword %0, %1, 0x0\n\ts_waitcnt lgkmcnt(0)" : "=s"(e) : "s"(ws) : "memory");
  return e + 1u;
}
DI void set_flag(GAS unsigned* ctl, int line, int word, unsigned tag) { __hip_atomic_store(ctl + line * 64 + word, tag, __ATOMIC_RELAXED, __HIP_MEMORY_SCOPE_AGENT); }
DI void acquire() {
  asm volatile("" ::: "memory");
}
DI void poll(GAS unsigned* ctl, int line, int lo, int n, unsigned tag, int lane) {
  GAS unsigned* p = ctl + line * 64 + lo + (lane < n ? lane : 0);
  unsigned spins = 0;
  while (!wave_all(__hip_atomic_load(p, __ATOMIC_RELAXED, __HIP_MEMORY_SCOPE_AGENT) == tag)) {
    __builtin_amdgcn_s_sleep(1);
    if (++spins > (1u << 17)) { if (lane == 0) __hip_atomic_store(ctl + C_TMO * 64, (unsigned)line, __ATOMIC_RELAXED, __HIP_MEMORY_SCOPE_AGENT); break; }
  }
  acquire();
}
struct FastPoll {
  unsigned a, b, c, d;
  DI void run(GAS unsigned* ctl, int line, int lo, int n, unsigned tag, int lane) {
    GAS unsigned* p = ctl + line * 64 + lo + (lane < n ? lane : 0);
    unsigned cnt = 1u << 17;
#define FP_LD(r) "global_load_dword %" #r ", %5, off sc1\n\ts_sleep " PSLEEP "\n\t"
#define FP_CK(r) "s_waitcnt vmcnt(3)\n\tv_cmp_eq_u32 vcc, %6, %" #r "\n\ts_cmp_eq_u64 vcc, -1\n\ts_cbranch_scc1 2f\n\t" FP_LD(r)
    asm volatile(FP_LD(0) FP_LD(1) FP_LD(2) FP_LD(3) "1:\n\t" FP_CK(0) FP_CK(1) FP_CK(2) FP_CK(3)
                 "s_sub_u32 %4, %4, 1\n\ts_cbranch_scc0 1b\n\t2:"
                 : "=&v"(a), "=&v"(b), "=&v"(c), "=&v"(d), "+s"(cnt) : "v"(p), "s"(tag) : "vcc", "scc", "memory");
#undef FP_LD
#undef FP_CK
  }
  DI void keep() const { asm volatile("" :: "v"(a), "v"(b), "v"(c), "v"(d)); }
};
DI void poll4(FastPoll& fp, LAS char* smem, unsigned go, int lw, GAS unsigned* ctl, int line, int lo, int n, unsigned tag, int lane) {
  LAS volatile unsigned* w = (LAS volatile unsigned*)(smem + L_GO);
  if (lw == 0) {
    fp.run(ctl, line, lo, n, tag, lane);
    *w = go;
  } else {
    fp.a = fp.b = fp.c = fp.d = 0u;
    unsigned spins = 0;
    while (__builtin_amdgcn_readfirstlane(*w) != go && ++spins < (1u << 24)) __builtin_amdgcn_s_sleep(1);
  }
  asm volatile("" ::: "memory");
}

template <int BN> DI constexpr int bswz(int k) {
  return BN == 224 ? (((k >> 3) & 1) << 1)
       : BN == 128 ? (((k & 3) | (((k >> 3) & 1) << 2)) << 1)
                   : ((((k >> 1) & 1) | (((k >> 3) & 1) << 1)) << 1);
}

template <int BN, int NS> DI int bslot(int s) {
  if (BN == 224) return ((0x4c3078 >> (8 * s)) & 255) << 10;
  if (BN == 128) return ((0x78504068 >> (8 * s)) & 255) << 10;
  return NS * 16384 + s * BN * 128;
}
DI constexpr int wo_slot(int t) { return t < 5 ? 81920 + t * 8192 : t < 15 ? (t - 5) * 8192 : L_CSS; }

template <int BN, int NS, int NT, bool AROW, int LAG = 0>
struct Loader {
  static constexpr int PA = 4, PB = BN / 32, PW = PA + PB, BST = BN * 128, CPR = BN / 8;
  static_assert(NS * (16384 + BST) <= L_TAB, "");
  LAS char* dst0; rsrc_t ra, rb; int va[PA], vb[PB], vh[2]; int ldb64, own0, tn;
  static constexpr int NOWN = BN == 128 ? 3 : BN == 224 ? 1 : 0;
  int kh;
  DI int ktile(int t) const {
    if (AROW) return (t + own0) & (NT - 1);
    if (BN != 128) return t < NOWN ? own0 + t : (t - NOWN < own0 ? t - NOWN : t);
    const int pi = t < 17 ? (t - 3) >> 1 : t - 17, p = t < 3 ? own0 : pi + (pi >= own0 ? 1 : 0);
    const int k = t >= 24 ? 7 * ((t >= 28 ? t - 3 : t) - 24) + 3 : ((7 * p + 1) >> 1) + (t < 3 ? t : t < 17 ? 2 * ((t - 3) & 1) : 1);
    return k + ((t >= 28) != (kh != 0) ? 28 : 0);
  }
  DI void issueA(int t) {
    const int kk = ktile(t), slot = t % NS;
#pragma unroll
    for (int i = 0; i < PA; ++i)
      __builtin_amdgcn_raw_ptr_buffer_load_lds(ra, dst0 + slot * 16384 + i * 4096, 16, va[i], kk * (AROW ? 128 : 16384), 0, AROW ? 0 : SC1);
  }
  DI void issueB(int t) {
    const int kk = ktile(t), slot = t % NS;
#pragma unroll
    for (int i = 0; i < PB; ++i) __builtin_amdgcn_raw_ptr_buffer_load_lds(rb, dst0 + bslot<BN, NS>(slot) + i * 4096, 16, vb[i], kk * ldb64, 0, 0);
  }
  DI void issueH(int t) {
    issueA(t);
    const int kk = ktile(t), slot = t % NS;
#pragma unroll
    for (int i = 0; i < 2; ++i) __builtin_amdgcn_raw_ptr_buffer_load_lds(rb, dst0 + bslot<BN, NS>(slot) + i * 4096, 16, vh[i], kk * ldb64, 0, 0);
  }
  DI void init(LAS char* smem, rsrc_t ra_, rsrc_t rb_, int a_off, int lda_b, int b_off, int ldb_b, int segb, int lw, int lane, int own) {
    dst0 = smem + lw * 1024; ra = ra_; rb = rb_; ldb64 = 64 * ldb_b; own0 = own; tn = NS - 1;
#pragma unroll
    for (int i = 0; i < PA; ++i) {
      const int ins = lw + i * 4, r = ins * 8 + (lane >> 3), c = (lane & 7) ^ ((r >> 1) & 7);
      va[i] = AROW ? a_off + r * lda_b + c * 16 : a_off + ins * 1024 + lane * 16;
    }
#pragma unroll
    for (int i = 0; i < PB; ++i) {
      const int ch = (lw + i * 4) * 64 + lane, k = ch / CPR, c = (ch % CPR) ^ bswz<BN>(k);
      vb[i] = b_off + k * ldb_b + (c >> 3) * segb + (c & 7) * 16;
    }
  }
  DI void init_half(int b_off, int ldb_b, int lw, int lane) {
#pragma unroll
    for (int i = 0; i < 2; ++i) { const int k = (lw + i * 4) * 8 + (lane >> 3); vh[i] = b_off + k * ldb_b + (((lane & 7) ^ bswz<64>(k)) * 16); }
  }
  template <int ALLOW = (NS - 2 - LAG) * PW> DI void step() { wait_vm<ALLOW>(); lbar(); issueA(tn); issueB(tn); ++tn; }
  DI void steps(int n) {
#pragma unroll 1
    for (int t = 0; t < n; ++t) step();
  }
  DI void finish() {
    if constexpr (NS - LAG >= 5) { wait_vm<3 * PW>(); lbar(); }
    if constexpr (NS - LAG >= 4) { wait_vm<2 * PW>(); lbar(); }
    if constexpr (NS - LAG >= 3) { wait_vm<1 * PW>(); lbar(); }
    wait_vm<0>(); lbar();
  }
};
template <int BN, int I0, int I1> DI void load_wtile(LAS char* dst, rsrc_t r, int off, int ldb_b, int segb, int lw, int lane) {
#pragma unroll
  for (int i = I0; i < I1; ++i) {
    const int ch = (lw + i * 4) * 64 + lane, k = ch / (BN / 8), c = (ch % (BN / 8)) ^ bswz<BN>(k);
    __builtin_amdgcn_raw_ptr_buffer_load_lds(r, dst + (lw + i * 4) * 1024, 16, off + k * ldb_b + (c >> 3) * segb + (c & 7) * 16, 0, 0, 0);
  }
}
template <int T0, int T1> DI void load_wo(LAS char* smem, rsrc_t ro, int gp, int lw, int lane) {
  int vo[2];
#pragma unroll
  for (int i = 0; i < 2; ++i) { const int k = (lw + i * 4) * 8 + (lane >> 3); vo[i] = gp * 128 + k * (D_ * 2) + (((lane & 7) ^ bswz<64>(k)) * 16); }
#pragma unroll
  for (int t = T0; t < T1; ++t)
#pragma unroll
    for (int i = 0; i < 2; ++i) __builtin_amdgcn_raw_ptr_buffer_load_lds(ro, smem + wo_slot(t) + (lw + i * 4) * 1024, 16, vo[i], t * 64 * (D_ * 2), 0, 0);
}
DI int opaque(int v) { asm volatile("" : "+v"(v)); return v; }

DI void touch_lines(LAS char* smem, rsrc_t r, int base, int line, int nlines) {
  __builtin_amdgcn_raw_ptr_buffer_load_lds(r, smem + L_DUMMY, 4, line < nlines ? base + line * 128 : (int)0x80000000, 0, 0, 0);
}

template <int BN> DI void colsum_tile(LAS char* img, rsrc_t rws, unsigned off, int lw, int lane) {
  constexpr int RS = BN * 2;
  const int r16 = lane & 15, g = lane >> 4, q = (lane & 15) >> 2, p = lane & 3;
  const s16x8 one_ = {0x3f80, 0x3f80, 0x3f80, 0x3f80, 0x3f80, 0x3f80, 0x3f80, 0x3f80};
  LAS const char* rd[4];
#pragma unroll
  for (int i = 0; i < 4; ++i) {
    const int nt = lw + 4 * i < BN / 16 ? lw + 4 * i : lw;
    rd[i] = img + (8 * g + q) * RS + (((2 * nt + (p >> 1)) ^ bswz<BN>(8 * g + q)) * 16) + 8 * (p & 1);
  }
  s16x4 v[4][4];
#define TR4(a, b, c, d, r) "ds_read_b64_tr_b16 %" #a ", %" #r "\n\tds_read_b64_tr_b16 %" #b ", %" #r " offset:%c20\n\t" \
                           "ds_read_b64_tr_b16 %" #c ", %" #r " offset:%c21\n\tds_read_b64_tr_b16 %" #d ", %" #r " offset:%c22\n\t"
  asm volatile(TR4(0, 1, 2, 3, 16) TR4(4, 5, 6, 7, 17) TR4(8, 9, 10, 11, 18) TR4(12, 13, 14, 15, 19) "s_waitcnt lgkmcnt(0)"
               : "=&v"(v[0][0]), "=&v"(v[0][1]), "=&v"(v[0][2]), "=&v"(v[0][3]), "=&v"(v[1][0]), "=&v"(v[1][1]), "=&v"(v[1][2]), "=&v"(v[1][3]),
                 "=&v"(v[2][0]), "=&v"(v[2][1]), "=&v"(v[2][2]), "=&v"(v[2][3]), "=&v"(v[3][0]), "=&v"(v[3][1]), "=&v"(v[3][2]), "=&v"(v[3][3])
               : "v"(rd[0]), "v"(rd[1]), "v"(rd[2]), "v"(rd[3]), "i"(4 * RS), "i"(32 * RS), "i"(36 * RS) : "memory");
#undef TR4
  f32x4 c[4];
#pragma unroll
  for (int i = 0; i < 4; ++i) {
    c[i] = (f32x4){0.f, 0.f, 0.f, 0.f};
    c[i] = MFMA16(__builtin_bit_cast(bf16x8, __builtin_shufflevector(v[i][0], v[i][1], 0, 1, 2, 3, 4, 5, 6, 7)), __builtin_bit_cast(bf16x8, one_), c[i]);
    c[i] = MFMA16(__builtin_bit_cast(bf16x8, __builtin_shufflevector(v[i][2], v[i][3], 0, 1, 2, 3, 4, 5, 6, 7)), __builtin_bit_cast(bf16x8, one_), c[i]);
  }
  const f32x4 out = r16 == 0 ? c[0] : r16 == 1 ? c[1] : r16 == 2 ? c[2] : c[3];
  st128(__builtin_bit_cast(u32x4, out), rws, lw + 4 * r16 < BN / 16 ? off + (lw + 4 * r16) * 64 + g * 16 : 0x80000000u);
}
DI f32x4 sum4(u32x4 (&t)[4]) {
  asm volatile("" : "+v"(t[0]), "+v"(t[1]), "+v"(t[2]), "+v"(t[3]));
  return (__builtin_bit_cast(f32x4, t[0]) + __builtin_bit_cast(f32x4, t[1])) + (__builtin_bit_cast(f32x4, t[2]) + __builtin_bit_cast(f32x4, t[3]));
}
DI f32x4 lds_sum4(LAS const char* p) {
  return (*(LAS const f32x4*)p + *(LAS const f32x4*)(p + 1024)) + (*(LAS const f32x4*)(p + 2048) + *(LAS const f32x4*)(p + 3072));
}
DI unsigned peek(GAS unsigned* ctl, int line, int lane, int n = 64) { return __hip_atomic_load(ctl + line * 64 + (lane & (n - 1)), __ATOMIC_RELAXED, __HIP_MEMORY_SCOPE_AGENT); }
DI void confirm(unsigned seen, GAS unsigned* ctl, int line, unsigned tag, int lane, int n = 64) {
  asm volatile("" : "+v"(seen));
  if (!wave_all(seen == tag)) poll(ctl, line, 0, n, tag, lane);
  else acquire();
}

DI void mfma16(f32x4& c, const bf16x8& a, const bf16x8& b) { asm volatile("v_mfma_f32_16x16x32_bf16 %0, %1, %2, %0" : "+v"(c) : "v"(a), "v"(b)); }
DI void mfma_drain() { asm volatile("s_nop 7\n\ts_nop 7" ::: "memory"); }
template <int TM, int TN> struct Frag { s16x4 b[TN][2]; bf16x8 a[TM]; };
template <int BN, int NS, int NT, int TM, int TN, bool DEEP, bool OWN, class NMap, class Mid, class Send>
DI void mma_loop(LAS char* smem, f32x4 (&acc)[TM][TN], int row0, int lane, NMap&& nt, Mid&& mid, Send&& send) {
  constexpr int RS = BN * 2, BST = BN * 128, NL = TM + 2 * TN, NM = TM * TN;
  typedef Frag<TM, TN> F;
  const int r16 = lane & 15, g = lane >> 4, q = (lane & 15) >> 2, p = lane & 3;
  constexpr bool AROW = BN == 192;
  const int aoff = AROW ? (row0 + r16) * 128 + ((g ^ (r16 >> 1)) * 16) : row0 * 64 + lane * 16;
  const int aoff1 = AROW ? aoff ^ 64 : aoff + 8192;
  int boff[TN];
#pragma unroll
  for (int j = 0; j < TN; ++j) boff[j] = (8 * g + q) * RS + (((2 * nt(j) + (p >> 1)) ^ bswz<BN>(8 * g + q)) * 16) + 8 * (p & 1);
#pragma unroll
  for (int i = 0; i < TM; ++i)
#pragma unroll
    for (int j = 0; j < TN; ++j) { acc[i][j] = (f32x4){0.f, 0.f, 0.f, 0.f}; asm volatile("" : "+v"(acc[i][j])); }
  int boffh[2];
  auto ld1 = [&](F& f, int slot, int s, int k, int tn = TN) {
    if (k == 0 || k > 2 * tn) { const int i = k == 0 ? 0 : k - 2 * tn; f.a[i] = ld_b128(smem + slot * 16384 + (s ? aoff1 : aoff) + i * (AROW ? 2048 : 1024)); }
    else {
      const int j = (k - 1) >> 1, u = (k - 1) & 1;
      f.b[j][u] = __builtin_amdgcn_ds_read_tr16_b64_v4i16((LAS s16x4*)(smem + bslot<BN, NS>(slot) + (tn == TN ? boff[j] + (32 * s + 4 * u) * RS : boffh[j & 1] + (32 * s + 4 * u) * 128)));
    }
  };
  auto mm1 = [&](const F& f, int m, int tn = TN) {
    const int i = m / tn, j = m % tn;
    const bf16x8 b = __builtin_bit_cast(bf16x8, __builtin_shufflevector(f.b[j][0], f.b[j][1], 0, 1, 2, 3, 4, 5, 6, 7));
    if (AROW && j >= 4) mfma16(acc[i][j], f.a[i], b); else mfma16(acc[i][j], b, f.a[i]);
  };
  auto ldall = [&](F& f, int slot, int s) {
#pragma unroll
    for (int k = 0; k < NL; ++k) ld1(f, slot, s, k);
  };
  auto mmall = [&](const F& f) {
#pragma unroll
    for (int m = 0; m < NM; ++m) mm1(f, m);
  };
  auto sub = [&](const F& x, F& u, int slot, int s) {
    constexpr int SP = DEEP ? NM : (NL < NM ? NL : NM - 2);
#pragma unroll
    for (int m = 0; m < NM; ++m) {
      mm1(x, m);
#pragma unroll
      for (int k = (m * NL + SP - 1) / SP; k < NL && k < ((m + 1) * NL + SP - 1) / SP; ++k) ld1(u, slot, s, k);
      __builtin_amdgcn_sched_barrier(0);
    }
  };
  auto subh = [&](const F& x, F& u, int slot, int s, auto tx, auto tu) {
    constexpr int TX = decltype(tx)::value, TU = decltype(tu)::value, M = TM * TX, LN = TM + 2 * TU;
#pragma unroll
    for (int m = 0; m < M; ++m) {
      mm1(x, m, TX);
#pragma unroll
      for (int k = (m * LN + M - 1) / M; k < LN && k < ((m + 1) * LN + M - 1) / M; ++k) ld1(u, slot, s, k, TU);
      __builtin_amdgcn_sched_barrier(0);
    }
  };
  auto nxt = [](int s) { return s == NS - 1 ? 0 : s + 1; };
  lbar();
  if (threadIdx.x < 64) STAMP(BN == 192 ? 0 : BN == 64 ? 1 : BN == 224 ? 2 : 3, 1);
  TS_DECL; TS(0);
  int slot = 0;
  if constexpr (!DEEP) {
    constexpr int NB = AROW ? 2 : TN, NA = AROW ? 2 : 1;
    LAS char* ca[NA];
    LAS char* cb[NB];
    ca[0] = smem + aoff;
    if constexpr (AROW) ca[NA - 1] = smem + aoff1;
#pragma unroll
    for (int j = 0; j < NB; ++j) cb[j] = smem + bslot<BN, NS>(0) + boff[j];
    auto ldc = [&](F& f, int s, int k) {
      if (k == 0 || k > 2 * TN) { const int i = k == 0 ? 0 : k - 2 * TN; f.a[i] = ld_b128(AROW ? ca[s] + i * 2048 : ca[0] + s * 8192 + i * 1024); }
      else {
        const int j = (k - 1) >> 1, u = (k - 1) & 1;
        f.b[j][u] = __builtin_amdgcn_ds_read_tr16_b64_v4i16((LAS s16x4*)((AROW ? cb[j & 1] + (j >> 1) * 128 : cb[j]) + (32 * s + 4 * u) * RS));
      }
    };
    auto subc = [&](const F& x, F& u, int s, bool adv) {
      const int ns = nxt(slot), da = (ns - slot) * 16384, db = bslot<BN, NS>(ns) - bslot<BN, NS>(slot);
#pragma unroll
      for (int m = 0; m < NM; ++m) {
        mm1(x, m);
        if (m < NL) ldc(u, s, m);
        else if (adv && m - NL < NA) asm volatile("v_add_u32 %0, %1, %0" : "+v"(ca[m - NL < NA ? m - NL : 0]) : "s"(da));
        else if (adv && m - NL - NA < NB) asm volatile("v_add_u32 %0, %1, %0" : "+v"(cb[m - NL - NA < NB ? m - NL - NA : 0]) : "s"(db));
        __builtin_amdgcn_sched_barrier(0);
      }
      if (adv) slot = ns;
    };
    static_assert(NL + NA + NB <= NM, "");
    F f0, f1;
#pragma unroll
    for (int k = 0; k < NL; ++k) ldc(f0, 0, k);
    if constexpr (OWN) {
      subc(f0, f1, 1, true);
      mmall(f1);
      __builtin_amdgcn_sched_barrier(0);
      TS(32);
      bar();
      TS(1);
#pragma unroll
      for (int k = 0; k < NL; ++k) ldc(f0, 0, k);
    }
#pragma unroll 1
    for (int t = OWN ? 1 : 0; t < NT - 1; ++t) {
      subc(f0, f1, 1, true);
      TS(32 + t);
      lbar();
      TS(t + 1);
      subc(f1, f0, 0, false);
    }
    subc(f0, f1, 1, false);
    lbar();
    TS(NT); TS_OUT(BN == 192 ? 0 : BN == 64 ? 1 : BN == 224 ? 2 : 3);
    mmall(f1);
  } else {
    F f0, f1, g0, g1;
    auto tile = [&](const F& x0, const F& x1, F& u0, F& u1, int t) {
      TS(32 + t);
      bar();
      TS(t + 1);
      slot = nxt(slot);
      sub(x0, u0, slot, 0);
      sub(x1, u1, slot, 1);
    };
    ldall(f0, 0, 0); ldall(f1, 0, 1);
    int t0 = 0;
    if constexpr (OWN) {
      tile(f0, f1, g0, g1, 0);
      tile(g0, g1, f0, f1, 1);
      mmall(f0); mmall(f1);
      __builtin_amdgcn_sched_barrier(0);
      TS(34);
      lbar();
      TS(3);
      slot = 3;
      ldall(g0, 3, 0); ldall(g1, 3, 1);
      tile(g0, g1, f0, f1, 3);
      t0 = 4;
    }
    if constexpr (!OWN) {
#pragma unroll 1
      for (int t = t0; t < NT - 2; t += 2) {
        tile(f0, f1, g0, g1, t);
        tile(g0, g1, f0, f1, t + 1);
      }
      tile(f0, f1, g0, g1, NT - 2);
      lbar();
      TS(NT); TS_OUT(BN == 192 ? 0 : BN == 64 ? 1 : BN == 224 ? 2 : 3);
      mmall(g0); mmall(g1);
    } else {
#pragma unroll 1
      for (int t = t0; t < 24; t += 2) {
        tile(f0, f1, g0, g1, t);
        if (t == 4) mid();
        tile(g0, g1, f0, f1, t + 1);
      }
      typedef std::integral_constant<int, TN> Full;
      typedef std::integral_constant<int, TN / 2> Half;
      auto tileh = [&](const F& x0, const F& x1, F& u0, F& u1, auto tx) {
        bar();
        slot = nxt(slot);
        subh(x0, u0, slot, 0, tx, Half{});
        subh(x1, u1, slot, 1, tx, Half{});
      };
#pragma unroll
      for (int j = 0; j < 2; ++j) {
        const int l2 = opaque(lane), k = 8 * (l2 >> 4) + ((l2 & 15) >> 2);
        boffh[j] = k * 128 + (((2 * nt(j) + ((l2 & 3) >> 1)) ^ bswz<64>(k)) * 16) + 8 * (l2 & 1);
      }
      tileh(f0, f1, g0, g1, Full{});
      mfma_drain();
#pragma unroll
      for (int i = 0; i < TM; ++i)
#pragma unroll
        for (int j = TN / 2; j < TN; ++j) asm volatile("" : "+v"(acc[i][j]));
      send(0);
      __builtin_amdgcn_sched_barrier(0);
      tileh(g0, g1, f0, f1, Half{});
      tileh(f0, f1, g0, g1, Half{});
      send(1);
      tileh(g0, g1, f0, f1, Half{});
      tileh(f0, f1, g0, g1, Half{});
      send(2);
      tileh(g0, g1, f0, f1, Half{});
      send(3);
      lbar();
      TS(28); TS_OUT(3);
#pragma unroll
      for (int m = 0; m < TM * TN / 2; ++m) mm1(f0, m, TN / 2);
#pragma unroll
      for (int m = 0; m < TM * TN / 2; ++m) mm1(f1, m, TN / 2);
    }
  }
  mfma_drain();
#pragma unroll
  for (int i = 0; i < TM; ++i)
#pragma unroll
    for (int j = 0; j < TN; ++j) asm volatile("" : "+v"(acc[i][j]));
}
DI void swap_pair(f32x4& x, f32x4& y) {
#pragma unroll
  for (int e = 0; e < 4; ++e) {
    auto r = __builtin_amdgcn_permlane16_swap(f2u(x[e]), f2u(y[e]), false, false);
    x[e] = u2f(r[0]); y[e] = u2f(r[1]);
  }
}
DI void add_bf8(f32x4& x, f32x4& y, const u32x4& rv) {
  x[0] += bflo(rv[0]); x[1] += bfhi(rv[0]); x[2] += bflo(rv[1]); x[3] += bfhi(rv[1]);
  y[0] += bflo(rv[2]); y[1] += bfhi(rv[2]); y[2] += bflo(rv[3]); y[3] += bfhi(rv[3]);
}
DI u32x4 pack_xy(const f32x4& x, const f32x4& y) { return (u32x4){pk2(x[0], x[1]), pk2(x[2], x[3]), pk2(y[0], y[1]), pk2(y[2], y[3])}; }

template <class Op> DI void red2(float& a, float& b, Op&& op) {
  auto r = __builtin_amdgcn_permlane16_swap(f2u(a), f2u(b), false, false);
  const float c = op(u2f(r[0]), u2f(r[1]));
  auto t = __builtin_amdgcn_permlane32_swap(f2u(c), f2u(c), false, false);
  const float d = op(u2f(t[0]), u2f(t[1]));
  auto u = __builtin_amdgcn_permlane16_swap(f2u(d), f2u(d), false, false);
  a = u2f(u[0]); b = u2f(u[1]);
}
DI void dual_softmax(const f32x4 (&p)[2][2], const float (&sc)[2], f32x4 (&e)[2][2][2]) {
  float mx[2], mn[2], s1[2], s2[2];
#pragma unroll
  for (int t = 0; t < 2; ++t) {
    mx[t] = p[t][0][0]; mn[t] = p[t][0][0];
#pragma unroll
    for (int r = 1; r < 8; ++r) { mx[t] = __builtin_fmaxf(mx[t], p[t][r >> 2][r & 3]); mn[t] = __builtin_fminf(mn[t], p[t][r >> 2][r & 3]); }
  }
  red2(mx[0], mx[1], [](float x, float y) { return __builtin_fmaxf(x, y); });
  red2(mn[0], mn[1], [](float x, float y) { return __builtin_fminf(x, y); });
#pragma unroll
  for (int t = 0; t < 2; ++t) {
    const float a = -mx[t] * sc[t], b = mn[t] * sc[t];
    s1[t] = 0.f; s2[t] = 0.f;
#pragma unroll
    for (int r = 0; r < 8; ++r) {
      const float x = __builtin_amdgcn_exp2f(__builtin_fmaf(p[t][r >> 2][r & 3], sc[t], a));
      const float y = __builtin_amdgcn_exp2f(__builtin_fmaf(p[t][r >> 2][r & 3], -sc[t], b));
      e[0][t][r >> 2][r & 3] = x; e[1][t][r >> 2][r & 3] = y;
      s1[t] += x; s2[t] += y;
    }
  }
  red2(s1[0], s1[1], [](float x, float y) { return x + y; });
  red2(s2[0], s2[1], [](float x, float y) { return x + y; });
#pragma unroll
  for (int t = 0; t < 2; ++t) {
    const float r1 = __builtin_amdgcn_rcpf(s1[t]), r2 = __builtin_amdgcn_rcpf(s2[t]);
#pragma unroll
    for (int r = 0; r < 8; ++r) { e[0][t][r >> 2][r & 3] *= r1; e[1][t][r >> 2][r & 3] *= r2; }
  }
}
DI bf16x8 pack_frag(const f32x4& x, const f32x4& y) { return __builtin_bit_cast(bf16x8, pack_xy(x, y)); }
DI void st4(LAS char* p, const f32x4& a, const f32x4& b, const f32x4& c, const f32x4& d) {
  *(LAS f32x4*)p = a; *(LAS f32x4*)(p + 1024) = b; *(LAS f32x4*)(p + 2048) = c; *(LAS f32x4*)(p + 3072) = d;
}
DI void ld4(LAS const char* p, f32x4 (&v)[2][2]) {
  v[0][0] = *(LAS const f32x4*)p; v[0][1] = *(LAS const f32x4*)(p + 1024); v[1][0] = *(LAS const f32x4*)(p + 2048); v[1][1] = *(LAS const f32x4*)(p + 3072);
}
#endif

__global__ __launch_bounds__(512, 2) void block_kernel(const u16* __restrict__ X, const u16* __restrict__ WQKV, const u16* __restrict__ WPHI,
                                                        const u16* __restrict__ WO, const u16* __restrict__ W1, const u16* __restrict__ W2,
                                                        u16* __restrict__ OUT, char* WS) {
#if defined(__HIP_DEVICE_COMPILE__)
  extern __shared__ __attribute__((aligned(16))) char smem_[];
  LAS char* smem = (LAS char*)smem_;
  const int tid = threadIdx.x, wid = __builtin_amdgcn_readfirstlane(tid >> 6);
  int lane = tid & 63;
  const int bid = blockIdx.x, xcd = bid & 7, loc = bid >> 3;
  const int tm = (xcd & 1) * 8 + (loc & 7), gp = (xcd >> 1) * 4 + (loc >> 3);
  const bool loader = wid >= 4;
  const int lw = wid & 3;
  GAS unsigned* ctl = (GAS unsigned*)WS;
  const rsrc_t rws = mkrsrc(WS, WS_END);
  if (wid == CPW) {
    unsigned long long pc;
    asm volatile("s_getpc_b64 %0" : "=s"(pc));
    touch_lines(smem, mkrsrc((const void*)(pc & ~127ull), CODE_AHEAD), loc * 2048, lane < 16 ? lane : 4096, 16);
  }
  if (wid == 0) STAMP(0, 0);
  unsigned tag = 0;

  int r16, g, cpair, cswz;
  auto relane = [&]() {
    lane = opaque(lane);
    r16 = lane & 15; g = lane >> 4;
    cpair = (g & 1) * 16 + (g >> 1) * 8;
    cswz = (cpair >> 3) * 256 + r16 * 16;
  };
  {
    constexpr int NT = 16, NS = 3;
    constexpr int L_PHI = 0;
    constexpr int L_XV = 2 * 17408;
    constexpr int L_VF = L_XV + 16384;
    constexpr int L_XQ = L_VF + 8192;
    constexpr int L_KVT = L_XQ;
    constexpr int L_WF = L_XQ + 16384;
    static_assert(L_WF + 4096 <= 5 * 16384, "phase 2's B ring is filled during this epilogue");
    relane();
    const int wm = (wid >> 1) & 1, hh = wid & 1, mi = wid >> 2;
    const int head = gp * 2 + hh;
    const unsigned kvp_grp = WS_KVP + gp * 16 * KVP_BYTES;
    LAS char* const xk = smem + L_PHI + hh * 17408 + (wm * 64 + 32) * 136 + lane * 16;
    bf16x8 wf[2];
    f32x4 fq[2][2], fk[2][2], fv[2][2];
    if (loader) {
      const rsrc_t rb = mkrsrc(WQKV, D_ * 3 * D_ * 2), ro = mkrsrc(WO, D_ * D_ * 2);
      Loader<192, NS, NT, true> L;
      const rsrc_t rx = mkrsrc(X, S_ * D_ * 2);
      L.init(smem, rx, rb, tm * 128 * D_ * 2, D_ * 2, gp * 128, 3 * D_ * 2, 2048, lw, lane, (tm & 3) * 4);
      L.issueA(0); L.issueB(0); L.issueA(1); L.issueB(1);
      if (lw == 0) *(LAS volatile unsigned*)(smem + L_GO) = 0u;
      tag = read_tag(WS);
      __builtin_amdgcn_s_setprio(3);
      if (wid == 4) STAMP(4, 1);
      auto cs1 = [&](int t) { if ((tm >> 2) == t) colsum_tile<192>(smem + NS * 16384 + (t % NS) * 24576, rws, WS_CS1 + (gp * 16 + tm) * 768, lw, lane); };
      L.step();
      cs1(0);
      const int xrow = tm * 128 + gp * 8 + lw * 2 + (lane >> 5);
      u32x4 xv[4];
#pragma unroll
      for (int j = 0; j < 4; ++j) xv[j] = __builtin_amdgcn_raw_buffer_load_b128(rx, xrow * (D_ * 2) + (lane & 31) * 16, j * 512, 0);
      L.step<14>();
      cs1(1);
      L.step<14>();
      cs1(2);
      L.step();
      cs1(3);
      {
        asm volatile("" : "+v"(xv[0]), "+v"(xv[1]), "+v"(xv[2]), "+v"(xv[3]));
        float f[32], sum = 0.f, qq = 0.f;
#pragma unroll
        for (int i = 0; i < 16; ++i) { f[2 * i] = bflo(xv[i >> 2][i & 3]); f[2 * i + 1] = bfhi(xv[i >> 2][i & 3]); sum += f[2 * i] + f[2 * i + 1]; }
        const float mean = half_sum(sum) * (1.0f / D_);
#pragma unroll
        for (int i = 0; i < 32; ++i) { const float d = f[i] - mean; qq = __builtin_fmaf(d, d, qq); }
        const float rstd = 1.0f / __builtin_sqrtf(half_sum(qq) * (1.0f / D_) + LN_EPS);
        __builtin_amdgcn_raw_buffer_store_b64(__builtin_bit_cast(u32x2, (f32x2){mean, rstd}), rws, (lane & 31) == 0 ? WS_XST + xrow * 8 : 0x80000000u, 0, SC1);
      }
      L.step();
      L.step<0>();
      if (lane == 0) { set_flag(ctl, C_CS1 + gp, tm * 4 + lw, tag); set_flag(ctl, C_ST + tm, gp * 4 + lw, tag); }
      touch_lines(smem, ro, bid * 8192, lane < 16 ? lw * 16 + lane : 4096, 64);
      L.step();
      L.step<13>();
      L.steps(3);
      const unsigned seen = peek(ctl, C_CS1 + gp, lane), seen2 = peek(ctl, C_ST + tm, lane);
      L.steps(2);
      confirm(seen, ctl, C_CS1 + gp, tag, lane);
      confirm(seen2, ctl, C_ST + tm, tag, lane);
      u32x4 gt[4];
#pragma unroll
      for (int j = 0; j < 4; ++j) gt[j] = ld128(rws, WS_CS1 + (gp * 16 + lw * 4) * 768 + lane * 16, j * 768);
      u32x4 xs = ld128(rws, WS_XST + tm * 1024 + lane * 16, 0);
      L.step();
      wait_vm<10>(); lbar();
      *(LAS f32x4*)(smem + L_CSS + lw * 1024 + lane * 16) = sum4(gt);
      asm volatile("" : "+v"(xs));
      if (lw == 1) *(LAS u32x4*)(smem + L_RST + lane * 16) = xs;
      wait_vm<0>(); lbar();
      if (lw == 0) *(LAS f32x4*)(smem + L_CS + lane * 16) = lds_sum4(smem + L_CSS + lane * 16);
      const rsrc_t r1 = mkrsrc(W1, D_ * FF_ * 2);
      load_wtile<224, 0, 4>(smem + L_WT, r1, tm * 64 * (FF_ * 2) + gp * 448, FF_ * 2, 128, lw, lane);
      lbar();
      bar();
      __builtin_amdgcn_s_setprio(1);
      ld4(xk, fk);
      wf[0] = ld_b128(smem + L_WF + hh * 2048 + lane * 16);
      wf[1] = ld_b128(smem + L_WF + hh * 2048 + 1024 + lane * 16);
      ld4(smem + L_XV + lw * 4096 + lane * 16, fv);
    } else {
      {
#pragma unroll
        for (int t = 0; t < 2; ++t) {
          s16x8 w;
#pragma unroll
          for (int j = 0; j < 8; ++j) w[j] = (short)WPHI[head * 1024 + (16 * (j >> 2) + 4 * g + (j & 3)) * 32 + 16 * t + r16];
          wf[t] = __builtin_bit_cast(bf16x8, w);
        }
        tag = read_tag(WS);
      }
      if (wid == 0) STAMP(4, 0);
      f32x4 acc[4][6];
      mma_loop<192, NS, NT, 4, 6, false, false>(smem, acc, wm * 64, lane, [&](int j) { return (j >> 1) * 4 + hh * 2 + (j & 1); }, []() {}, [](int) {});
      if (wid == 0) STAMP(0, 2);
      if (wm == 0) {
        *(LAS bf16x8*)(smem + L_WF + hh * 2048 + lane * 16) = wf[0];
        *(LAS bf16x8*)(smem + L_WF + hh * 2048 + 1024 + lane * 16) = wf[1];
      }
      st4(xk, acc[2][2], acc[2][3], acc[3][2], acc[3][3]);
      st4(smem + L_XV + wid * 4096 + lane * 16, acc[2][4], acc[2][5], acc[3][4], acc[3][5]);
      lbar();
      if (wid == 0) STAMP(4, 4);
      st4(smem + L_XQ + wid * 4096 + lane * 16, acc[2][0], acc[2][1], acc[3][0], acc[3][1]);
#pragma unroll
      for (int t = 0; t < 2; ++t)
#pragma unroll
        for (int u = 0; u < 2; ++u) { fq[t][u] = acc[t][u]; fk[t][u] = acc[t][2 + u]; fv[t][u] = acc[t][4 + u]; }
    }
    const int rbase = wm * 64 + mi * 32, q = r16 >> 2, p = lane & 3;
    LAS const float* cs = (LAS const float*)(smem + L_CS) + hh * 32;
    LAS const f32x2* rst = (LAS const f32x2*)(smem + L_RST) + rbase;
    auto feature = [&](const f32x4 (&x)[2][2], int part, f32x4 (&e)[2][2][2]) {
      const f32x4 c0 = *(LAS const f32x4*)(cs + part * 64 + 4 * g), c1 = *(LAS const f32x4*)(cs + part * 64 + 16 + 4 * g);
      f32x4 pp[2][2];
      float sc[2];
#pragma unroll
      for (int t = 0; t < 2; ++t) {
        const f32x2 ms = rst[t * 16 + r16];
        sc[t] = ms[1] * 1.4426950408889634f;
        f32x4 y0, y1;
#pragma unroll
        for (int e_ = 0; e_ < 4; ++e_) { y0[e_] = __builtin_fmaf(-ms[0], c0[e_], x[t][0][e_]); y1[e_] = __builtin_fmaf(-ms[0], c1[e_], x[t][1][e_]); }
        const bf16x8 yb = pack_frag(y0, y1);
        pp[t][0] = MFMA16(wf[0], yb, ((f32x4){0.f, 0.f, 0.f, 0.f}));
        pp[t][1] = MFMA16(wf[1], yb, ((f32x4){0.f, 0.f, 0.f, 0.f}));
      }
      dual_softmax(pp, sc, e);
    };
    if (wid == 0) STAMP(4, 5);
    {
      const float cv0 = cs[128 + r16], cv1 = cs[144 + r16];
#pragma unroll
      for (int t = 0; t < 2; ++t) {
        const f32x4 s01 = *(LAS const f32x4*)(rst + t * 16 + 4 * g), s23 = *(LAS const f32x4*)(rst + t * 16 + 4 * g + 2);
        const float mv[4] = {s01[0], s01[2], s23[0], s23[2]}, rv[4] = {s01[1], s01[3], s23[1], s23[3]};
#pragma unroll
        for (int e_ = 0; e_ < 4; ++e_) {
          fv[t][0][e_] = __builtin_fmaf(-mv[e_], cv0, fv[t][0][e_]) * rv[e_];
          fv[t][1][e_] = __builtin_fmaf(-mv[e_], cv1, fv[t][1][e_]) * rv[e_];
        }
      }
      LAS char* vd = smem + (loader ? L_XV + lw * 4096 : L_VF + lw * 2048) + lane * 16;
      *(LAS bf16x8*)vd = pack_frag(fv[0][0], fv[1][0]);
      *(LAS bf16x8*)(vd + 1024) = pack_frag(fv[0][1], fv[1][1]);
      f32x4 e[2][2][2];
      feature(fk, 1, e);
      LAS char* wr = smem + L_PHI + hh * 17408 + (rbase + r16) * 136 + g * 8;
#pragma unroll
      for (int w = 0; w < 2; ++w)
#pragma unroll
        for (int t = 0; t < 2; ++t)
#pragma unroll
          for (int u = 0; u < 2; ++u)
            *(LAS u32x2*)(wr + t * 16 * 136 + w * 64 + u * 32) = (u32x2){pk2(e[w][t][u][0], e[w][t][u][1]), pk2(e[w][t][u][2], e[w][t][u][3])};
    }
    if (wid == 0) STAMP(4, 7);
    if (wid == 4) STAMP(4, 9);
    lbar();
    __builtin_amdgcn_s_setprio(0);
    if (wid == 0) STAMP(4, 8);
    bf16x8 pf[2][2];
    auto qpath = [&]() {
      f32x4 e[2][2][2];
      feature(fq, 0, e);
#pragma unroll
      for (int t = 0; t < 2; ++t)
#pragma unroll
        for (int w = 0; w < 2; ++w) { pf[t][w] = pack_frag(e[w][t][0], e[w][t][1]); asm volatile("" : "+v"(pf[t][w])); }
    };
    if (loader) {
      ld4(smem + L_XQ + lw * 4096 + lane * 16, fq);
      qpath();
      load_wtile<224, 4, 7>(smem + L_WT, mkrsrc(W1, D_ * FF_ * 2), tm * 64 * (FF_ * 2) + gp * 448, FF_ * 2, 128, lw, lane);
    } else {
      f32x4 k00 = {0.f, 0.f, 0.f, 0.f}, k01 = k00, k10 = k00, k11 = k00, z0 = k00, z1 = k00;
      const s16x8 one_ = {0x3f80, 0x3f80, 0x3f80, 0x3f80, 0x3f80, 0x3f80, 0x3f80, 0x3f80};
      LAS const char* rd = smem + L_PHI + hh * 17408 + (4 * g + q) * 136 + wm * 64 + p * 8;
#pragma unroll
      for (int u = 0; u < 4; ++u) {
        const bf16x8 pa0 = ld_tr2(rd + 32 * u * 136, rd + (32 * u + 16) * 136), pa1 = ld_tr2(rd + 32 * u * 136 + 32, rd + (32 * u + 16) * 136 + 32);
        LAS const char* vp = smem + ((u & 1) ? L_XV + ((u >> 1) * 2 + hh) * 4096 : L_VF + ((u >> 1) * 2 + hh) * 2048) + lane * 16;
        const bf16x8 v0 = ld_b128(vp), v1 = ld_b128(vp + 1024);
        k00 = MFMA16(pa0, v0, k00); k10 = MFMA16(pa1, v0, k10);
        k01 = MFMA16(pa0, v1, k01); k11 = MFMA16(pa1, v1, k11);
        z0 = MFMA16(pa0, __builtin_bit_cast(bf16x8, one_), z0); z1 = MFMA16(pa1, __builtin_bit_cast(bf16x8, one_), z1);
      }
      const int kb = kvp_grp + tm * KVP_BYTES;
      st128(pack_xy(k00, k10), rws, kb + hh * 4096 + wm * 2048 + lane * 16);
      st128(pack_xy(k01, k11), rws, kb + hh * 4096 + wm * 2048 + 1024 + lane * 16);
      st128(__builtin_bit_cast(u32x4, r16 == 0 ? z0 : z1), rws, r16 < 2 ? kb + 8192 + hh * 256 + wm * 128 + g * 32 + r16 * 16 : (int)0x80000000);
      if (wid == 0) STAMP(0, 3);
      wait_vm<0>();
    }
    lbar();
    if (tid == 0) set_flag(ctl, C_KV + gp, tm, tag);
    if (wid == 0) STAMP(0, 4);
    if (loader) {
      load_wo<0, WO1>(smem, mkrsrc(WO, D_ * D_ * 2), gp, lw, lane);
    } else {
      qpath();
    }
    FastPoll fpk = {0u, 0u, 0u, 0u};
    if (wid == 0) {
      STAMP(0, 5);
      fpk.run(ctl, C_KV + gp, 0, 16, tag, lane);
    }
    bar();
    if (wid == 0) STAMP(0, 6);
    {
      u32x4 tz[16];
      if (wid == 7) {
#pragma unroll
        for (int j = 0; j < 16; ++j) tz[j] = ld128(rws, lane < 32 ? kvp_grp + 8192 + lane * 16 : 0x80000000u, j * KVP_BYTES);
      }
      u32x4 t[16];
#pragma unroll
      for (int j = 0; j < 16; ++j) t[j] = ld128(rws, kvp_grp + tid * 16, j * KVP_BYTES);
      if (wid == 7) {
        f32x4 z = __builtin_bit_cast(f32x4, tz[0]);
#pragma unroll
        for (int j = 1; j < 16; ++j) z += __builtin_bit_cast(f32x4, tz[j]);
        if (lane < 32) *(LAS u32x2*)(smem + L_KVT + 8192 + lane * 8) = (u32x2){pk2(z[0], z[1]), pk2(z[2], z[3])};
      }
      f32x4 x = {0.f, 0.f, 0.f, 0.f}, y = x;
#pragma unroll
      for (int j = 0; j < 16; ++j) add_bf8(x, y, t[j]);
      *(LAS u32x4*)(smem + L_KVT + tid * 16) = pack_xy(x, y);
    }
    fpk.keep();
    lbar();
    if (wid == 0) STAMP(0, 7);
    {
      bf16x8 ka[2][2], za[2];
#pragma unroll
      for (int w = 0; w < 2; ++w) {
        ka[w][0] = ld_b128(smem + L_KVT + hh * 4096 + w * 2048 + lane * 16);
        ka[w][1] = ld_b128(smem + L_KVT + hh * 4096 + w * 2048 + 1024 + lane * 16);
        za[w] = ld_b128(smem + L_KVT + 8192 + hh * 128 + w * 64 + g * 16);
      }
#pragma unroll
      for (int t = 0; t < 2; ++t) {
        f32x4 o0 = {0.f, 0.f, 0.f, 0.f}, o1 = o0, dn = o0;
#pragma unroll
        for (int w = 0; w < 2; ++w) { dn = MFMA16(za[w], pf[t][w], dn); o0 = MFMA16(ka[w][0], pf[t][w], o0); o1 = MFMA16(ka[w][1], pf[t][w], o1); }
        const float rd = 1.0f / (dn[0] + 1e-6f);
#pragma unroll
        for (int e_ = 0; e_ < 4; ++e_) { o0[e_] *= rd; o1[e_] *= rd; }
        swap_pair(o0, o1);
        st128(pack_xy(o0, o1), rws, WS_O + (tm * 32 + head) * 8192 + (rbase + t * 16) * 64 + cswz);
      }
    }
    wait_vm<0>();
    lbar();
    if (tid == 0) set_flag(ctl, C_O + tm, gp, tag);
    if (wid == 0) STAMP(0, 8);
  }

  relane();
  {
    FastPoll fp = {0u, 0u, 0u, 0u};
    if (loader) {
      load_wo<WO1, 16>(smem, mkrsrc(WO, D_ * D_ * 2), gp, lw, lane);
      colsum_tile<224>(smem + L_WT, rws, WS_CS3 + (gp * 16 + tm) * 896, lw, lane);
    }
    if (wid == 0) {
      fp.run(ctl, C_O + tm, 0, 16, tag, lane);
      STAMP(1, 0);
      if (bid == 0 && lane == 0) __hip_atomic_store(ctl + C_EPOCH * 64, tag, __ATOMIC_RELAXED, __HIP_MEMORY_SCOPE_AGENT);
    }
    if (loader) {
      wait_vm<0>();
      bar();
      if (lane == 0) set_flag(ctl, C_CS3 + gp, tm * 4 + lw, tag);
    } else {
      const int row = tm * 128 + wid * 32 + r16, col = gp * 64 + cpair;
      u32x4 res[2][2];
#pragma unroll
      for (int i = 0; i < 2; ++i)
#pragma unroll
        for (int j = 0; j < 2; ++j) res[i][j] = *(const u32x4*)(X + (size_t)(row + i * 16) * D_ + col + j * 32);
      f32x4 acc[2][4];
#pragma unroll
      for (int j = 0; j < 8; ++j) { acc[j >> 2][j & 3] = (f32x4){0.f, 0.f, 0.f, 0.f}; asm volatile("" : "+v"(acc[j >> 2][j & 3])); }
      constexpr int PF = PFD, BD = BDEPTH;
      const int q = r16 >> 2, p = lane & 3;
      int bo[4];
#pragma unroll
      for (int j = 0; j < 4; ++j) bo[j] = (8 * g + q) * 128 + (((2 * j + (p >> 1)) ^ bswz<64>(8 * g + q)) * 16) + 8 * (p & 1);
      const int ao = WS_O + tm * 262144 + wid * 2048 + lane * 16;
      u32x4 af[PF][2];
      s16x4 bf[BD][4][2];
      auto ldb = [&](int sl, int k) {
        bf[sl % BD][k >> 1][k & 1] = __builtin_amdgcn_ds_read_tr16_b64_v4i16((LAS s16x4*)(smem + wo_slot(sl >> 1) + bo[k >> 1] + (32 * (sl & 1) + 4 * (k & 1)) * 128));
      };
      bar();
      if (wid == 0) STAMP(1, 1);
#pragma unroll
      for (int sl = 0; sl < PF; ++sl) { af[sl][0] = ld128(rws, ao, sl * 8192); af[sl][1] = ld128(rws, ao + 1024, sl * 8192); }
#pragma unroll
      for (int k = 0; k < 8 * (BD - 1); ++k) ldb(k >> 3, k & 7);
      __builtin_amdgcn_sched_barrier(0);
#pragma unroll
      for (int sl = 0; sl < 32; ++sl) {
#pragma unroll
        for (int m = 0; m < 8; ++m) {
          const int i = m >> 2, j = m & 3;
          mfma16(acc[i][j], __builtin_bit_cast(bf16x8, __builtin_shufflevector(bf[sl % BD][j][0], bf[sl % BD][j][1], 0, 1, 2, 3, 4, 5, 6, 7)),
                 __builtin_bit_cast(bf16x8, af[sl % PF][i]));
          if (sl + BD - 1 < 32) ldb(sl + BD - 1, m);
          if (j == 3 && sl + PF < 32) af[sl % PF][i] = ld128(rws, ao + i * 1024, (sl + PF) * 8192);
          __builtin_amdgcn_sched_barrier(0);
        }
        if (sl == 30) lbar();
      }
      fp.keep();
      mfma_drain();
#pragma unroll
      for (int j = 0; j < 8; ++j) asm volatile("" : "+v"(acc[j >> 2][j & 3]));
      if (wid == 0) STAMP(1, 2);
#pragma unroll
      for (int i = 0; i < 2; ++i) {
        float s = 0.f, qq = 0.f;
#pragma unroll
        for (int j = 0; j < 2; ++j) {
          f32x4 x = acc[i][2 * j], y = acc[i][2 * j + 1];
          swap_pair(x, y);
          add_bf8(x, y, res[i][j]);
          const u32x4 o = pack_xy(x, y);
          st128(o, rws, WS_H + (tm * 32 + gp * 2 + j) * 8192 + (wid * 32 + i * 16) * 64 + cswz);
          *(LAS u32x4*)(smem + j * 8192 + (wid * 32 + i * 16) * 64 + cswz) = o;
#pragma unroll
          for (int e = 0; e < 4; ++e) { const float lo = bflo(o[e]), hi = bfhi(o[e]); s += lo + hi; qq = __builtin_fmaf(lo, lo, __builtin_fmaf(hi, hi, qq)); }
        }
        red2(s, qq, [](float x, float y) { return x + y; });
        __builtin_amdgcn_raw_buffer_store_b64(__builtin_bit_cast(u32x2, (f32x2){s, qq}), rws,
                                              g == 0 ? WS_HST + ((tm * 16 + gp) * 128 + wid * 32 + i * 16 + r16) * 8 : 0x80000000u, 0, SC1);
      }
      if (wid == 0) STAMP(1, 3);
    }
  }

  const int kh = gp >> 3;
  if (loader) {
    __builtin_amdgcn_s_setprio(3);
    relane();
    const rsrc_t r2 = mkrsrc(W2, FF_ * D_ * 2);
    FastPoll fp;
    {
      Loader<224, 3, 16, false> L;
      L.init(smem, rws, mkrsrc(W1, D_ * FF_ * 2), WS_H + tm * 128 * D_ * 2, 0, gp * 448, FF_ * 2, 128, lw, lane, gp);
      __builtin_amdgcn_s_sleep(B3DELAY);
      L.issueB(0);
      bar();
      bar();
      if (wid == 4 && lane == 0) set_flag(ctl, C_H + tm, gp, tag);
      wait_vm<0>(); bar();
      L.issueB(1); L.issueB(2);
      poll4(fp, smem, 2, lw, ctl, C_H + tm, 0, 16, tag, lane);
      if (wid == 4) STAMP(2, 0);
      L.issueA(1); L.issueA(2);
      u32x4 gt[4], hs[4];
#pragma unroll
      for (int j = 0; j < 4; ++j) hs[j] = ld128(rws, WS_HST + tm * 16384 + lw * 4096 + lane * 16, j * 1024);
      const unsigned seen = peek(ctl, C_CS3 + gp, lane);
      L.tn = 3;
      L.step<4 + 5>();
      fp.keep();
      L.step<5 + 11>();
      confirm(seen, ctl, C_CS3 + gp, tag, lane);
#pragma unroll
      for (int j = 0; j < 4; ++j) gt[j] = ld128(rws, WS_CS3 + (gp * 16 + lw * 4) * 896 + lane * 16, j * 896);
      *(LAS f32x4*)(smem + L_CSS + 4096 + lw * 1024 + lane * 16) = sum4(hs);
      L.step();
      if (lw == 1) {
        const f32x4 sum = lds_sum4(smem + L_CSS + 4096 + lane * 16);
        const float m0 = sum[0] * (1.0f / D_), m1 = sum[2] * (1.0f / D_);
        const float v0 = __builtin_fmaxf(sum[1] * (1.0f / D_) - m0 * m0, 0.f), v1 = __builtin_fmaxf(sum[3] * (1.0f / D_) - m1 * m1, 0.f);
        *(LAS f32x4*)(smem + L_RST + lane * 16) = (f32x4){m0, 1.0f / __builtin_sqrtf(v0 + LN_EPS), m1, 1.0f / __builtin_sqrtf(v1 + LN_EPS)};
      }
      L.step();
      *(LAS f32x4*)(smem + L_CSS + lw * 1024 + lane * 16) = sum4(gt);
      L.step();
      if (lw == 0) *(LAS f32x4*)(smem + L_CS + lane * 16) = lds_sum4(smem + L_CSS + lane * 16);
      const int stag = (loc & 7) < 6 ? (loc & 7) : 6;
      L.steps(stag);
      touch_lines(smem, r2, bid * 28672, lane < 56 ? lw * 56 + lane : 4096, 224);
      L.step();
      L.step<12>();
      L.steps(6 - stag);
    }
    {
      relane();
      Loader<128, 4, 31, false, 1> L;
      L.kh = kh;
      L.init(smem, rws, r2, WS_H1 + tm * 112 * 8192, 0, gp * 128, D_ * 2, kh ? -1024 : 1024, lw, lane, gp & 7);
      L.issueB(0);
      wait_vm<11 + 4>(); lbar(); wait_vm<4>(); bar();
      bar();
      L.issueB(1); L.issueB(2); L.issueB(3);
      wait_vm<12>(); bar();
      if (wid == 4 && lane == 0) set_flag(ctl, C_H1 + tm, gp, tag);
      wait_vm<8>(); bar();
      wait_vm<4>(); bar();
      L.issueB(4);
      poll4(fp, smem, 3, lw, ctl, C_H1 + tm, kh * 8, 8, tag, lane);
      if (wid == 4) STAMP(3, 0);
      L.issueA(3); L.issueA(4);
      L.tn = 5;
      L.step<4>();
      fp.keep();
      L.steps(3);
      if (wid == 4 && lane == 0) set_flag(ctl, C_H1B + tm, gp, tag);
      L.steps(5);
      const unsigned seen = peek(ctl, C_H1B + tm, lane, 16);
      L.steps(3);
      confirm(seen, ctl, C_H1B + tm, tag, lane, 16);
      L.steps(8);
      L.init_half(gp * 128, D_ * 2, lw, lane);
      wait_vm<8>(); bar(); L.issueH(25);
      wait_vm<6>(); bar(); L.issueH(26);
#pragma unroll 1
      for (int t = 27; t <= 30; ++t) { wait_vm<6>(); bar(); L.issueH(t); }
      wait_vm<6>(); bar();
      wait_vm<0>(); bar();
      bar();
    }
  } else {
    const int wm = wid >> 1, wn = wid & 1;
    relane();
    wait_vm<0>();
    bar();
    {
      const int par = gp & 1;
      auto slab3 = [&](int a) { return wn * 4 + (a + par == 3 ? 0 : a + par); };
      auto ntile3 = [&](int j) { return j < 6 ? slab3(j >> 1) * 2 + (j & 1) : 6 + wn; };
      f32x4 acc[4][7];
      mma_loop<224, 3, 16, 4, 7, false, true>(smem, acc, wm * 64, lane, ntile3, []() {}, [](int) {});
      if (wid == 0) STAMP(2, 2);
      relane();
      f32x4 cs[7];
#pragma unroll
      for (int j = 0; j < 7; ++j) cs[j] = *(LAS const f32x4*)(smem + L_CS + (ntile3(j) * 16 + g * 4) * 4);
      float fa[4], fb[4];
#pragma unroll
      for (int i = 0; i < 4; ++i) {
        const f32x2 ms = *(LAS const f32x2*)(smem + L_RST + (wm * 64 + i * 16 + r16) * 8);
        fa[i] = ms[1]; fb[i] = -ms[0] * ms[1];
      }
      auto act = [&](int i, int j) {
        f32x4 y;
#pragma unroll
        for (int e = 0; e < 4; ++e) y[e] = __builtin_fmaxf(__builtin_fmaf(acc[i][j][e], fa[i], fb[i] * cs[j][e]), 0.f);
        return y;
      };
      const int hb = WS_H1 + (tm * 112 + gp * 7) * 8192;
#pragma unroll
      for (int a = 0; a < 3; ++a) {
        const int sl = slab3(a);
#pragma unroll
        for (int i = 0; i < 4; ++i) {
          f32x4 x = act(i, 2 * a), y = act(i, 2 * a + 1);
          swap_pair(x, y);
          const u32x4 o = pack_xy(x, y);
          const int ro = (wm * 64 + i * 16) * 64 + cswz;
          st128(o, rws, hb + sl * 8192 + ro);
          if (a < 2 || (unsigned)(sl - par) < 6u) *(LAS u32x4*)(smem + (sl - par) * 8192 + ro) = o;
        }
        asm volatile("" ::: "memory");
      }
#pragma unroll
      for (int i = 1; i < 4; i += 2) {
        f32x4 x = act(i - 1, 6), y = act(i, 6);
        swap_pair(x, y);
        const u32x4 o = pack_xy(x, y);
        const int r3 = (wm * 64 + (i - 1 + (g & 1)) * 16) * 64 + (2 * wn + (g >> 1)) * 256 + r16 * 16;
        st128(o, rws, hb + 3 * 8192 + r3);
        *(LAS u32x4*)(smem + (3 - par) * 8192 + r3) = o;
      }
      if (wid == 0) STAMP(2, 5);
      wait_vm<6>();
      if (wid == 0) STAMP(2, 3);
    }
    relane();
    {
      const int row = tm * 128 + wm * 64 + r16, col = gp * 64 + wn * 32 + cpair;
      u32x4 res[4], t[4];
      unsigned seen = 0;
      f32x4 acc[4][4];
      mma_loop<128, 4, 31, 4, 4, true, true>(smem, acc, wm * 64, lane, [&](int j) { return (j >> 1) * 4 + wn * 2 + (j & 1); }, [&]() {
        wait_vm<0>();
        if (wid == 0) STAMP(2, 4);
#pragma unroll
        for (int i = 0; i < 4; ++i) res[i] = ld128(rws, WS_H + (tm * 32 + gp * 2 + wn) * 8192 + (wm * 64 + i * 16) * 64 + cswz, 0);
      }, [&](int what) {
        if (what == 0) {
          const int pb = WS_P4 + (tm * 16 + gp) * 16384 + wid * 4096 + lane * 16;
#pragma unroll
          for (int i = 0; i < 4; ++i) st128(pack_xy(acc[i][2], acc[i][3]), rws, pb + i * 1024);
        } else if (what == 1) {
          wait_vm<0>();
          if (lane == 0) set_flag(ctl, C_P4 + tm, gp * 4 + wid, tag);
          if (wid == 0) STAMP(3, 3);
        } else if (what == 2) {
          seen = __hip_atomic_load(ctl + (C_P4 + tm) * 64 + (gp ^ 8) * 4 + wid, __ATOMIC_RELAXED, __HIP_MEMORY_SCOPE_AGENT);
        } else {
          asm volatile("" : "+v"(seen));
          if (!wave_all(seen == tag)) poll(ctl, C_P4 + tm, (gp ^ 8) * 4 + wid, 1, tag, lane);
          acquire();
          if (wid == 0) STAMP(3, 4);
          const int qb = WS_P4 + (tm * 16 + (gp ^ 8)) * 16384 + wid * 4096 + lane * 16;
#pragma unroll
          for (int i = 0; i < 4; ++i) t[i] = ld128(rws, qb + i * 1024, 0);
        }
      });
      if (wid == 0) STAMP(3, 2);
#pragma unroll
      for (int i = 0; i < 4; ++i) {
        f32x4 x = acc[i][0], y = acc[i][1];
        add_bf8(x, y, t[i]);
        swap_pair(x, y);
        add_bf8(x, y, res[i]);
        *(u32x4*)(OUT + (size_t)(row + i * 16) * D_ + col) = pack_xy(x, y);
      }
      if (wid == 0) STAMP(3, 5);
    }
  }
#endif
}
#if defined(__HIP_DEVICE_COMPILE__)
#pragma clang attribute pop
#endif

extern "C" void kernel_launch(void* const* d_in, const int* in_sizes, int n_in, void* d_out, int out_size, void* d_ws, size_t ws_size,
                              hipStream_t stream) {
  (void)hipFuncSetAttribute((const void*)block_kernel, hipFuncAttributeMaxDynamicSharedMemorySize, LDS_BYTES);
  block_kernel<<<256, 512, LDS_BYTES, stream>>>((const u16*)d_in[0], (const u16*)d_in[1], (const u16*)d_in[2], (const u16*)d_in[3],
                                                (const u16*)d_in[5], (const u16*)d_in[6], (u16*)d_out, (char*)d_ws);
}
```

```cpp
#include <hip/hip_runtime.h>
#include <hip/hip_bf16.h>
#include <type_traits>

#define LAS __attribute__((address_space(3)))
#define GAS __attribute__((address_space(1)))
#define DI __device__ __forceinline__
typedef __bf16 bf16x8 __attribute__((ext_vector_type(8)));
typedef __bf16 bf16x2 __attribute__((ext_vector_type(2)));
typedef short s16x4 __attribute__((ext_vector_type(4)));
typedef short s16x8 __attribute__((ext_vector_type(8)));
typedef float f32x2 __attribute__((ext_vector_type(2)));
typedef float f32x4 __attribute__((ext_vector_type(4)));
typedef unsigned u32x2 __attribute__((ext_vector_type(2)));
typedef unsigned u32x4 __attribute__((ext_vector_type(4)));
typedef unsigned short u16;

constexpr int S_ = 2048, D_ = 1024, FF_ = 3584;
constexpr float LN_EPS = 1e-5f;

constexpr int C_EPOCH = 0, C_TMO = 1, C_ST = 2, C_CS1 = 18, C_CS3 = 34, C_KV = 50, C_O = 66, C_H = 82, C_H1 = 98, C_P4 = 114, C_H1B = 130, C_LINES = 146;
constexpr unsigned WS_XST = 40960;
constexpr unsigned WS_CS1 = WS_XST + S_ * 8;
constexpr unsigned WS_CS3 = WS_CS1 + 16 * 16 * 192 * 4;
constexpr unsigned WS_HST = WS_CS3 + 16 * 16 * 224 * 4;
constexpr unsigned KVP_BYTES = 8192 + 512;
constexpr unsigned WS_KVP = WS_HST + S_ * 16 * 8;
constexpr unsigned WS_O = WS_KVP + 256 * KVP_BYTES;
constexpr unsigned WS_H = WS_O + S_ * D_ * 2;
constexpr unsigned WS_H1 = WS_H + S_ * D_ * 2;
constexpr unsigned WS_P4 = WS_O, WS_END = WS_H1 + S_ * FF_ * 2;
static_assert(C_LINES * 256 <= (int)WS_XST && WS_O % 256 == 0 && WS_KVP % 256 == 0, "");

constexpr int L_WT = 120 * 1024;
constexpr int L_TAB = 149 * 1024;
constexpr int L_DUMMY = L_TAB;
constexpr int L_RST = L_TAB + 256;
constexpr int L_CS = L_RST + 1024;
constexpr int L_CSS = L_CS + 1024;
constexpr int L_GO = L_CSS + 8192;
constexpr int LDS_BYTES = L_GO + 16;
static_assert(LDS_BYTES <= 160 * 1024, "");

#if defined(__HIP_DEVICE_COMPILE__)
#pragma clang attribute push(__attribute__((target("no-packed-fp32-ops"))), apply_to = function)
typedef __amdgpu_buffer_rsrc_t rsrc_t;
DI float u2f(unsigned u) { return __builtin_bit_cast(float, u); }
DI unsigned f2u(float f) { return __builtin_bit_cast(unsigned, f); }
DI bool wave_all(bool p) { return __builtin_amdgcn_ballot_w64(p) == ~0ull; }
#define CODE_AHEAD 52224
#define PSLEEP "2"
#define WO1 9
#define BDEPTH 3
#define PFD 8
#define B3DELAY 48
#define CPW 0
constexpr int SC1 = 16;
template <int N> DI void wait_vm() { asm volatile("s_waitcnt vmcnt(%0)" ::"n"(N) : "memory"); }
DI void wait_lgkm0() { asm volatile("" ::: "memory"); __builtin_amdgcn_s_waitcnt(0xC07F); asm volatile("" ::: "memory"); }
DI void bar() { asm volatile("" ::: "memory"); __builtin_amdgcn_s_barrier(); asm volatile("" ::: "memory"); }
DI void lbar() { wait_lgkm0(); bar(); }
DI unsigned pk2(float a, float b) { f32x2 v = {a, b}; return __builtin_bit_cast(unsigned, __builtin_convertvector(v, bf16x2)); }
DI float bflo(unsigned u) { return u2f(u << 16); }
DI float bfhi(unsigned u) { return u2f(u & 0xffff0000u); }
DI bf16x8 ld_b128(LAS const char* p) { return *(LAS const bf16x8*)p; }
DI bf16x8 ld_tr2(LAS const char* p0, LAS const char* p1) {
  s16x4 a = __builtin_amdgcn_ds_read_tr16_b64_v4i16((LAS s16x4*)p0);
  s16x4 b = __builtin_amdgcn_ds_read_tr16_b64_v4i16((LAS s16x4*)p1);
  return __builtin_bit_cast(bf16x8, __builtin_shufflevector(a, b, 0, 1, 2, 3, 4, 5, 6, 7));
}
DI float x32_sum(float v) { auto r = __builtin_amdgcn_permlane32_swap(f2u(v), f2u(v), false, false); return u2f(r[0]) + u2f(r[1]); }
DI float x16_sum(float v) { auto r = __builtin_amdgcn_permlane16_swap(f2u(v), f2u(v), false, false); return u2f(r[0]) + u2f(r[1]); }
template <int CTRL> DI float dpp_add(float v) { return v + u2f(__builtin_amdgcn_update_dpp(0, (int)f2u(v), CTRL, 0xf, 0xf, true)); }
DI float half_sum(float v) {
  v = dpp_add<0xB1>(v);
  v = dpp_add<0x4E>(v);
  v = dpp_add<0x141>(v);
  v = dpp_add<0x140>(v);
  return x16_sum(v);
}
#define MFMA16(a, b, c) __builtin_amdgcn_mfma_f32_16x16x32_bf16((a), (b), (c), 0, 0, 0)
DI rsrc_t mkrsrc(const void* p, unsigned bytes) { return __builtin_amdgcn_make_buffer_rsrc((void*)p, 0, bytes, 0x00020000); }
DI void st128(u32x4 v, rsrc_t r, int off) { __builtin_amdgcn_raw_buffer_store_b128(v, r, off, 0, SC1); }
DI u32x4 ld128(rsrc_t r, int off, int soff) { return __builtin_amdgcn_raw_buffer_load_b128(r, off, soff, SC1); }

DI void set_flag(GAS unsigned* ctl, int line, int word, unsigned tag) { __hip_atomic_store(ctl + line * 64 + word, tag, __ATOMIC_RELAXED, __HIP_MEMORY_SCOPE_AGENT); }
DI void acquire() {
  asm volatile("" ::: "memory");
}
DI void poll(GAS unsigned* ctl, int line, int lo, int n, unsigned tag, int lane) {
  GAS unsigned* p = ctl + line * 64 + lo + (lane < n ? lane : 0);
  unsigned spins = 0;
  while (!wave_all(__hip_atomic_load(p, __ATOMIC_RELAXED, __HIP_MEMORY_SCOPE_AGENT) == tag)) {
    __builtin_amdgcn_s_sleep(1);
    if (++spins > (1u << 17)) { if (lane == 0) __hip_atomic_store(ctl + C_TMO * 64, (unsigned)line, __ATOMIC_RELAXED, __HIP_MEMORY_SCOPE_AGENT); break; }
  }
  acquire();
}
struct FastPoll {
  unsigned a, b, c, d;
  DI void run(GAS unsigned* ctl, int line, int lo, int n, unsigned tag, int lane) {
    GAS unsigned* p = ctl + line * 64 + lo + (lane < n ? lane : 0);
    unsigned cnt = 1u << 17;
#define FP_LD(r) "global_load_dword %" #r ", %5, off sc1\n\ts_sleep " PSLEEP "\n\t"
#define FP_CK(r) "s_waitcnt vmcnt(3)\n\tv_cmp_eq_u32 vcc, %6, %" #r "\n\ts_cmp_eq_u64 vcc, -1\n\ts_cbranch_scc1 2f\n\t" FP_LD(r)
    asm volatile(FP_LD(0) FP_LD(1) FP_LD(2) FP_LD(3) "1:\n\t" FP_CK(0) FP_CK(1) FP_CK(2) FP_CK(3)
                 "s_sub_u32 %4, %4, 1\n\ts_cbranch_scc0 1b\n\t2:"
                 : "=&v"(a), "=&v"(b), "=&v"(c), "=&v"(d), "+s"(cnt) : "v"(p), "s"(tag) : "vcc", "scc", "memory");
#undef FP_LD
#undef FP_CK
  }
  DI void keep() const { asm volatile("" :: "v"(a), "v"(b), "v"(c), "v"(d)); }
};
DI void poll4(FastPoll& fp, LAS char* smem, unsigned go, int lw, GAS unsigned* ctl, int line, int lo, int n, unsigned tag, int lane) {
  LAS volatile unsigned* w = (LAS volatile unsigned*)(smem + L_GO);
  if (lw == 0) {
    fp.run(ctl, line, lo, n, tag, lane);
    *w = go;
  } else {
    fp.a = fp.b = fp.c = fp.d = 0u;
    unsigned spins = 0;
    while (__builtin_amdgcn_readfirstlane(*w) != go && ++spins < (1u << 24)) __builtin_amdgcn_s_sleep(1);
  }
  asm volatile("" ::: "memory");
}

template <int BN> DI constexpr int bswz(int k) {
  return BN == 224 ? (((k >> 3) & 1) << 1)
       : BN == 128 ? (((k & 3) | (((k >> 3) & 1) << 2)) << 1)
                   : ((((k >> 1) & 1) | (((k >> 3) & 1) << 1)) << 1);
}

template <int BN, int NS> DI int bslot(int s) {
  if (BN == 224) return ((0x4c3078 >> (8 * s)) & 255) << 10;
  if (BN == 128) return ((0x78504068 >> (8 * s)) & 255) << 10;
  return NS * 16384 + s * BN * 128;
}
DI constexpr int wo_slot(int t) { return t < 5 ? 81920 + t * 8192 : t < 15 ? (t - 5) * 8192 : L_CSS; }

template <int BN, int NS, int NT, bool AROW, int LAG = 0>
struct Loader {
  static constexpr int PA = 4, PB = BN / 32, PW = PA + PB, BST = BN * 128, CPR = BN / 8;
  static_assert(NS * (16384 + BST) <= L_TAB, "");
  LAS char* dst0; rsrc_t ra, rb; int va[PA], vb[PB], vh[2]; int ldb64, own0, tn;
  static constexpr int NOWN = BN == 128 ? 3 : BN == 224 ? 1 : 0;
  int kh;
  DI int ktile(int t) const {
    if (AROW) return (t + own0) & (NT - 1);
    if (BN != 128) return t < NOWN ? own0 + t : (t - NOWN < own0 ? t - NOWN : t);
    const int pi = t < 17 ? (t - 3) >> 1 : t - 17, p = t < 3 ? own0 : pi + (pi >= own0 ? 1 : 0);
    const int k = t >= 24 ? 7 * ((t >= 28 ? t - 3 : t) - 24) + 3 : ((7 * p + 1) >> 1) + (t < 3 ? t : t < 17 ? 2 * ((t - 3) & 1) : 1);
    return k + ((t >= 28) != (kh != 0) ? 28 : 0);
  }
  DI void issueA(int t) {
    const int kk = ktile(t), slot = t % NS;
#pragma unroll
    for (int i = 0; i < PA; ++i)
      __builtin_amdgcn_raw_ptr_buffer_load_lds(ra, dst0 + slot * 16384 + i * 4096, 16, va[i], kk * (AROW ? 128 : 16384), 0, AROW ? 0 : SC1);
  }
  DI void issueB(int t) {
    const int kk = ktile(t), slot = t % NS;
#pragma unroll
    for (int i = 0; i < PB; ++i) __builtin_amdgcn_raw_ptr_buffer_load_lds(rb, dst0 + bslot<BN, NS>(slot) + i * 4096, 16, vb[i], kk * ldb64, 0, 0);
  }
  DI void issueH(int t) {
    issueA(t);
    const int kk = ktile(t), slot = t % NS;
#pragma unroll
    for (int i = 0; i < 2; ++i) __builtin_amdgcn_raw_ptr_buffer_load_lds(rb, dst0 + bslot<BN, NS>(slot) + i * 4096, 16, vh[i], kk * ldb64, 0, 0);
  }
  DI void init(LAS char* smem, rsrc_t ra_, rsrc_t rb_, int a_off, int lda_b, int b_off, int ldb_b, int segb, int lw, int lane, int own) {
    dst0 = smem + lw * 1024; ra = ra_; rb = rb_; ldb64 = 64 * ldb_b; own0 = own; tn = NS - 1;
#pragma unroll
    for (int i = 0; i < PA; ++i) {
      const int ins = lw + i * 4, r = ins * 8 + (lane >> 3), c = (lane & 7) ^ ((r >> 1) & 7);
      va[i] = AROW ? a_off + r * lda_b + c * 16 : a_off + ins * 1024 + lane * 16;
    }
#pragma unroll
    for (int i = 0; i < PB; ++i) {
      const int ch = (lw + i * 4) * 64 + lane, k = ch / CPR, c = (ch % CPR) ^ bswz<BN>(k);
      vb[i] = b_off + k * ldb_b + (c >> 3) * segb + (c & 7) * 16;
    }
  }
  DI void init_half(int b_off, int ldb_b, int lw, int lane) {
#pragma unroll
    for (int i = 0; i < 2; ++i) { const int k = (lw + i * 4) * 8 + (lane >> 3); vh[i] = b_off + k * ldb_b + (((lane & 7) ^ bswz<64>(k)) * 16); }
  }
  template <int ALLOW = (NS - 2 - LAG) * PW> DI void step() { wait_vm<ALLOW>(); lbar(); issueA(tn); issueB(tn); ++tn; }
  DI void steps(int n) {
#pragma unroll 1
    for (int t = 0; t < n; ++t) step();
  }
  DI void finish() {
    if constexpr (NS - LAG >= 5) { wait_vm<3 * PW>(); lbar(); }
    if constexpr (NS - LAG >= 4) { wait_vm<2 * PW>(); lbar(); }
    if constexpr (NS - LAG >= 3) { wait_vm<1 * PW>(); lbar(); }
    wait_vm<0>(); lbar();
  }
};
template <int BN, int I0, int I1> DI void load_wtile(LAS char* dst, rsrc_t r, int off, int ldb_b, int segb, int lw, int lane) {
#pragma unroll
  for (int i = I0; i < I1; ++i) {
    const int ch = (lw + i * 4) * 64 + lane, k = ch / (BN / 8), c = (ch % (BN / 8)) ^ bswz<BN>(k);
    __builtin_amdgcn_raw_ptr_buffer_load_lds(r, dst + (lw + i * 4) * 1024, 16, off + k * ldb_b + (c >> 3) * segb + (c & 7) * 16, 0, 0, 0);
  }
}
template <int T0, int T1> DI void load_wo(LAS char* smem, rsrc_t ro, int gp, int lw, int lane) {
  int vo[2];
#pragma unroll
  for (int i = 0; i < 2; ++i) { const int k = (lw + i * 4) * 8 + (lane >> 3); vo[i] = gp * 128 + k * (D_ * 2) + (((lane & 7) ^ bswz<64>(k)) * 16); }
#pragma unroll
  for (int t = T0; t < T1; ++t)
#pragma unroll
    for (int i = 0; i < 2; ++i) __builtin_amdgcn_raw_ptr_buffer_load_lds(ro, smem + wo_slot(t) + (lw + i * 4) * 1024, 16, vo[i], t * 64 * (D_ * 2), 0, 0);
}
DI int opaque(int v) { asm volatile("" : "+v"(v)); return v; }

DI void touch_lines(LAS char* smem, rsrc_t r, int base, int line, int nlines) {
  __builtin_amdgcn_raw_ptr_buffer_load_lds(r, smem + L_DUMMY, 4, line < nlines ? base + line * 128 : (int)0x80000000, 0, 0, 0);
}

template <int BN> DI void colsum_tile(LAS char* img, rsrc_t rws, unsigned off, int lw, int lane) {
  constexpr int RS = BN * 2;
  const int r16 = lane & 15, g = lane >> 4, q = (lane & 15) >> 2, p = lane & 3;
  const s16x8 one_ = {0x3f80, 0x3f80, 0x3f80, 0x3f80, 0x3f80, 0x3f80, 0x3f80, 0x3f80};
  LAS const char* rd[4];
#pragma unroll
  for (int i = 0; i < 4; ++i) {
    const int nt = lw + 4 * i < BN / 16 ? lw + 4 * i : lw;
    rd[i] = img + (8 * g + q) * RS + (((2 * nt + (p >> 1)) ^ bswz<BN>(8 * g + q)) * 16) + 8 * (p & 1);
  }
  s16x4 v[4][4];
#define TR4(a, b, c, d, r) "ds_read_b64_tr_b16 %" #a ", %" #r "\n\tds_read_b64_tr_b16 %" #b ", %" #r " offset:%c20\n\t" \
                           "ds_read_b64_tr_b16 %" #c ", %" #r " offset:%c21\n\tds_read_b64_tr_b16 %" #d ", %" #r " offset:%c22\n\t"
  asm volatile(TR4(0, 1, 2, 3, 16) TR4(4, 5, 6, 7, 17) TR4(8, 9, 10, 11, 18) TR4(12, 13, 14, 15, 19) "s_waitcnt lgkmcnt(0)"
               : "=&v"(v[0][0]), "=&v"(v[0][1]), "=&v"(v[0][2]), "=&v"(v[0][3]), "=&v"(v[1][0]), "=&v"(v[1][1]), "=&v"(v[1][2]), "=&v"(v[1][3]),
                 "=&v"(v[2][0]), "=&v"(v[2][1]), "=&v"(v[2][2]), "=&v"(v[2][3]), "=&v"(v[3][0]), "=&v"(v[3][1]), "=&v"(v[3][2]), "=&v"(v[3][3])
               : "v"(rd[0]), "v"(rd[1]), "v"(rd[2]), "v"(rd[3]), "i"(4 * RS), "i"(32 * RS), "i"(36 * RS) : "memory");
#undef TR4
  f32x4 c[4];
#pragma unroll
  for (int i = 0; i < 4; ++i) {
    c[i] = (f32x4){0.f, 0.f, 0.f, 0.f};
    c[i] = MFMA16(__builtin_bit_cast(bf16x8, __builtin_shufflevector(v[i][0], v[i][1], 0, 1, 2, 3, 4, 5, 6, 7)), __builtin_bit_cast(bf16x8, one_), c[i]);
    c[i] = MFMA16(__builtin_bit_cast(bf16x8, __builtin_shufflevector(v[i][2], v[i][3], 0, 1, 2, 3, 4, 5, 6, 7)), __builtin_bit_cast(bf16x8, one_), c[i]);
  }
  const f32x4 out = r16 == 0 ? c[0] : r16 == 1 ? c[1] : r16 == 2 ? c[2] : c[3];
  st128(__builtin_bit_cast(u32x4, out), rws, lw + 4 * r16 < BN / 16 ? off + (lw + 4 * r16) * 64 + g * 16 : 0x80000000u);
}
DI f32x4 sum4(u32x4 (&t)[4]) {
  asm volatile("" : "+v"(t[0]), "+v"(t[1]), "+v"(t[2]), "+v"(t[3]));
  return (__builtin_bit_cast(f32x4, t[0]) + __builtin_bit_cast(f32x4, t[1])) + (__builtin_bit_cast(f32x4, t[2]) + __builtin_bit_cast(f32x4, t[3]));
}
DI f32x4 lds_sum4(LAS const char* p) {
  return (*(LAS const f32x4*)p + *(LAS const f32x4*)(p + 1024)) + (*(LAS const f32x4*)(p + 2048) + *(LAS const f32x4*)(p + 3072));
}
DI unsigned peek(GAS unsigned* ctl, int line, int lane, int n = 64) { return __hip_atomic_load(ctl + line * 64 + (lane & (n - 1)), __ATOMIC_RELAXED, __HIP_MEMORY_SCOPE_AGENT); }
DI void confirm(unsigned seen, GAS unsigned* ctl, int line, unsigned tag, int lane, int n = 64) {
  asm volatile("" : "+v"(seen));
  if (!wave_all(seen == tag)) poll(ctl, line, 0, n, tag, lane);
  else acquire();
}

DI void mfma16(f32x4& c, const bf16x8& a, const bf16x8& b) { asm volatile("v_mfma_f32_16x16x32_bf16 %0, %1, %2, %0" : "+v"(c) : "v"(a), "v"(b)); }
DI void mfma_drain() { asm volatile("s_nop 7\n\ts_nop 7" ::: "memory"); }
template <int TM, int TN> struct Frag { s16x4 b[TN][2]; bf16x8 a[TM]; };
template <int BN, int NS, int NT, int TM, int TN, bool DEEP, bool OWN, class NMap, class Mid, class Send>
DI void mma_loop(LAS char* smem, f32x4 (&acc)[TM][TN], int row0, int lane, NMap&& nt, Mid&& mid, Send&& send) {
  constexpr int RS = BN * 2, BST = BN * 128, NL = TM + 2 * TN, NM = TM * TN;
  typedef Frag<TM, TN> F;
  const int r16 = lane & 15, g = lane >> 4, q = (lane & 15) >> 2, p = lane & 3;
  constexpr bool AROW = BN == 192;
  const int aoff = AROW ? (row0 + r16) * 128 + ((g ^ (r16 >> 1)) * 16) : row0 * 64 + lane * 16;
  const int aoff1 = AROW ? aoff ^ 64 : aoff + 8192;
  int boff[TN];
#pragma unroll
  for (int j = 0; j < TN; ++j) boff[j] = (8 * g + q) * RS + (((2 * nt(j) + (p >> 1)) ^ bswz<BN>(8 * g + q)) * 16) + 8 * (p & 1);
#pragma unroll
  for (int i = 0; i < TM; ++i)
#pragma unroll
    for (int j = 0; j < TN; ++j) { acc[i][j] = (f32x4){0.f, 0.f, 0.f, 0.f}; asm volatile("" : "+v"(acc[i][j])); }
  int boffh[2];
  auto ld1 = [&](F& f, int slot, int s, int k, int tn = TN) {
    if (k == 0 || k > 2 * tn) { const int i = k == 0 ? 0 : k - 2 * tn; f.a[i] = ld_b128(smem + slot * 16384 + (s ? aoff1 : aoff) + i * (AROW ? 2048 : 1024)); }
    else {
      const int j = (k - 1) >> 1, u = (k - 1) & 1;
      f.b[j][u] = __builtin_amdgcn_ds_read_tr16_b64_v4i16((LAS s16x4*)(smem + bslot<BN, NS>(slot) + (tn == TN ? boff[j] + (32 * s + 4 * u) * RS : boffh[j & 1] + (32 * s + 4 * u) * 128)));
    }
  };
  auto mm1 = [&](const F& f, int m, int tn = TN) {
    const int i = m / tn, j = m % tn;
    const bf16x8 b = __builtin_bit_cast(bf16x8, __builtin_shufflevector(f.b[j][0], f.b[j][1], 0, 1, 2, 3, 4, 5, 6, 7));
    if (AROW && j >= 4) mfma16(acc[i][j], f.a[i], b); else mfma16(acc[i][j], b, f.a[i]);
  };
  auto ldall = [&](F& f, int slot, int s) {
#pragma unroll
    for (int k = 0; k < NL; ++k) ld1(f, slot, s, k);
  };
  auto mmall = [&](const F& f) {
#pragma unroll
    for (int m = 0; m < NM; ++m) mm1(f, m);
  };
  auto sub = [&](const F& x, F& u, int slot, int s) {
    constexpr int SP = DEEP ? NM : (NL < NM ? NL : NM - 2);
#pragma unroll
    for (int m = 0; m < NM; ++m) {
      mm1(x, m);
#pragma unroll
      for (int k = (m * NL + SP - 1) / SP; k < NL && k < ((m + 1) * NL + SP - 1) / SP; ++k) ld1(u, slot, s, k);
      __builtin_amdgcn_sched_barrier(0);
    }
  };
  auto subh = [&](const F& x, F& u, int slot, int s, auto tx, auto tu) {
    constexpr int TX = decltype(tx)::value, TU = decltype(tu)::value, M = TM * TX, LN = TM + 2 * TU;
#pragma unroll
    for (int m = 0; m < M; ++m) {
      mm1(x, m, TX);
#pragma unroll
      for (int k = (m * LN + M - 1) / M; k < LN && k < ((m + 1) * LN + M - 1) / M; ++k) ld1(u, slot, s, k, TU);
      __builtin_amdgcn_sched_barrier(0);
    }
  };
  auto nxt = [](int s) { return s == NS - 1 ? 0 : s + 1; };
  lbar();
  int slot = 0;
  if constexpr (!DEEP) {
    constexpr int NB = AROW ? 2 : TN, NA = AROW ? 2 : 1;
    LAS char* ca[NA];
    LAS char* cb[NB];
    ca[0] = smem + aoff;
    if constexpr (AROW) ca[NA - 1] = smem + aoff1;
#pragma unroll
    for (int j = 0; j < NB; ++j) cb[j] = smem + bslot<BN, NS>(0) + boff[j];
    auto ldc = [&](F& f, int s, int k) {
      if (k == 0 || k > 2 * TN) { const int i = k == 0 ? 0 : k - 2 * TN; f.a[i] = ld_b128(AROW ? ca[s] + i * 2048 : ca[0] + s * 8192 + i * 1024); }
      else {
        const int j = (k - 1) >> 1, u = (k - 1) & 1;
        f.b[j][u] = __builtin_amdgcn_ds_read_tr16_b64_v4i16((LAS s16x4*)((AROW ? cb[j & 1] + (j >> 1) * 128 : cb[j]) + (32 * s + 4 * u) * RS));
      }
    };
    auto subc = [&](const F& x, F& u, int s, bool adv) {
      const int ns = nxt(slot), da = (ns - slot) * 16384, db = bslot<BN, NS>(ns) - bslot<BN, NS>(slot);
#pragma unroll
      for (int m = 0; m < NM; ++m) {
        mm1(x, m);
        if (m < NL) ldc(u, s, m);
        else if (adv && m - NL < NA) asm volatile("v_add_u32 %0, %1, %0" : "+v"(ca[m - NL < NA ? m - NL : 0]) : "s"(da));
        else if (adv && m - NL - NA < NB) asm volatile("v_add_u32 %0, %1, %0" : "+v"(cb[m - NL - NA < NB ? m - NL - NA : 0]) : "s"(db));
        __builtin_amdgcn_sched_barrier(0);
      }
      if (adv) slot = ns;
    };
    static_assert(NL + NA + NB <= NM, "");
    F f0, f1;
#pragma unroll
    for (int k = 0; k < NL; ++k) ldc(f0, 0, k);
    if constexpr (OWN) {
      subc(f0, f1, 1, true);
      mmall(f1);
      __builtin_amdgcn_sched_barrier(0);
      bar();
#pragma unroll
      for (int k = 0; k < NL; ++k) ldc(f0, 0, k);
    }
#pragma unroll 1
    for (int t = OWN ? 1 : 0; t < NT - 1; ++t) {
      subc(f0, f1, 1, true);
      lbar();
      subc(f1, f0, 0, false);
    }
    subc(f0, f1, 1, false);
    lbar();
    mmall(f1);
  } else {
    F f0, f1, g0, g1;
    auto tile = [&](const F& x0, const F& x1, F& u0, F& u1, int t) {
      bar();
      slot = nxt(slot);
      sub(x0, u0, slot, 0);
      sub(x1, u1, slot, 1);
    };
    ldall(f0, 0, 0); ldall(f1, 0, 1);
    int t0 = 0;
    if constexpr (OWN) {
      tile(f0, f1, g0, g1, 0);
      tile(g0, g1, f0, f1, 1);
      mmall(f0); mmall(f1);
      __builtin_amdgcn_sched_barrier(0);
      lbar();
      slot = 3;
      ldall(g0, 3, 0); ldall(g1, 3, 1);
      tile(g0, g1, f0, f1, 3);
      t0 = 4;
    }
    if constexpr (!OWN) {
#pragma unroll 1
      for (int t = t0; t < NT - 2; t += 2) {
        tile(f0, f1, g0, g1, t);
        tile(g0, g1, f0, f1, t + 1);
      }
      tile(f0, f1, g0, g1, NT - 2);
      lbar();
      mmall(g0); mmall(g1);
    } else {
#pragma unroll 1
      for (int t = t0; t < 24; t += 2) {
        tile(f0, f1, g0, g1, t);
        if (t == 4) mid();
        tile(g0, g1, f0, f1, t + 1);
      }
      typedef std::integral_constant<int, TN> Full;
      typedef std::integral_constant<int, TN / 2> Half;
      auto tileh = [&](const F& x0, const F& x1, F& u0, F& u1, auto tx) {
        bar();
        slot = nxt(slot);
        subh(x0, u0, slot, 0, tx, Half{});
        subh(x1, u1, slot, 1, tx, Half{});
      };
#pragma unroll
      for (int j = 0; j < 2; ++j) {
        const int l2 = opaque(lane), k = 8 * (l2 >> 4) + ((l2 & 15) >> 2);
        boffh[j] = k * 128 + (((2 * nt(j) + ((l2 & 3) >> 1)) ^ bswz<64>(k)) * 16) + 8 * (l2 & 1);
      }
      tileh(f0, f1, g0, g1, Full{});
      mfma_drain();
#pragma unroll
      for (int i = 0; i < TM; ++i)
#pragma unroll
        for (int j = TN / 2; j < TN; ++j) asm volatile("" : "+v"(acc[i][j]));
      send(0);
      __builtin_amdgcn_sched_barrier(0);
      tileh(g0, g1, f0, f1, Half{});
      tileh(f0, f1, g0, g1, Half{});
      send(1);
      tileh(g0, g1, f0, f1, Half{});
      tileh(f0, f1, g0, g1, Half{});
      send(2);
      tileh(g0, g1, f0, f1, Half{});
      send(3);
      lbar();
#pragma unroll
      for (int m = 0; m < TM * TN / 2; ++m) mm1(f0, m, TN / 2);
#pragma unroll
      for (int m = 0; m < TM * TN / 2; ++m) mm1(f1, m, TN / 2);
    }
  }
  mfma_drain();
#pragma unroll
  for (int i = 0; i < TM; ++i)
#pragma unroll
    for (int j = 0; j < TN; ++j) asm volatile("" : "+v"(acc[i][j]));
}
DI void swap_pair(f32x4& x, f32x4& y) {
#pragma unroll
  for (int e = 0; e < 4; ++e) {
    auto r = __builtin_amdgcn_permlane16_swap(f2u(x[e]), f2u(y[e]), false, false);
    x[e] = u2f(r[0]); y[e] = u2f(r[1]);
  }
}
DI void add_bf8(f32x4& x, f32x4& y, const u32x4& rv) {
  x[0] += bflo(rv[0]); x[1] += bfhi(rv[0]); x[2] += bflo(rv[1]); x[3] += bfhi(rv[1]);
  y[0] += bflo(rv[2]); y[1] += bfhi(rv[2]); y[2] += bflo(rv[3]); y[3] += bfhi(rv[3]);
}
DI u32x4 pack_xy(const f32x4& x, const f32x4& y) { return (u32x4){pk2(x[0], x[1]), pk2(x[2], x[3]), pk2(y[0], y[1]), pk2(y[2], y[3])}; }

template <class Op> DI void red2(float& a, float& b, Op&& op) {
  auto r = __builtin_amdgcn_permlane16_swap(f2u(a), f2u(b), false, false);
  const float c = op(u2f(r[0]), u2f(r[1]));
  auto t = __builtin_amdgcn_permlane32_swap(f2u(c), f2u(c), false, false);
  const float d = op(u2f(t[0]), u2f(t[1]));
  auto u = __builtin_amdgcn_permlane16_swap(f2u(d), f2u(d), false, false);
  a = u2f(u[0]); b = u2f(u[1]);
}
DI void dual_softmax(const f32x4 (&p)[2][2], const float (&sc)[2], f32x4 (&e)[2][2][2]) {
  float mx[2], mn[2], s1[2], s2[2];
#pragma unroll
  for (int t = 0; t < 2; ++t) {
    mx[t] = p[t][0][0]; mn[t] = p[t][0][0];
#pragma unroll
    for (int r = 1; r < 8; ++r) { mx[t] = __builtin_fmaxf(mx[t], p[t][r >> 2][r & 3]); mn[t] = __builtin_fminf(mn[t], p[t][r >> 2][r & 3]); }
  }
  red2(mx[0], mx[1], [](float x, float y) { return __builtin_fmaxf(x, y); });
  red2(mn[0], mn[1], [](float x, float y) { return __builtin_fminf(x, y); });
#pragma unroll
  for (int t = 0; t < 2; ++t) {
    const float a = -mx[t] * sc[t], b = mn[t] * sc[t];
    s1[t] = 0.f; s2[t] = 0.f;
#pragma unroll
    for (int r = 0; r < 8; ++r) {
      const float x = __builtin_amdgcn_exp2f(__builtin_fmaf(p[t][r >> 2][r & 3], sc[t], a));
      const float y = __builtin_amdgcn_exp2f(__builtin_fmaf(p[t][r >> 2][r & 3], -sc[t], b));
      e[0][t][r >> 2][r & 3] = x; e[1][t][r >> 2][r & 3] = y;
      s1[t] += x; s2[t] += y;
    }
  }
  red2(s1[0], s1[1], [](float x, float y) { return x + y; });
  red2(s2[0], s2[1], [](float x, float y) { return x + y; });
#pragma unroll
  for (int t = 0; t < 2; ++t) {
    const float r1 = __builtin_amdgcn_rcpf(s1[t]), r2 = __builtin_amdgcn_rcpf(s2[t]);
#pragma unroll
    for (int r = 0; r < 8; ++r) { e[0][t][r >> 2][r & 3] *= r1; e[1][t][r >> 2][r & 3] *= r2; }
  }
}
DI bf16x8 pack_frag(const f32x4& x, const f32x4& y) { return __builtin_bit_cast(bf16x8, pack_xy(x, y)); }
DI void st4(LAS char* p, const f32x4& a, const f32x4& b, const f32x4& c, const f32x4& d) {
  *(LAS f32x4*)p = a; *(LAS f32x4*)(p + 1024) = b; *(LAS f32x4*)(p + 2048) = c; *(LAS f32x4*)(p + 3072) = d;
}
DI void ld4(LAS const char* p, f32x4 (&v)[2][2]) {
  v[0][0] = *(LAS const f32x4*)p; v[0][1] = *(LAS const f32x4*)(p + 1024); v[1][0] = *(LAS const f32x4*)(p + 2048); v[1][1] = *(LAS const f32x4*)(p + 3072);
}
#endif

__global__ __launch_bounds__(512, 2) void block_kernel(const u16* __restrict__ X, const u16* __restrict__ WQKV, const u16* __restrict__ WPHI,
                                                        const u16* __restrict__ WO, const u16* __restrict__ W1, const u16* __restrict__ W2,
                                                        u16* __restrict__ OUT, char* WS) {
#if defined(__HIP_DEVICE_COMPILE__)
  extern __shared__ __attribute__((aligned(16))) char smem_[];
  LAS char* smem = (LAS char*)smem_;
  const int tid = threadIdx.x, wid = __builtin_amdgcn_readfirstlane(tid >> 6);
  int lane = tid & 63;
  const int bid = blockIdx.x, xcd = bid & 7, loc = bid >> 3;
  const int tm = (xcd & 1) * 8 + (loc & 7), gp = (xcd >> 1) * 4 + (loc >> 3);
  const bool loader = wid >= 4;
  const int lw = wid & 3;
  GAS unsigned* ctl = (GAS unsigned*)WS;
  const rsrc_t rws = mkrsrc(WS, WS_END);
  if (wid == CPW) {
    unsigned long long pc;
    asm volatile("s_getpc_b64 %0" : "=s"(pc));
    touch_lines(smem, mkrsrc((const void*)(pc & ~127ull), CODE_AHEAD), loc * 2048, lane < 16 ? lane : 4096, 16);
  }
  unsigned epoch, tag;
  auto load_tag = [&]() { epoch = __hip_atomic_load(ctl + C_EPOCH * 64, __ATOMIC_RELAXED, __HIP_MEMORY_SCOPE_AGENT); };
  auto read_tag = [&]() { asm volatile("" : "+v"(epoch)); tag = __builtin_amdgcn_readfirstlane(epoch) + 1u; };

  int r16, g, cpair, cswz;
  auto relane = [&]() {
    lane = opaque(lane);
    r16 = lane & 15; g = lane >> 4;
    cpair = (g & 1) * 16 + (g >> 1) * 8;
    cswz = (cpair >> 3) * 256 + r16 * 16;
  };
  {
    constexpr int NT = 16, NS = 3;
    constexpr int L_PHI = 0;
    constexpr int L_XV = 2 * 17408;
    constexpr int L_VF = L_XV + 16384;
    constexpr int L_XQ = L_VF + 8192;
    constexpr int L_KVT = L_XQ;
    constexpr int L_WF = L_XQ + 16384;
    static_assert(L_WF + 4096 <= 5 * 16384, "phase 2's B ring is filled during this epilogue");
    relane();
    const int wm = (wid >> 1) & 1, hh = wid & 1, mi = wid >> 2;
    const int head = gp * 2 + hh;
    const unsigned kvp_grp = WS_KVP + gp * 16 * KVP_BYTES;
    LAS char* const xk = smem + L_PHI + hh * 17408 + (wm * 64 + 32) * 136 + lane * 16;
    bf16x8 wf[2];
    f32x4 fq[2][2], fk[2][2], fv[2][2];
    if (loader) {
      const rsrc_t rb = mkrsrc(WQKV, D_ * 3 * D_ * 2), ro = mkrsrc(WO, D_ * D_ * 2);
      Loader<192, NS, NT, true> L;
      const rsrc_t rx = mkrsrc(X, S_ * D_ * 2);
      L.init(smem, rx, rb, tm * 128 * D_ * 2, D_ * 2, gp * 128, 3 * D_ * 2, 2048, lw, lane, (tm & 3) * 4);
      L.issueA(0); L.issueB(0); L.issueA(1); L.issueB(1);
      if (lw == 0) *(LAS volatile unsigned*)(smem + L_GO) = 0u;
      load_tag();
      __builtin_amdgcn_s_setprio(3);
      auto cs1 = [&](int t) { if ((tm >> 2) == t) colsum_tile<192>(smem + NS * 16384 + (t % NS) * 24576, rws, WS_CS1 + (gp * 16 + tm) * 768, lw, lane); };
      L.step();
      cs1(0);
      const int xrow = tm * 128 + gp * 8 + lw * 2 + (lane >> 5);
      u32x4 xv[4];
#pragma unroll
      for (int j = 0; j < 4; ++j) xv[j] = __builtin_amdgcn_raw_buffer_load_b128(rx, xrow * (D_ * 2) + (lane & 31) * 16, j * 512, 0);
      L.step<14>();
      cs1(1);
      L.step<14>();
      cs1(2);
      L.step();
      cs1(3);
      L.step();
      read_tag();
      asm volatile("" : "+v"(xv[0]), "+v"(xv[1]), "+v"(xv[2]), "+v"(xv[3]));
      float sum = 0.f, qq = 0.f;
#pragma unroll
      for (int i = 0; i < 16; ++i) sum += bflo(xv[i >> 2][i & 3]) + bfhi(xv[i >> 2][i & 3]);
      float mean = half_sum(sum) * (1.0f / D_);
      asm volatile("" : "+v"(mean));
      L.step();
      asm volatile("" : "+v"(xv[0]), "+v"(xv[1]), "+v"(xv[2]), "+v"(xv[3]));
#pragma unroll
      for (int i = 0; i < 16; ++i) {
        const float d0 = bflo(xv[i >> 2][i & 3]) - mean, d1 = bfhi(xv[i >> 2][i & 3]) - mean;
        qq = __builtin_fmaf(d0, d0, __builtin_fmaf(d1, d1, qq));
      }
      const float rstd = __builtin_amdgcn_rsqf(half_sum(qq) * (1.0f / D_) + LN_EPS);
      __builtin_amdgcn_raw_buffer_store_b64(__builtin_bit_cast(u32x2, (f32x2){mean, rstd}), rws, (lane & 31) == 0 ? WS_XST + xrow * 8 : 0x80000000u, 0, SC1);
      L.step();
      L.step<0>();
      if (lane == 0) { set_flag(ctl, C_CS1 + gp, tm * 4 + lw, tag); set_flag(ctl, C_ST + tm, gp * 4 + lw, tag); }
      touch_lines(smem, ro, bid * 8192, lane < 16 ? lw * 16 + lane : 4096, 64);
      L.step();
      L.step<13>();
      L.step();
      const unsigned seen = peek(ctl, C_CS1 + gp, lane), seen2 = peek(ctl, C_ST + tm, lane);
      L.steps(2);
      confirm(seen, ctl, C_CS1 + gp, tag, lane);
      confirm(seen2, ctl, C_ST + tm, tag, lane);
      u32x4 gt[4];
#pragma unroll
      for (int j = 0; j < 4; ++j) gt[j] = ld128(rws, WS_CS1 + (gp * 16 + lw * 4) * 768 + lane * 16, j * 768);
      u32x4 xs = ld128(rws, WS_XST + tm * 1024 + lane * 16, 0);
      L.step();
      wait_vm<10>(); lbar();
      *(LAS f32x4*)(smem + L_CSS + lw * 1024 + lane * 16) = sum4(gt);
      asm volatile("" : "+v"(xs));
      if (lw == 1) *(LAS u32x4*)(smem + L_RST + lane * 16) = xs;
      wait_vm<0>(); lbar();
      if (lw == 0) *(LAS f32x4*)(smem + L_CS + lane * 16) = lds_sum4(smem + L_CSS + lane * 16);
      const rsrc_t r1 = mkrsrc(W1, D_ * FF_ * 2);
      load_wtile<224, 0, 4>(smem + L_WT, r1, tm * 64 * (FF_ * 2) + gp * 448, FF_ * 2, 128, lw, lane);
      lbar();
      bar();
      __builtin_amdgcn_s_setprio(1);
      ld4(xk, fk);
      wf[0] = ld_b128(smem + L_WF + hh * 2048 + lane * 16);
      wf[1] = ld_b128(smem + L_WF + hh * 2048 + 1024 + lane * 16);
      ld4(smem + L_XV + lw * 4096 + lane * 16, fv);
    } else {
      {
#pragma unroll
        for (int t = 0; t < 2; ++t) {
          s16x8 w;
#pragma unroll
          for (int j = 0; j < 8; ++j) w[j] = (short)WPHI[head * 1024 + (16 * (j >> 2) + 4 * g + (j & 3)) * 32 + 16 * t + r16];
          wf[t] = __builtin_bit_cast(bf16x8, w);
        }
        load_tag();
      }
      f32x4 acc[4][6];
      mma_loop<192, NS, NT, 4, 6, false, false>(smem, acc, wm * 64, lane, [&](int j) { return (j >> 1) * 4 + hh * 2 + (j & 1); }, []() {}, [](int) {});
      read_tag();
      if (wm == 0) {
        *(LAS bf16x8*)(smem + L_WF + hh * 2048 + lane * 16) = wf[0];
        *(LAS bf16x8*)(smem + L_WF + hh * 2048 + 1024 + lane * 16) = wf[1];
      }
      st4(xk, acc[2][2], acc[2][3], acc[3][2], acc[3][3]);
      st4(smem + L_XV + wid * 4096 + lane * 16, acc[2][4], acc[2][5], acc[3][4], acc[3][5]);
      lbar();
      st4(smem + L_XQ + wid * 4096 + lane * 16, acc[2][0], acc[2][1], acc[3][0], acc[3][1]);
#pragma unroll
      for (int t = 0; t < 2; ++t)
#pragma unroll
        for (int u = 0; u < 2; ++u) { fq[t][u] = acc[t][u]; fk[t][u] = acc[t][2 + u]; fv[t][u] = acc[t][4 + u]; }
    }
    const int rbase = wm * 64 + mi * 32, q = r16 >> 2, p = lane & 3;
    LAS const float* cs = (LAS const float*)(smem + L_CS) + hh * 32;
    LAS const f32x2* rst = (LAS const f32x2*)(smem + L_RST) + rbase;
    auto feature = [&](const f32x4 (&x)[2][2], int part, f32x4 (&e)[2][2][2]) {
      const f32x4 c0 = *(LAS const f32x4*)(cs + part * 64 + 4 * g), c1 = *(LAS const f32x4*)(cs + part * 64 + 16 + 4 * g);
      f32x4 pp[2][2];
      float sc[2];
#pragma unroll
      for (int t = 0; t < 2; ++t) {
        const f32x2 ms = rst[t * 16 + r16];
        sc[t] = ms[1] * 1.4426950408889634f;
        f32x4 y0, y1;
#pragma unroll
        for (int e_ = 0; e_ < 4; ++e_) { y0[e_] = __builtin_fmaf(-ms[0], c0[e_], x[t][0][e_]); y1[e_] = __builtin_fmaf(-ms[0], c1[e_], x[t][1][e_]); }
        const bf16x8 yb = pack_frag(y0, y1);
        pp[t][0] = MFMA16(wf[0], yb, ((f32x4){0.f, 0.f, 0.f, 0.f}));
        pp[t][1] = MFMA16(wf[1], yb, ((f32x4){0.f, 0.f, 0.f, 0.f}));
      }
      dual_softmax(pp, sc, e);
    };
    {
      const float cv0 = cs[128 + r16], cv1 = cs[144 + r16];
#pragma unroll
      for (int t = 0; t < 2; ++t) {
        const f32x4 s01 = *(LAS const f32x4*)(rst + t * 16 + 4 * g), s23 = *(LAS const f32x4*)(rst + t * 16 + 4 * g + 2);
        const float mv[4] = {s01[0], s01[2], s23[0], s23[2]}, rv[4] = {s01[1], s01[3], s23[1], s23[3]};
#pragma unroll
        for (int e_ = 0; e_ < 4; ++e_) {
          fv[t][0][e_] = __builtin_fmaf(-mv[e_], cv0, fv[t][0][e_]) * rv[e_];
          fv[t][1][e_] = __builtin_fmaf(-mv[e_], cv1, fv[t][1][e_]) * rv[e_];
        }
      }
      LAS char* vd = smem + (loader ? L_XV + lw * 4096 : L_VF + lw * 2048) + lane * 16;
      *(LAS bf16x8*)vd = pack_frag(fv[0][0], fv[1][0]);
      *(LAS bf16x8*)(vd + 1024) = pack_frag(fv[0][1], fv[1][1]);
      f32x4 e[2][2][2];
      feature(fk, 1, e);
      LAS char* wr = smem + L_PHI + hh * 17408 + (rbase + r16) * 136 + g * 8;
#pragma unroll
      for (int w = 0; w < 2; ++w)
#pragma unroll
        for (int t = 0; t < 2; ++t)
#pragma unroll
          for (int u = 0; u < 2; ++u)
            *(LAS u32x2*)(wr + t * 16 * 136 + w * 64 + u * 32) = (u32x2){pk2(e[w][t][u][0], e[w][t][u][1]), pk2(e[w][t][u][2], e[w][t][u][3])};
    }
    lbar();
    __builtin_amdgcn_s_setprio(0);
    bf16x8 pf[2][2];
    auto qpath = [&]() {
      f32x4 e[2][2][2];
      feature(fq, 0, e);
#pragma unroll
      for (int t = 0; t < 2; ++t)
#pragma unroll
        for (int w = 0; w < 2; ++w) { pf[t][w] = pack_frag(e[w][t][0], e[w][t][1]); asm volatile("" : "+v"(pf[t][w])); }
    };
    if (loader) {
      ld4(smem + L_XQ + lw * 4096 + lane * 16, fq);
      qpath();
      load_wtile<224, 4, 7>(smem + L_WT, mkrsrc(W1, D_ * FF_ * 2), tm * 64 * (FF_ * 2) + gp * 448, FF_ * 2, 128, lw, lane);
    } else {
      f32x4 k00 = {0.f, 0.f, 0.f, 0.f}, k01 = k00, k10 = k00, k11 = k00, z0 = k00, z1 = k00;
      const s16x8 one_ = {0x3f80, 0x3f80, 0x3f80, 0x3f80, 0x3f80, 0x3f80, 0x3f80, 0x3f80};
      LAS const char* rd = smem + L_PHI + hh * 17408 + (4 * g + q) * 136 + wm * 64 + p * 8;
#pragma unroll
      for (int u = 0; u < 4; ++u) {
        const bf16x8 pa0 = ld_tr2(rd + 32 * u * 136, rd + (32 * u + 16) * 136), pa1 = ld_tr2(rd + 32 * u * 136 + 32, rd + (32 * u + 16) * 136 + 32);
        LAS const char* vp = smem + ((u & 1) ? L_XV + ((u >> 1) * 2 + hh) * 4096 : L_VF + ((u >> 1) * 2 + hh) * 2048) + lane * 16;
        const bf16x8 v0 = ld_b128(vp), v1 = ld_b128(vp + 1024);
        k00 = MFMA16(pa0, v0, k00); k10 = MFMA16(pa1, v0, k10);
        k01 = MFMA16(pa0, v1, k01); k11 = MFMA16(pa1, v1, k11);
        z0 = MFMA16(pa0, __builtin_bit_cast(bf16x8, one_), z0); z1 = MFMA16(pa1, __builtin_bit_cast(bf16x8, one_), z1);
      }
      const int kb = kvp_grp + tm * KVP_BYTES;
      st128(pack_xy(k00, k10), rws, kb + hh * 4096 + wm * 2048 + lane * 16);
      st128(pack_xy(k01, k11), rws, kb + hh * 4096 + wm * 2048 + 1024 + lane * 16);
      st128(__builtin_bit_cast(u32x4, r16 == 0 ? z0 : z1), rws, r16 < 2 ? kb + 8192 + hh * 256 + wm * 128 + g * 32 + r16 * 16 : (int)0x80000000);
      wait_vm<0>();
    }
    lbar();
    if (tid == 0) set_flag(ctl, C_KV + gp, tm, tag);
    if (loader) {
      load_wo<0, WO1>(smem, mkrsrc(WO, D_ * D_ * 2), gp, lw, lane);
    } else {
      qpath();
    }
    FastPoll fpk = {0u, 0u, 0u, 0u};
    if (wid == 0) {
      fpk.run(ctl, C_KV + gp, 0, 16, tag, lane);
    }
    bar();
    {
      u32x4 tz[16];
      if (wid == 7) {
#pragma unroll
        for (int j = 0; j < 16; ++j) tz[j] = ld128(rws, lane < 32 ? kvp_grp + 8192 + lane * 16 : 0x80000000u, j * KVP_BYTES);
      }
      u32x4 t[16];
#pragma unroll
      for (int j = 0; j < 16; ++j) t[j] = ld128(rws, kvp_grp + tid * 16, j * KVP_BYTES);
      if (wid == 7) {
        f32x4 z = __builtin_bit_cast(f32x4, tz[0]);
#pragma unroll
        for (int j = 1; j < 16; ++j) z += __builtin_bit_cast(f32x4, tz[j]);
        if (lane < 32) *(LAS u32x2*)(smem + L_KVT + 8192 + lane * 8) = (u32x2){pk2(z[0], z[1]), pk2(z[2], z[3])};
      }
      f32x4 x = {0.f, 0.f, 0.f, 0.f}, y = x;
#pragma unroll
      for (int j = 0; j < 16; ++j) add_bf8(x, y, t[j]);
      *(LAS u32x4*)(smem + L_KVT + tid * 16) = pack_xy(x, y);
    }
    fpk.keep();
    lbar();
    {
      bf16x8 ka[2][2], za[2];
#pragma unroll
      for (int w = 0; w < 2; ++w) {
        ka[w][0] = ld_b128(smem + L_KVT + hh * 4096 + w * 2048 + lane * 16);
        ka[w][1] = ld_b128(smem + L_KVT + hh * 4096 + w * 2048 + 1024 + lane * 16);
        za[w] = ld_b128(smem + L_KVT + 8192 + hh * 128 + w * 64 + g * 16);
      }
#pragma unroll
      for (int t = 0; t < 2; ++t) {
        f32x4 o0 = {0.f, 0.f, 0.f, 0.f}, o1 = o0, dn = o0;
#pragma unroll
        for (int w = 0; w < 2; ++w) { dn = MFMA16(za[w], pf[t][w], dn); o0 = MFMA16(ka[w][0], pf[t][w], o0); o1 = MFMA16(ka[w][1], pf[t][w], o1); }
        const float rd = __builtin_amdgcn_rcpf(dn[0] + 1e-6f);
#pragma unroll
        for (int e_ = 0; e_ < 4; ++e_) { o0[e_] *= rd; o1[e_] *= rd; }
        swap_pair(o0, o1);
        st128(pack_xy(o0, o1), rws, WS_O + (tm * 32 + head) * 8192 + (rbase + t * 16) * 64 + cswz);
      }
    }
    wait_vm<0>();
    lbar();
    if (tid == 0) set_flag(ctl, C_O + tm, gp, tag);
  }

  relane();
  {
    FastPoll fp = {0u, 0u, 0u, 0u};
    if (loader) {
      load_wo<WO1, 16>(smem, mkrsrc(WO, D_ * D_ * 2), gp, lw, lane);
      colsum_tile<224>(smem + L_WT, rws, WS_CS3 + (gp * 16 + tm) * 896, lw, lane);
    }
    if (wid == 0) {
      fp.run(ctl, C_O + tm, 0, 16, tag, lane);
      if (bid == 0 && lane == 0) __hip_atomic_store(ctl + C_EPOCH * 64, tag, __ATOMIC_RELAXED, __HIP_MEMORY_SCOPE_AGENT);
    }
    if (loader) {
      wait_vm<0>();
      bar();
      if (lane == 0) set_flag(ctl, C_CS3 + gp, tm * 4 + lw, tag);
    } else {
      const int row = tm * 128 + wid * 32 + r16, col = gp * 64 + cpair;
      u32x4 res[2][2];
#pragma unroll
      for (int i = 0; i < 2; ++i)
#pragma unroll
        for (int j = 0; j < 2; ++j) res[i][j] = *(const u32x4*)(X + (size_t)(row + i * 16) * D_ + col + j * 32);
      f32x4 acc[2][4];
#pragma unroll
      for (int j = 0; j < 8; ++j) { acc[j >> 2][j & 3] = (f32x4){0.f, 0.f, 0.f, 0.f}; asm volatile("" : "+v"(acc[j >> 2][j & 3])); }
      constexpr int PF = PFD, BD = BDEPTH;
      const int q = r16 >> 2, p = lane & 3;
      int bo[4];
#pragma unroll
      for (int j = 0; j < 4; ++j) bo[j] = (8 * g + q) * 128 + (((2 * j + (p >> 1)) ^ bswz<64>(8 * g + q)) * 16) + 8 * (p & 1);
      const int ao = WS_O + tm * 262144 + wid * 2048 + lane * 16;
      u32x4 af[PF][2];
      s16x4 bf[BD][4][2];
      auto ldb = [&](int sl, int k) {
        bf[sl % BD][k >> 1][k & 1] = __builtin_amdgcn_ds_read_tr16_b64_v4i16((LAS s16x4*)(smem + wo_slot(sl >> 1) + bo[k >> 1] + (32 * (sl & 1) + 4 * (k & 1)) * 128));
      };
      bar();
#pragma unroll
      for (int sl = 0; sl < PF; ++sl) { af[sl][0] = ld128(rws, ao, sl * 8192); af[sl][1] = ld128(rws, ao + 1024, sl * 8192); }
#pragma unroll
      for (int k = 0; k < 8 * (BD - 1); ++k) ldb(k >> 3, k & 7);
      __builtin_amdgcn_sched_barrier(0);
#pragma unroll
      for (int sl = 0; sl < 32; ++sl) {
#pragma unroll
        for (int m = 0; m < 8; ++m) {
          const int i = m >> 2, j = m & 3;
          mfma16(acc[i][j], __builtin_bit_cast(bf16x8, __builtin_shufflevector(bf[sl % BD][j][0], bf[sl % BD][j][1], 0, 1, 2, 3, 4, 5, 6, 7)),
                 __builtin_bit_cast(bf16x8, af[sl % PF][i]));
          if (sl + BD - 1 < 32) ldb(sl + BD - 1, m);
          if (j == 3 && sl + PF < 32) af[sl % PF][i] = ld128(rws, ao + i * 1024, (sl + PF) * 8192);
          __builtin_amdgcn_sched_barrier(0);
        }
        if (sl == 30) lbar();
      }
      fp.keep();
      mfma_drain();
#pragma unroll
      for (int j = 0; j < 8; ++j) asm volatile("" : "+v"(acc[j >> 2][j & 3]));
#pragma unroll
      for (int i = 0; i < 2; ++i) {
        float s = 0.f, qq = 0.f;
#pragma unroll
        for (int j = 0; j < 2; ++j) {
          f32x4 x = acc[i][2 * j], y = acc[i][2 * j + 1];
          swap_pair(x, y);
          add_bf8(x, y, res[i][j]);
          const u32x4 o = pack_xy(x, y);
          st128(o, rws, WS_H + (tm * 32 + gp * 2 + j) * 8192 + (wid * 32 + i * 16) * 64 + cswz);
          *(LAS u32x4*)(smem + j * 8192 + (wid * 32 + i * 16) * 64 + cswz) = o;
#pragma unroll
          for (int e = 0; e < 4; ++e) { const float lo = bflo(o[e]), hi = bfhi(o[e]); s += lo + hi; qq = __builtin_fmaf(lo, lo, __builtin_fmaf(hi, hi, qq)); }
        }
        red2(s, qq, [](float x, float y) { return x + y; });
        __builtin_amdgcn_raw_buffer_store_b64(__builtin_bit_cast(u32x2, (f32x2){s, qq}), rws,
                                              g == 0 ? WS_HST + ((tm * 16 + gp) * 128 + wid * 32 + i * 16 + r16) * 8 : 0x80000000u, 0, SC1);
      }
    }
  }

  const int kh = gp >> 3;
  if (loader) {
    __builtin_amdgcn_s_setprio(3);
    relane();
    const rsrc_t r2 = mkrsrc(W2, FF_ * D_ * 2);
    FastPoll fp;
    {
      Loader<224, 3, 16, false> L;
      L.init(smem, rws, mkrsrc(W1, D_ * FF_ * 2), WS_H + tm * 128 * D_ * 2, 0, gp * 448, FF_ * 2, 128, lw, lane, gp);
      __builtin_amdgcn_s_sleep(B3DELAY);
      L.issueB(0);
      bar();
      bar();
      if (wid == 4 && lane == 0) set_flag(ctl, C_H + tm, gp, tag);
      wait_vm<0>(); bar();
      L.issueB(1); L.issueB(2);
      poll4(fp, smem, 2, lw, ctl, C_H + tm, 0, 16, tag, lane);
      L.issueA(1); L.issueA(2);
      u32x4 gt[4], hs[4];
#pragma unroll
      for (int j = 0; j < 4; ++j) hs[j] = ld128(rws, WS_HST + tm * 16384 + lw * 4096 + lane * 16, j * 1024);
      const unsigned seen = peek(ctl, C_CS3 + gp, lane);
      L.tn = 3;
      L.step<4 + 5>();
      fp.keep();
      L.step<5 + 11>();
      confirm(seen, ctl, C_CS3 + gp, tag, lane);
#pragma unroll
      for (int j = 0; j < 4; ++j) gt[j] = ld128(rws, WS_CS3 + (gp * 16 + lw * 4) * 896 + lane * 16, j * 896);
      *(LAS f32x4*)(smem + L_CSS + 4096 + lw * 1024 + lane * 16) = sum4(hs);
      L.step();
      if (lw == 1) {
        const f32x4 sum = lds_sum4(smem + L_CSS + 4096 + lane * 16);
        const float m0 = sum[0] * (1.0f / D_), m1 = sum[2] * (1.0f / D_);
        const float v0 = __builtin_fmaxf(sum[1] * (1.0f / D_) - m0 * m0, 0.f), v1 = __builtin_fmaxf(sum[3] * (1.0f / D_) - m1 * m1, 0.f);
        *(LAS f32x4*)(smem + L_RST + lane * 16) = (f32x4){m0, __builtin_amdgcn_rsqf(v0 + LN_EPS), m1, __builtin_amdgcn_rsqf(v1 + LN_EPS)};
      }
      L.step();
      *(LAS f32x4*)(smem + L_CSS + lw * 1024 + lane * 16) = sum4(gt);
      L.step();
      if (lw == 0) *(LAS f32x4*)(smem + L_CS + lane * 16) = lds_sum4(smem + L_CSS + lane * 16);
      const int stag = (loc & 7) < 6 ? (loc & 7) : 6;
      L.steps(stag);
      touch_lines(smem, r2, bid * 28672, lane < 56 ? lw * 56 + lane : 4096, 224);
      L.step();
      L.step<12>();
      L.steps(6 - stag);
    }
    {
      relane();
      Loader<128, 4, 31, false, 1> L;
      L.kh = kh;
      L.init(smem, rws, r2, WS_H1 + tm * 112 * 8192, 0, gp * 128, D_ * 2, kh ? -1024 : 1024, lw, lane, gp & 7);
      L.issueB(0);
      wait_vm<11 + 4>(); lbar(); wait_vm<4>(); bar();
      bar();
      L.issueB(1); L.issueB(2); L.issueB(3);
      wait_vm<12>(); bar();
      if (wid == 4 && lane == 0) set_flag(ctl, C_H1 + tm, gp, tag);
      wait_vm<8>(); bar();
      wait_vm<4>(); bar();
      L.issueB(4);
      poll4(fp, smem, 3, lw, ctl, C_H1 + tm, kh * 8, 8, tag, lane);
      L.issueA(3); L.issueA(4);
      L.tn = 5;
      L.step<4>();
      fp.keep();
      L.steps(3);
      if (wid == 4 && lane == 0) set_flag(ctl, C_H1B + tm, gp, tag);
      L.steps(5);
      const unsigned seen = peek(ctl, C_H1B + tm, lane, 16);
      L.steps(3);
      confirm(seen, ctl, C_H1B + tm, tag, lane, 16);
      L.steps(8);
      L.init_half(gp * 128, D_ * 2, lw, lane);
      wait_vm<8>(); bar(); L.issueH(25);
      wait_vm<6>(); bar(); L.issueH(26);
#pragma unroll 1
      for (int t = 27; t <= 30; ++t) { wait_vm<6>(); bar(); L.issueH(t); }
      wait_vm<6>(); bar();
      wait_vm<0>(); bar();
      bar();
    }
  } else {
    const int wm = wid >> 1, wn = wid & 1;
    relane();
    wait_vm<0>();
    bar();
    {
      const int par = gp & 1;
      auto slab3 = [&](int a) { return wn * 4 + (a + par == 3 ? 0 : a + par); };
      auto ntile3 = [&](int j) { return j < 6 ? slab3(j >> 1) * 2 + (j & 1) : 6 + wn; };
      f32x4 acc[4][7];
      mma_loop<224, 3, 16, 4, 7, false, true>(smem, acc, wm * 64, lane, ntile3, []() {}, [](int) {});
      relane();
      f32x4 cs[7];
#pragma unroll
      for (int j = 0; j < 7; ++j) cs[j] = *(LAS const f32x4*)(smem + L_CS + (ntile3(j) * 16 + g * 4) * 4);
      float fa[4], fb[4];
#pragma unroll
      for (int i = 0; i < 4; ++i) {
        const f32x2 ms = *(LAS const f32x2*)(smem + L_RST + (wm * 64 + i * 16 + r16) * 8);
        fa[i] = ms[1]; fb[i] = -ms[0] * ms[1];
      }
      auto act = [&](int i, int j) {
        f32x4 y;
#pragma unroll
        for (int e = 0; e < 4; ++e) y[e] = __builtin_fmaxf(__builtin_fmaf(acc[i][j][e], fa[i], fb[i] * cs[j][e]), 0.f);
        return y;
      };
      const int hb = WS_H1 + (tm * 112 + gp * 7) * 8192;
#pragma unroll
      for (int a = 0; a < 3; ++a) {
        const int sl = slab3(a);
#pragma unroll
        for (int i = 0; i < 4; ++i) {
          f32x4 x = act(i, 2 * a), y = act(i, 2 * a + 1);
          swap_pair(x, y);
          const u32x4 o = pack_xy(x, y);
          const int ro = (wm * 64 + i * 16) * 64 + cswz;
          st128(o, rws, hb + sl * 8192 + ro);
          if (a < 2 || (unsigned)(sl - par) < 6u) *(LAS u32x4*)(smem + (sl - par) * 8192 + ro) = o;
        }
        asm volatile("" ::: "memory");
      }
#pragma unroll
      for (int i = 1; i < 4; i += 2) {
        f32x4 x = act(i - 1, 6), y = act(i, 6);
        swap_pair(x, y);
        const u32x4 o = pack_xy(x, y);
        const int r3 = (wm * 64 + (i - 1 + (g & 1)) * 16) * 64 + (2 * wn + (g >> 1)) * 256 + r16 * 16;
        st128(o, rws, hb + 3 * 8192 + r3);
        *(LAS u32x4*)(smem + (3 - par) * 8192 + r3) = o;
      }
      wait_vm<6>();
    }
    relane();
    {
      const int row = tm * 128 + wm * 64 + r16, col = gp * 64 + wn * 32 + cpair;
      u32x4 res[4], t[4];
      unsigned seen = 0;
      f32x4 acc[4][4];
      mma_loop<128, 4, 31, 4, 4, true, true>(smem, acc, wm * 64, lane, [&](int j) { return (j >> 1) * 4 + wn * 2 + (j & 1); }, [&]() {
        wait_vm<0>();
#pragma unroll
        for (int i = 0; i < 4; ++i) res[i] = ld128(rws, WS_H + (tm * 32 + gp * 2 + wn) * 8192 + (wm * 64 + i * 16) * 64 + cswz, 0);
      }, [&](int what) {
        if (what == 0) {
          const int pb = WS_P4 + (tm * 16 + gp) * 16384 + wid * 4096 + lane * 16;
#pragma unroll
          for (int i = 0; i < 4; ++i) st128(pack_xy(acc[i][2], acc[i][3]), rws, pb + i * 1024);
        } else if (what == 1) {
          wait_vm<0>();
          if (lane == 0) set_flag(ctl, C_P4 + tm, gp * 4 + wid, tag);
        } else if (what == 2) {
          seen = __hip_atomic_load(ctl + (C_P4 + tm) * 64 + (gp ^ 8) * 4 + wid, __ATOMIC_RELAXED, __HIP_MEMORY_SCOPE_AGENT);
        } else {
          asm volatile("" : "+v"(seen));
          if (!wave_all(seen == tag)) poll(ctl, C_P4 + tm, (gp ^ 8) * 4 + wid, 1, tag, lane);
          acquire();
          const int qb = WS_P4 + (tm * 16 + (gp ^ 8)) * 16384 + wid * 4096 + lane * 16;
#pragma unroll
          for (int i = 0; i < 4; ++i) t[i] = ld128(rws, qb + i * 1024, 0);
        }
      });
#pragma unroll
      for (int i = 0; i < 4; ++i) {
        f32x4 x = acc[i][0], y = acc[i][1];
        add_bf8(x, y, t[i]);
        swap_pair(x, y);
        add_bf8(x, y, res[i]);
        *(u32x4*)(OUT + (size_t)(row + i * 16) * D_ + col) = pack_xy(x, y);
      }
    }
  }
#endif
}
#if defined(__HIP_DEVICE_COMPILE__)
#pragma clang attribute pop
#endif

extern "C" void kernel_launch(void* const* d_in, const int* in_sizes, int n_in, void* d_out, int out_size, void* d_ws, size_t ws_size,
                              hipStream_t stream) {
  (void)hipFuncSetAttribute((const void*)block_kernel, hipFuncAttributeMaxDynamicSharedMemorySize, LDS_BYTES);
  block_kernel<<<256, 512, LDS_BYTES, stream>>>((const u16*)d_in[0], (const u16*)d_in[1], (const u16*)d_in[2], (const u16*)d_in[3],
                                                (const u16*)d_in[5], (const u16*)d_in[6], (u16*)d_out, (char*)d_ws);
}
```

```cpp
#define SEGA 200
#define SEGB 200
#include <hip/hip_runtime.h>
#include <hip/hip_bf16.h>
#include <type_traits>

#define LAS __attribute__((address_space(3)))
#define GAS __attribute__((address_space(1)))
#define DI __device__ __forceinline__
typedef __bf16 bf16x8 __attribute__((ext_vector_type(8)));
typedef __bf16 bf16x2 __attribute__((ext_vector_type(2)));
typedef short s16x4 __attribute__((ext_vector_type(4)));
typedef short s16x8 __attribute__((ext_vector_type(8)));
typedef float f32x2 __attribute__((ext_vector_type(2)));
typedef float f32x4 __attribute__((ext_vector_type(4)));
typedef unsigned u32x2 __attribute__((ext_vector_type(2)));
typedef unsigned u32x4 __attribute__((ext_vector_type(4)));
typedef unsigned short u16;

constexpr int S_ = 2048, D_ = 1024, FF_ = 3584;
constexpr float LN_EPS = 1e-5f;

constexpr int C_EPOCH = 0, C_TMO = 1, C_ST = 2, C_CS1 = 18, C_CS3 = 34, C_KV = 50, C_O = 66, C_H = 82, C_H1 = 98, C_P4 = 114, C_H1B = 130, C_LINES = 146;
constexpr unsigned WS_XST = 40960;
constexpr unsigned WS_CS1 = WS_XST + S_ * 8;
constexpr unsigned WS_CS3 = WS_CS1 + 16 * 16 * 192 * 4;
constexpr unsigned WS_HST = WS_CS3 + 16 * 16 * 224 * 4;
constexpr unsigned KVP_BYTES = 8192 + 512;
constexpr unsigned WS_KVP = WS_HST + S_ * 16 * 8;
#define BLOCKED 2
constexpr unsigned WS_O = WS_KVP + 256 * KVP_BYTES;
constexpr unsigned WS_H = WS_O + S_ * D_ * 2;
constexpr unsigned WS_H1 = WS_H + S_ * D_ * 2;
constexpr unsigned WS_P4 = WS_O, WS_END = WS_H1 + S_ * FF_ * 2;
static_assert(C_LINES * 256 <= (int)WS_XST && WS_O % 256 == 0 && WS_KVP % 256 == 0, "");

constexpr int L_WT = 120 * 1024;
constexpr int L_TAB = 149 * 1024;
constexpr int L_DUMMY = L_TAB;
constexpr int L_RST = L_TAB + 256;
constexpr int L_CS = L_RST + 1024;
constexpr int L_CSS = L_CS + 1024;
constexpr int L_GO = L_CSS + 8192;
constexpr int LDS_BYTES = L_GO + 16;
static_assert(LDS_BYTES <= 160 * 1024, "");

#define STAMP(k, i) do { if ((k) * 16 + (i) == SEGA) tseg_a = __builtin_amdgcn_s_memrealtime(); if ((k) * 16 + (i) == SEGB) tseg_b = __builtin_amdgcn_s_memrealtime(); } while (0)
#define TS_DECL
#define TS(t)
#define TS_OUT(k)

#if defined(__HIP_DEVICE_COMPILE__)
#pragma clang attribute push(__attribute__((target("no-packed-fp32-ops"))), apply_to = function)
typedef __amdgpu_buffer_rsrc_t rsrc_t;
DI float u2f(unsigned u) { return __builtin_bit_cast(float, u); }
DI unsigned f2u(float f) { return __builtin_bit_cast(unsigned, f); }
DI bool wave_all(bool p) { return __builtin_amdgcn_ballot_w64(p) == ~0ull; }
#define CODE_AHEAD 52224
#define PSLEEP "2"
#define WO1 9
#define BDEPTH 3
#define PFD 8
#define B3DELAY 48
#define CPW 0
constexpr int SC1 = 16;
template <int N> DI void wait_vm() { asm volatile("s_waitcnt vmcnt(%0)" ::"n"(N) : "memory"); }
DI void wait_lgkm0() { asm volatile("" ::: "memory"); __builtin_amdgcn_s_waitcnt(0xC07F); asm volatile("" ::: "memory"); }
DI void bar() { asm volatile("" ::: "memory"); __builtin_amdgcn_s_barrier(); asm volatile("" ::: "memory"); }
DI void lbar() { wait_lgkm0(); bar(); }
DI unsigned pk2(float a, float b) { f32x2 v = {a, b}; return __builtin_bit_cast(unsigned, __builtin_convertvector(v, bf16x2)); }
DI float bflo(unsigned u) { return u2f(u << 16); }
DI float bfhi(unsigned u) { return u2f(u & 0xffff0000u); }
DI bf16x8 ld_b128(LAS const char* p) { return *(LAS const bf16x8*)p; }
DI bf16x8 ld_tr2(LAS const char* p0, LAS const char* p1) {
  s16x4 a = __builtin_amdgcn_ds_read_tr16_b64_v4i16((LAS s16x4*)p0);
  s16x4 b = __builtin_amdgcn_ds_read_tr16_b64_v4i16((LAS s16x4*)p1);
  return __builtin_bit_cast(bf16x8, __builtin_shufflevector(a, b, 0, 1, 2, 3, 4, 5, 6, 7));
}
DI float x32_sum(float v) { auto r = __builtin_amdgcn_permlane32_swap(f2u(v), f2u(v), false, false); return u2f(r[0]) + u2f(r[1]); }
DI float x16_sum(float v) { auto r = __builtin_amdgcn_permlane16_swap(f2u(v), f2u(v), false, false); return u2f(r[0]) + u2f(r[1]); }
template <int CTRL> DI float dpp_add(float v) { return v + u2f(__builtin_amdgcn_update_dpp(0, (int)f2u(v), CTRL, 0xf, 0xf, true)); }
DI float half_sum(float v) {
  v = dpp_add<0xB1>(v);
  v = dpp_add<0x4E>(v);
  v = dpp_add<0x141>(v);
  v = dpp_add<0x140>(v);
  return x16_sum(v);
}
#define MFMA16(a, b, c) __builtin_amdgcn_mfma_f32_16x16x32_bf16((a), (b), (c), 0, 0, 0)
DI rsrc_t mkrsrc(const void* p, unsigned bytes) { return __builtin_amdgcn_make_buffer_rsrc((void*)p, 0, bytes, 0x00020000); }
DI void st128(u32x4 v, rsrc_t r, int off) { __builtin_amdgcn_raw_buffer_store_b128(v, r, off, 0, SC1); }
DI u32x4 ld128(rsrc_t r, int off, int soff) { return __builtin_amdgcn_raw_buffer_load_b128(r, off, soff, SC1); }

DI void set_flag(GAS unsigned* ctl, int line, int word, unsigned tag) { __hip_atomic_store(ctl + line * 64 + word, tag, __ATOMIC_RELAXED, __HIP_MEMORY_SCOPE_AGENT); }
DI void acquire() {
  asm volatile("" ::: "memory");
}
DI void poll(GAS unsigned* ctl, int line, int lo, int n, unsigned tag, int lane) {
  GAS unsigned* p = ctl + line * 64 + lo + (lane < n ? lane : 0);
  unsigned spins = 0;
  while (!wave_all(__hip_atomic_load(p, __ATOMIC_RELAXED, __HIP_MEMORY_SCOPE_AGENT) == tag)) {
    __builtin_amdgcn_s_sleep(1);
    if (++spins > (1u << 17)) { if (lane == 0) __hip_atomic_store(ctl + C_TMO * 64, (unsigned)line, __ATOMIC_RELAXED, __HIP_MEMORY_SCOPE_AGENT); break; }
  }
  acquire();
}
struct FastPoll {
  unsigned a, b, c, d;
  DI void run(GAS unsigned* ctl, int line, int lo, int n, unsigned tag, int lane) {
    GAS unsigned* p = ctl + line * 64 + lo + (lane < n ? lane : 0);
    unsigned cnt = 1u << 17;
#define FP_LD(r) "global_load_dword %" #r ", %5, off sc1\n\ts_sleep " PSLEEP "\n\t"
#define FP_CK(r) "s_waitcnt vmcnt(3)\n\tv_cmp_eq_u32 vcc, %6, %" #r "\n\ts_cmp_eq_u64 vcc, -1\n\ts_cbranch_scc1 2f\n\t" FP_LD(r)
    asm volatile(FP_LD(0) FP_LD(1) FP_LD(2) FP_LD(3) "1:\n\t" FP_CK(0) FP_CK(1) FP_CK(2) FP_CK(3)
                 "s_sub_u32 %4, %4, 1\n\ts_cbranch_scc0 1b\n\t2:"
                 : "=&v"(a), "=&v"(b), "=&v"(c), "=&v"(d), "+s"(cnt) : "v"(p), "s"(tag) : "vcc", "scc", "memory");
#undef FP_LD
#undef FP_CK
  }
  DI void keep() const { asm volatile("" :: "v"(a), "v"(b), "v"(c), "v"(d)); }
};
DI void poll4(FastPoll& fp, LAS char* smem, unsigned go, int lw, GAS unsigned* ctl, int line, int lo, int n, unsigned tag, int lane) {
  LAS volatile unsigned* w = (LAS volatile unsigned*)(smem + L_GO);
  if (lw == 0) {
    fp.run(ctl, line, lo, n, tag, lane);
    *w = go;
  } else {
    fp.a = fp.b = fp.c = fp.d = 0u;
    unsigned spins = 0;
    while (__builtin_amdgcn_readfirstlane(*w) != go && ++spins < (1u << 24)) __builtin_amdgcn_s_sleep(1);
  }
  asm volatile("" ::: "memory");
}

template <int BN> DI constexpr int bswz(int k) {
  return BN == 224 ? (((k >> 3) & 1) << 1)
       : BN == 128 ? (((k & 3) | (((k >> 3) & 1) << 2)) << 1)
                   : ((((k >> 1) & 1) | (((k >> 3) & 1) << 1)) << 1);
}

template <int BN, int NS> DI int bslot(int s) {
  if (BN == 224) return ((0x4c3078 >> (8 * s)) & 255) << 10;
  if (BN == 128) return ((0x78504068 >> (8 * s)) & 255) << 10;
  return NS * 16384 + s * BN * 128;
}
DI constexpr int wo_slot(int t) { return t < 5 ? 81920 + t * 8192 : t < 15 ? (t - 5) * 8192 : L_CSS; }

template <int BN, int NS, int NT, bool AROW, int LAG = 0>
struct Loader {
  static constexpr int PA = 4, PB = BN / 32, PW = PA + PB, BST = BN * 128, CPR = BN / 8;
  static_assert(NS * (16384 + BST) <= L_TAB, "");
  LAS char* dst0; rsrc_t ra, rb; int va[PA], vb[PB], vh[2]; int ldb64, own0, tn;
  static constexpr int NOWN = BN == 128 ? 3 : BN == 224 ? 1 : 0;
  int kh;
  DI int ktile(int t) const {
    if (AROW) return (t + own0) & (NT - 1);
    if (BN != 128) return t < NOWN ? own0 + t : (t - NOWN < own0 ? t - NOWN : t);
    const int pi = t < 17 ? (t - 3) >> 1 : t - 17, p = t < 3 ? own0 : pi + (pi >= own0 ? 1 : 0);
    const int k = t >= 24 ? 7 * ((t >= 28 ? t - 3 : t) - 24) + 3 : ((7 * p + 1) >> 1) + (t < 3 ? t : t < 17 ? 2 * ((t - 3) & 1) : 1);
    return k + ((t >= 28) != (kh != 0) ? 28 : 0);
  }
  DI void issueA(int t) {
    const int kk = ktile(t), slot = t % NS;
#pragma unroll
    for (int i = 0; i < PA; ++i)
      __builtin_amdgcn_raw_ptr_buffer_load_lds(ra, dst0 + slot * 16384 + i * 4096, 16, va[i], kk * (AROW ? 128 : 16384), 0, AROW ? 0 : SC1);
  }
  DI void issueB(int t) {
    const int kk = ktile(t), slot = t % NS;
#pragma unroll
    for (int i = 0; i < PB; ++i) __builtin_amdgcn_raw_ptr_buffer_load_lds(rb, dst0 + bslot<BN, NS>(slot) + i * 4096, 16, vb[i], kk * ldb64, 0, 0);
  }
  DI void issueH(int t) {
    issueA(t);
    const int kk = ktile(t), slot = t % NS;
#pragma unroll
    for (int i = 0; i < 2; ++i) __builtin_amdgcn_raw_ptr_buffer_load_lds(rb, dst0 + bslot<BN, NS>(slot) + i * 4096, 16, vh[i], kk * ldb64, 0, 0);
  }
  DI void init(LAS char* smem, rsrc_t ra_, rsrc_t rb_, int a_off, int lda_b, int b_off, int ldb_b, int segb, int lw, int lane, int own) {
    dst0 = smem + lw * 1024; ra = ra_; rb = rb_; ldb64 = 64 * ldb_b; own0 = own; tn = NS - 1;
#pragma unroll
    for (int i = 0; i < PA; ++i) {
      const int ins = lw + i * 4, r = ins * 8 + (lane >> 3), c = (lane & 7) ^ ((r >> 1) & 7);
      va[i] = AROW ? a_off + r * lda_b + c * 16 : a_off + ins * 1024 + lane * 16;
    }
#pragma unroll
    for (int i = 0; i < PB; ++i) {
      const int ch = (lw + i * 4) * 64 + lane, k = ch / CPR, c = (ch % CPR) ^ bswz<BN>(k);
      vb[i] = b_off + k * ldb_b + (c >> 3) * segb + (c & 7) * 16;
    }
  }
  DI void init_half(int b_off, int ldb_b, int lw, int lane) {
#pragma unroll
    for (int i = 0; i < 2; ++i) { const int k = (lw + i * 4) * 8 + (lane >> 3); vh[i] = b_off + k * ldb_b + (((lane & 7) ^ bswz<64>(k)) * 16); }
  }
  template <int ALLOW = (NS - 2 - LAG) * PW> DI void step() { wait_vm<ALLOW>(); lbar(); issueA(tn); issueB(tn); ++tn; }
  DI void steps(int n) {
#pragma unroll 1
    for (int t = 0; t < n; ++t) step();
  }
  DI void finish() {
    if constexpr (NS - LAG >= 5) { wait_vm<3 * PW>(); lbar(); }
    if constexpr (NS - LAG >= 4) { wait_vm<2 * PW>(); lbar(); }
    if constexpr (NS - LAG >= 3) { wait_vm<1 * PW>(); lbar(); }
    wait_vm<0>(); lbar();
  }
};
template <int BN, int I0, int I1> DI void load_wtile(LAS char* dst, rsrc_t r, int off, int ldb_b, int segb, int lw, int lane) {
#pragma unroll
  for (int i = I0; i < I1; ++i) {
    const int ch = (lw + i * 4) * 64 + lane, k = ch / (BN / 8), c = (ch % (BN / 8)) ^ bswz<BN>(k);
    __builtin_amdgcn_raw_ptr_buffer_load_lds(r, dst + (lw + i * 4) * 1024, 16, off + k * ldb_b + (c >> 3) * segb + (c & 7) * 16, 0, 0, 0);
  }
}
template <int T0, int T1> DI void load_wo(LAS char* smem, rsrc_t ro, int gp, int lw, int lane) {
  int vo[2];
#pragma unroll
  for (int i = 0; i < 2; ++i) { const int k = (lw + i * 4) * 8 + (lane >> 3); vo[i] = gp * 128 + k * (D_ * 2) + (((lane & 7) ^ bswz<64>(k)) * 16); }
#pragma unroll
  for (int t = T0; t < T1; ++t)
#pragma unroll
    for (int i = 0; i < 2; ++i) __builtin_amdgcn_raw_ptr_buffer_load_lds(ro, smem + wo_slot(t) + (lw + i * 4) * 1024, 16, vo[i], t * 64 * (D_ * 2), 0, 0);
}
DI int opaque(int v) { asm volatile("" : "+v"(v)); return v; }

DI void touch_lines(LAS char* smem, rsrc_t r, int base, int line, int nlines) {
  __builtin_amdgcn_raw_ptr_buffer_load_lds(r, smem + L_DUMMY, 4, line < nlines ? base + line * 128 : (int)0x80000000, 0, 0, 0);
}

template <int BN> DI void colsum_tile(LAS char* img, rsrc_t rws, unsigned off, int lw, int lane) {
  constexpr int RS = BN * 2;
  const int r16 = lane & 15, g = lane >> 4, q = (lane & 15) >> 2, p = lane & 3;
  const s16x8 one_ = {0x3f80, 0x3f80, 0x3f80, 0x3f80, 0x3f80, 0x3f80, 0x3f80, 0x3f80};
  LAS const char* rd[4];
#pragma unroll
  for (int i = 0; i < 4; ++i) {
    const int nt = lw + 4 * i < BN / 16 ? lw + 4 * i : lw;
    rd[i] = img + (8 * g + q) * RS + (((2 * nt + (p >> 1)) ^ bswz<BN>(8 * g + q)) * 16) + 8 * (p & 1);
  }
  s16x4 v[4][4];
#define TR4(a, b, c, d, r) "ds_read_b64_tr_b16 %" #a ", %" #r "\n\tds_read_b64_tr_b16 %" #b ", %" #r " offset:%c20\n\t" \
                           "ds_read_b64_tr_b16 %" #c ", %" #r " offset:%c21\n\tds_read_b64_tr_b16 %" #d ", %" #r " offset:%c22\n\t"
  asm volatile(TR4(0, 1, 2, 3, 16) TR4(4, 5, 6, 7, 17) TR4(8, 9, 10, 11, 18) TR4(12, 13, 14, 15, 19) "s_waitcnt lgkmcnt(0)"
               : "=&v"(v[0][0]), "=&v"(v[0][1]), "=&v"(v[0][2]), "=&v"(v[0][3]), "=&v"(v[1][0]), "=&v"(v[1][1]), "=&v"(v[1][2]), "=&v"(v[1][3]),
                 "=&v"(v[2][0]), "=&v"(v[2][1]), "=&v"(v[2][2]), "=&v"(v[2][3]), "=&v"(v[3][0]), "=&v"(v[3][1]), "=&v"(v[3][2]), "=&v"(v[3][3])
               : "v"(rd[0]), "v"(rd[1]), "v"(rd[2]), "v"(rd[3]), "i"(4 * RS), "i"(32 * RS), "i"(36 * RS) : "memory");
#undef TR4
  f32x4 c[4];
#pragma unroll
  for (int i = 0; i < 4; ++i) {
    c[i] = (f32x4){0.f, 0.f, 0.f, 0.f};
    c[i] = MFMA16(__builtin_bit_cast(bf16x8, __builtin_shufflevector(v[i][0], v[i][1], 0, 1, 2, 3, 4, 5, 6, 7)), __builtin_bit_cast(bf16x8, one_), c[i]);
    c[i] = MFMA16(__builtin_bit_cast(bf16x8, __builtin_shufflevector(v[i][2], v[i][3], 0, 1, 2, 3, 4, 5, 6, 7)), __builtin_bit_cast(bf16x8, one_), c[i]);
  }
  const f32x4 out = r16 == 0 ? c[0] : r16 == 1 ? c[1] : r16 == 2 ? c[2] : c[3];
  st128(__builtin_bit_cast(u32x4, out), rws, lw + 4 * r16 < BN / 16 ? off + (lw + 4 * r16) * 64 + g * 16 : 0x80000000u);
}
DI f32x4 sum4(u32x4 (&t)[4]) {
  asm volatile("" : "+v"(t[0]), "+v"(t[1]), "+v"(t[2]), "+v"(t[3]));
  return (__builtin_bit_cast(f32x4, t[0]) + __builtin_bit_cast(f32x4, t[1])) + (__builtin_bit_cast(f32x4, t[2]) + __builtin_bit_cast(f32x4, t[3]));
}
DI f32x4 lds_sum4(LAS const char* p) {
  return (*(LAS const f32x4*)p + *(LAS const f32x4*)(p + 1024)) + (*(LAS const f32x4*)(p + 2048) + *(LAS const f32x4*)(p + 3072));
}
DI unsigned peek(GAS unsigned* ctl, int line, int lane, int n = 64) { return __hip_atomic_load(ctl + line * 64 + (lane & (n - 1)), __ATOMIC_RELAXED, __HIP_MEMORY_SCOPE_AGENT); }
DI void confirm(unsigned seen, GAS unsigned* ctl, int line, unsigned tag, int lane, int n = 64) {
  asm volatile("" : "+v"(seen));
  if (!wave_all(seen == tag)) poll(ctl, line, 0, n, tag, lane);
  else acquire();
}

DI void mfma16(f32x4& c, const bf16x8& a, const bf16x8& b) { asm volatile("v_mfma_f32_16x16x32_bf16 %0, %1, %2, %0" : "+v"(c) : "v"(a), "v"(b)); }
DI void mfma_drain() { asm volatile("s_nop 7\n\ts_nop 7" ::: "memory"); }
template <int TM, int TN> struct Frag { s16x4 b[TN][2]; bf16x8 a[TM]; };
template <int BN, int NS, int NT, int TM, int TN, bool DEEP, bool OWN, class NMap, class Mid, class Send>
DI void mma_loop(LAS char* smem, f32x4 (&acc)[TM][TN], int row0, int lane, NMap&& nt, Mid&& mid, Send&& send) {
  constexpr int RS = BN * 2, BST = BN * 128, NL = TM + 2 * TN, NM = TM * TN;
  typedef Frag<TM, TN> F;
  const int r16 = lane & 15, g = lane >> 4, q = (lane & 15) >> 2, p = lane & 3;
  constexpr bool AROW = BN == 192;
  const int aoff = AROW ? (row0 + r16) * 128 + ((g ^ (r16 >> 1)) * 16) : row0 * 64 + lane * 16;
  const int aoff1 = AROW ? aoff ^ 64 : aoff + 8192;
  int boff[TN];
#pragma unroll
  for (int j = 0; j < TN; ++j) boff[j] = (8 * g + q) * RS + (((2 * nt(j) + (p >> 1)) ^ bswz<BN>(8 * g + q)) * 16) + 8 * (p & 1);
#pragma unroll
  for (int i = 0; i < TM; ++i)
#pragma unroll
    for (int j = 0; j < TN; ++j) { acc[i][j] = (f32x4){0.f, 0.f, 0.f, 0.f}; asm volatile("" : "+v"(acc[i][j])); }
  int boffh[2];
  auto ld1 = [&](F& f, int slot, int s, int k, int tn = TN) {
    if (k == 0 || k > 2 * tn) { const int i = k == 0 ? 0 : k - 2 * tn; f.a[i] = ld_b128(smem + slot * 16384 + (s ? aoff1 : aoff) + i * (AROW ? 2048 : 1024)); }
    else {
      const int j = (k - 1) >> 1, u = (k - 1) & 1;
      f.b[j][u] = __builtin_amdgcn_ds_read_tr16_b64_v4i16((LAS s16x4*)(smem + bslot<BN, NS>(slot) + (tn == TN ? boff[j] + (32 * s + 4 * u) * RS : boffh[j & 1] + (32 * s + 4 * u) * 128)));
    }
  };
  auto mm1 = [&](const F& f, int m, int tn = TN) {
    const int i = m / tn, j = m % tn;
    const bf16x8 b = __builtin_bit_cast(bf16x8, __builtin_shufflevector(f.b[j][0], f.b[j][1], 0, 1, 2, 3, 4, 5, 6, 7));
    if (AROW && j >= 4) mfma16(acc[i][j], f.a[i], b); else mfma16(acc[i][j], b, f.a[i]);
  };
  auto ldall = [&](F& f, int slot, int s) {
#pragma unroll
    for (int k = 0; k < NL; ++k) ld1(f, slot, s, k);
  };
  auto mmall = [&](const F& f) {
#pragma unroll
    for (int m = 0; m < NM; ++m) mm1(f, m);
  };
  auto sub = [&](const F& x, F& u, int slot, int s) {
    constexpr int SP = DEEP ? NM : (NL < NM ? NL : NM - 2);
#pragma unroll
    for (int m = 0; m < NM; ++m) {
      mm1(x, m);
#pragma unroll
      for (int k = (m * NL + SP - 1) / SP; k < NL && k < ((m + 1) * NL + SP - 1) / SP; ++k) ld1(u, slot, s, k);
      __builtin_amdgcn_sched_barrier(0);
    }
  };
  auto subh = [&](const F& x, F& u, int slot, int s, auto tx, auto tu) {
    constexpr int TX = decltype(tx)::value, TU = decltype(tu)::value, M = TM * TX, LN = TM + 2 * TU;
#pragma unroll
    for (int m = 0; m < M; ++m) {
      mm1(x, m, TX);
#pragma unroll
      for (int k = (m * LN + M - 1) / M; k < LN && k < ((m + 1) * LN + M - 1) / M; ++k) ld1(u, slot, s, k, TU);
      __builtin_amdgcn_sched_barrier(0);
    }
  };
  auto nxt = [](int s) { return s == NS - 1 ? 0 : s + 1; };
  lbar();
  TS_DECL; TS(0);
  int slot = 0;
  if constexpr (!DEEP) {
    constexpr int NB = AROW ? 2 : TN, NA = AROW ? 2 : 1;
    LAS char* ca[NA];
    LAS char* cb[NB];
    ca[0] = smem + aoff;
    if constexpr (AROW) ca[NA - 1] = smem + aoff1;
#pragma unroll
    for (int j = 0; j < NB; ++j) cb[j] = smem + bslot<BN, NS>(0) + boff[j];
    auto ldc = [&](F& f, int s, int k) {
      if (k == 0 || k > 2 * TN) { const int i = k == 0 ? 0 : k - 2 * TN; f.a[i] = ld_b128(AROW ? ca[s] + i * 2048 : ca[0] + s * 8192 + i * 1024); }
      else {
        const int j = (k - 1) >> 1, u = (k - 1) & 1;
        f.b[j][u] = __builtin_amdgcn_ds_read_tr16_b64_v4i16((LAS s16x4*)((AROW ? cb[j & 1] + (j >> 1) * 128 : cb[j]) + (32 * s + 4 * u) * RS));
      }
    };
    auto subc = [&](const F& x, F& u, int s, bool adv) {
      const int ns = nxt(slot), da = (ns - slot) * 16384, db = bslot<BN, NS>(ns) - bslot<BN, NS>(slot);
#pragma unroll
      for (int m = 0; m < NM; ++m) {
        mm1(x, m);
        if (m < NL) ldc(u, s, m);
        else if (adv && m - NL < NA) asm volatile("v_add_u32 %0, %1, %0" : "+v"(ca[m - NL < NA ? m - NL : 0]) : "s"(da));
        else if (adv && m - NL - NA < NB) asm volatile("v_add_u32 %0, %1, %0" : "+v"(cb[m - NL - NA < NB ? m - NL - NA : 0]) : "s"(db));
        __builtin_amdgcn_sched_barrier(0);
      }
      if (adv) slot = ns;
    };
    static_assert(NL + NA + NB <= NM, "");
    F f0, f1;
#pragma unroll
    for (int k = 0; k < NL; ++k) ldc(f0, 0, k);
    if constexpr (OWN) {
      subc(f0, f1, 1, true);
      mmall(f1);
      __builtin_amdgcn_sched_barrier(0);
      TS(32);
      bar();
      TS(1);
#pragma unroll
      for (int k = 0; k < NL; ++k) ldc(f0, 0, k);
    }
#pragma unroll 1
    for (int t = OWN ? 1 : 0; t < NT - 1; ++t) {
      subc(f0, f1, 1, true);
      TS(32 + t);
      lbar();
      TS(t + 1);
      subc(f1, f0, 0, false);
    }
    subc(f0, f1, 1, false);
    lbar();
    TS(NT); TS_OUT(BN == 192 ? 0 : BN == 64 ? 1 : BN == 224 ? 2 : 3);
    mmall(f1);
  } else {
    F f0, f1, g0, g1;
    auto tile = [&](const F& x0, const F& x1, F& u0, F& u1, int t) {
      TS(32 + t);
      bar();
      TS(t + 1);
      slot = nxt(slot);
      sub(x0, u0, slot, 0);
      sub(x1, u1, slot, 1);
    };
    ldall(f0, 0, 0); ldall(f1, 0, 1);
    int t0 = 0;
    if constexpr (OWN) {
      tile(f0, f1, g0, g1, 0);
      tile(g0, g1, f0, f1, 1);
      mmall(f0); mmall(f1);
      __builtin_amdgcn_sched_barrier(0);
      TS(34);
      lbar();
      TS(3);
      slot = 3;
      ldall(g0, 3, 0); ldall(g1, 3, 1);
      tile(g0, g1, f0, f1, 3);
      t0 = 4;
    }
    if constexpr (!OWN) {
#pragma unroll 1
      for (int t = t0; t < NT - 2; t += 2) {
        tile(f0, f1, g0, g1, t);
        tile(g0, g1, f0, f1, t + 1);
      }
      tile(f0, f1, g0, g1, NT - 2);
      lbar();
      TS(NT); TS_OUT(BN == 192 ? 0 : BN == 64 ? 1 : BN == 224 ? 2 : 3);
      mmall(g0); mmall(g1);
    } else {
#pragma unroll 1
      for (int t = t0; t < 24; t += 2) {
        tile(f0, f1, g0, g1, t);
        if (t == 4) mid();
        tile(g0, g1, f0, f1, t + 1);
      }
      typedef std::integral_constant<int, TN> Full;
      typedef std::integral_constant<int, TN / 2> Half;
      auto tileh = [&](const F& x0, const F& x1, F& u0, F& u1, auto tx) {
        bar();
        slot = nxt(slot);
        subh(x0, u0, slot, 0, tx, Half{});
        subh(x1, u1, slot, 1, tx, Half{});
      };
#pragma unroll
      for (int j = 0; j < 2; ++j) {
        const int l2 = opaque(lane), k = 8 * (l2 >> 4) + ((l2 & 15) >> 2);
        boffh[j] = k * 128 + (((2 * nt(j) + ((l2 & 3) >> 1)) ^ bswz<64>(k)) * 16) + 8 * (l2 & 1);
      }
      tileh(f0, f1, g0, g1, Full{});
      mfma_drain();
#pragma unroll
      for (int i = 0; i < TM; ++i)
#pragma unroll
        for (int j = TN / 2; j < TN; ++j) asm volatile("" : "+v"(acc[i][j]));
      send(0);
      __builtin_amdgcn_sched_barrier(0);
      tileh(g0, g1, f0, f1, Half{});
      tileh(f0, f1, g0, g1, Half{});
      send(1);
      tileh(g0, g1, f0, f1, Half{});
      tileh(f0, f1, g0, g1, Half{});
      send(2);
      tileh(g0, g1, f0, f1, Half{});
      send(3);
      lbar();
      TS(28); TS_OUT(3);
#pragma unroll
      for (int m = 0; m < TM * TN / 2; ++m) mm1(f0, m, TN / 2);
#pragma unroll
      for (int m = 0; m < TM * TN / 2; ++m) mm1(f1, m, TN / 2);
    }
  }
  mfma_drain();
#pragma unroll
  for (int i = 0; i < TM; ++i)
#pragma unroll
    for (int j = 0; j < TN; ++j) asm volatile("" : "+v"(acc[i][j]));
}
DI void swap_pair(f32x4& x, f32x4& y) {
#pragma unroll
  for (int e = 0; e < 4; ++e) {
    auto r = __builtin_amdgcn_permlane16_swap(f2u(x[e]), f2u(y[e]), false, false);
    x[e] = u2f(r[0]); y[e] = u2f(r[1]);
  }
}
DI void add_bf8(f32x4& x, f32x4& y, const u32x4& rv) {
  x[0] += bflo(rv[0]); x[1] += bfhi(rv[0]); x[2] += bflo(rv[1]); x[3] += bfhi(rv[1]);
  y[0] += bflo(rv[2]); y[1] += bfhi(rv[2]); y[2] += bflo(rv[3]); y[3] += bfhi(rv[3]);
}
DI u32x4 pack_xy(const f32x4& x, const f32x4& y) { return (u32x4){pk2(x[0], x[1]), pk2(x[2], x[3]), pk2(y[0], y[1]), pk2(y[2], y[3])}; }

template <class Op> DI void red2(float& a, float& b, Op&& op) {
  auto r = __builtin_amdgcn_permlane16_swap(f2u(a), f2u(b), false, false);
  const float c = op(u2f(r[0]), u2f(r[1]));
  auto t = __builtin_amdgcn_permlane32_swap(f2u(c), f2u(c), false, false);
  const float d = op(u2f(t[0]), u2f(t[1]));
  auto u = __builtin_amdgcn_permlane16_swap(f2u(d), f2u(d), false, false);
  a = u2f(u[0]); b = u2f(u[1]);
}
DI void dual_softmax(const f32x4 (&p)[2][2], const float (&sc)[2], f32x4 (&e)[2][2][2]) {
  float mx[2], mn[2], s1[2], s2[2];
#pragma unroll
  for (int t = 0; t < 2; ++t) {
    mx[t] = p[t][0][0]; mn[t] = p[t][0][0];
#pragma unroll
    for (int r = 1; r < 8; ++r) { mx[t] = __builtin_fmaxf(mx[t], p[t][r >> 2][r & 3]); mn[t] = __builtin_fminf(mn[t], p[t][r >> 2][r & 3]); }
  }
  red2(mx[0], mx[1], [](float x, float y) { return __builtin_fmaxf(x, y); });
  red2(mn[0], mn[1], [](float x, float y) { return __builtin_fminf(x, y); });
#pragma unroll
  for (int t = 0; t < 2; ++t) {
    const float a = -mx[t] * sc[t], b = mn[t] * sc[t];
    s1[t] = 0.f; s2[t] = 0.f;
#pragma unroll
    for (int r = 0; r < 8; ++r) {
      const float x = __builtin_amdgcn_exp2f(__builtin_fmaf(p[t][r >> 2][r & 3], sc[t], a));
      const float y = __builtin_amdgcn_exp2f(__builtin_fmaf(p[t][r >> 2][r & 3], -sc[t], b));
      e[0][t][r >> 2][r & 3] = x; e[1][t][r >> 2][r & 3] = y;
      s1[t] += x; s2[t] += y;
    }
  }
  red2(s1[0], s1[1], [](float x, float y) { return x + y; });
  red2(s2[0], s2[1], [](float x, float y) { return x + y; });
#pragma unroll
  for (int t = 0; t < 2; ++t) {
    const float r1 = __builtin_amdgcn_rcpf(s1[t]), r2 = __builtin_amdgcn_rcpf(s2[t]);
#pragma unroll
    for (int r = 0; r < 8; ++r) { e[0][t][r >> 2][r & 3] *= r1; e[1][t][r >> 2][r & 3] *= r2; }
  }
}
DI bf16x8 pack_frag(const f32x4& x, const f32x4& y) { return __builtin_bit_cast(bf16x8, pack_xy(x, y)); }
DI void st4(LAS char* p, const f32x4& a, const f32x4& b, const f32x4& c, const f32x4& d) {
  *(LAS f32x4*)p = a; *(LAS f32x4*)(p + 1024) = b; *(LAS f32x4*)(p + 2048) = c; *(LAS f32x4*)(p + 3072) = d;
}
DI void ld4(LAS const char* p, f32x4 (&v)[2][2]) {
  v[0][0] = *(LAS const f32x4*)p; v[0][1] = *(LAS const f32x4*)(p + 1024); v[1][0] = *(LAS const f32x4*)(p + 2048); v[1][1] = *(LAS const f32x4*)(p + 3072);
}
#endif

__global__ __launch_bounds__(512, 2) void block_kernel(const u16* __restrict__ X, const u16* __restrict__ WQKV, const u16* __restrict__ WPHI,
                                                        const u16* __restrict__ WO, const u16* __restrict__ W1, const u16* __restrict__ W2,
                                                        u16* __restrict__ OUT, char* WS) {
#if defined(__HIP_DEVICE_COMPILE__)
  extern __shared__ __attribute__((aligned(16))) char smem_[];
  LAS char* smem = (LAS char*)smem_;
  unsigned long long tseg_a = 0, tseg_b = 0;
  const int tid = threadIdx.x, wid = __builtin_amdgcn_readfirstlane(tid >> 6);
  int lane = tid & 63;
  const int bid = blockIdx.x, xcd = bid & 7, loc = bid >> 3;
  const int tm = (xcd & 1) * 8 + (loc & 7), gp = (xcd >> 1) * 4 + (loc >> 3);
  const bool loader = wid >= 4;
  const int lw = wid & 3;
  GAS unsigned* ctl = (GAS unsigned*)WS;
  const rsrc_t rws = mkrsrc(WS, WS_END);
  if (wid == CPW) {
    unsigned long long pc;
    asm volatile("s_getpc_b64 %0" : "=s"(pc));
    touch_lines(smem, mkrsrc((const void*)(pc & ~127ull), CODE_AHEAD), loc * 2048, lane < 16 ? lane : 4096, 16);
  }
  if (wid == 0) STAMP(0, 0);
  unsigned epoch, tag;
  auto load_tag = [&]() { epoch = __hip_atomic_load(ctl + C_EPOCH * 64, __ATOMIC_RELAXED, __HIP_MEMORY_SCOPE_AGENT); };
  auto read_tag = [&]() { asm volatile("" : "+v"(epoch)); tag = __builtin_amdgcn_readfirstlane(epoch) + 1u; };

  int r16, g, cpair, cswz;
  auto relane = [&]() {
    lane = opaque(lane);
    r16 = lane & 15; g = lane >> 4;
    cpair = (g & 1) * 16 + (g >> 1) * 8;
    cswz = (cpair >> 3) * 256 + r16 * 16;
  };
  {
    constexpr int NT = 16, NS = 3;
    constexpr int L_PHI = 0;
    constexpr int L_XV = 2 * 17408;
    constexpr int L_VF = L_XV + 16384;
    constexpr int L_XQ = L_VF + 8192;
    constexpr int L_KVT = L_XQ;
    constexpr int L_WF = L_XQ + 16384;
    static_assert(L_WF + 4096 <= 5 * 16384, "phase 2's B ring is filled during this epilogue");
    relane();
    const int wm = (wid >> 1) & 1, hh = wid & 1, mi = wid >> 2;
    const int head = gp * 2 + hh;
    const unsigned kvp_grp = WS_KVP + gp * 16 * KVP_BYTES;
    LAS char* const xk = smem + L_PHI + hh * 17408 + (wm * 64 + 32) * 136 + lane * 16;
    bf16x8 wf[2];
    f32x4 fq[2][2], fk[2][2], fv[2][2];
    if (loader) {
      const rsrc_t rb = mkrsrc(WQKV, D_ * 3 * D_ * 2), ro = mkrsrc(WO, D_ * D_ * 2);
      Loader<192, NS, NT, true> L;
      const rsrc_t rx = mkrsrc(X, S_ * D_ * 2);
      L.init(smem, rx, rb, tm * 128 * D_ * 2, D_ * 2, gp * 128, 3 * D_ * 2, 2048, lw, lane, (tm & 3) * 4);
      L.issueA(0); L.issueB(0); L.issueA(1); L.issueB(1);
      if (lw == 0) *(LAS volatile unsigned*)(smem + L_GO) = 0u;
      load_tag();
      __builtin_amdgcn_s_setprio(3);
      auto cs1 = [&](int t) { if ((tm >> 2) == t) colsum_tile<192>(smem + NS * 16384 + (t % NS) * 24576, rws, WS_CS1 + (gp * 16 + tm) * 768, lw, lane); };
      L.step();
      cs1(0);
      const int xrow = tm * 128 + gp * 8 + lw * 2 + (lane >> 5);
      u32x4 xv[4];
#pragma unroll
      for (int j = 0; j < 4; ++j) xv[j] = __builtin_amdgcn_raw_buffer_load_b128(rx, xrow * (D_ * 2) + (lane & 31) * 16, j * 512, 0);
      L.step<14>();
      cs1(1);
      L.step<14>();
      cs1(2);
      L.step();
      cs1(3);
      L.step();
      read_tag();
      asm volatile("" : "+v"(xv[0]), "+v"(xv[1]), "+v"(xv[2]), "+v"(xv[3]));
      float sum = 0.f, qq = 0.f;
#pragma unroll
      for (int i = 0; i < 16; ++i) sum += bflo(xv[i >> 2][i & 3]) + bfhi(xv[i >> 2][i & 3]);
      float mean = half_sum(sum) * (1.0f / D_);
      asm volatile("" : "+v"(mean));
      L.step();
      asm volatile("" : "+v"(xv[0]), "+v"(xv[1]), "+v"(xv[2]), "+v"(xv[3]));
#pragma unroll
      for (int i = 0; i < 16; ++i) {
        const float d0 = bflo(xv[i >> 2][i & 3]) - mean, d1 = bfhi(xv[i >> 2][i & 3]) - mean;
        qq = __builtin_fmaf(d0, d0, __builtin_fmaf(d1, d1, qq));
      }
      const float rstd = __builtin_amdgcn_rsqf(half_sum(qq) * (1.0f / D_) + LN_EPS);
      __builtin_amdgcn_raw_buffer_store_b64(__builtin_bit_cast(u32x2, (f32x2){mean, rstd}), rws, (lane & 31) == 0 ? WS_XST + xrow * 8 : 0x80000000u, 0, SC1);
      L.step();
      L.step<0>();
      if (lane == 0) { set_flag(ctl, C_CS1 + gp, tm * 4 + lw, tag); set_flag(ctl, C_ST + tm, gp * 4 + lw, tag); }
      touch_lines(smem, ro, bid * 8192, lane < 16 ? lw * 16 + lane : 4096, 64);
      L.step();
      L.step<13>();
      L.step();
      const unsigned seen = peek(ctl, C_CS1 + gp, lane), seen2 = peek(ctl, C_ST + tm, lane);
      L.steps(2);
      confirm(seen, ctl, C_CS1 + gp, tag, lane);
      confirm(seen2, ctl, C_ST + tm, tag, lane);
      u32x4 gt[4];
#pragma unroll
      for (int j = 0; j < 4; ++j) gt[j] = ld128(rws, WS_CS1 + (gp * 16 + lw * 4) * 768 + lane * 16, j * 768);
      u32x4 xs = ld128(rws, WS_XST + tm * 1024 + lane * 16, 0);
      L.step();
      wait_vm<10>(); lbar();
      *(LAS f32x4*)(smem + L_CSS + lw * 1024 + lane * 16) = sum4(gt);
      asm volatile("" : "+v"(xs));
      if (lw == 1) *(LAS u32x4*)(smem + L_RST + lane * 16) = xs;
      wait_vm<0>(); lbar();
      if (lw == 0) *(LAS f32x4*)(smem + L_CS + lane * 16) = lds_sum4(smem + L_CSS + lane * 16);
      const rsrc_t r1 = mkrsrc(W1, D_ * FF_ * 2);
      load_wtile<224, 0, 4>(smem + L_WT, r1, tm * 64 * (FF_ * 2) + gp * 448, FF_ * 2, 128, lw, lane);
      lbar();
      bar();
      __builtin_amdgcn_s_setprio(1);
      ld4(xk, fk);
      wf[0] = ld_b128(smem + L_WF + hh * 2048 + lane * 16);
      wf[1] = ld_b128(smem + L_WF + hh * 2048 + 1024 + lane * 16);
      ld4(smem + L_XV + lw * 4096 + lane * 16, fv);
    } else {
      {
#pragma unroll
        for (int t = 0; t < 2; ++t) {
          s16x8 w;
#pragma unroll
          for (int j = 0; j < 8; ++j) w[j] = (short)WPHI[head * 1024 + (16 * (j >> 2) + 4 * g + (j & 3)) * 32 + 16 * t + r16];
          wf[t] = __builtin_bit_cast(bf16x8, w);
        }
        load_tag();
      }
      if (wid == 0) STAMP(4, 0);
      f32x4 acc[4][6];
      mma_loop<192, NS, NT, 4, 6, false, false>(smem, acc, wm * 64, lane, [&](int j) { return (j >> 1) * 4 + hh * 2 + (j & 1); }, []() {}, [](int) {});
      if (wid == 0) STAMP(0, 2);
      read_tag();
      if (wm == 0) {
        *(LAS bf16x8*)(smem + L_WF + hh * 2048 + lane * 16) = wf[0];
        *(LAS bf16x8*)(smem + L_WF + hh * 2048 + 1024 + lane * 16) = wf[1];
      }
      st4(xk, acc[2][2], acc[2][3], acc[3][2], acc[3][3]);
      st4(smem + L_XV + wid * 4096 + lane * 16, acc[2][4], acc[2][5], acc[3][4], acc[3][5]);
      lbar();
      if (wid == 0) STAMP(4, 4);
      st4(smem + L_XQ + wid * 4096 + lane * 16, acc[2][0], acc[2][1], acc[3][0], acc[3][1]);
#pragma unroll
      for (int t = 0; t < 2; ++t)
#pragma unroll
        for (int u = 0; u < 2; ++u) { fq[t][u] = acc[t][u]; fk[t][u] = acc[t][2 + u]; fv[t][u] = acc[t][4 + u]; }
    }
    const int rbase = wm * 64 + mi * 32, q = r16 >> 2, p = lane & 3;
    LAS const float* cs = (LAS const float*)(smem + L_CS) + hh * 32;
    LAS const f32x2* rst = (LAS const f32x2*)(smem + L_RST) + rbase;
    auto feature = [&](const f32x4 (&x)[2][2], int part, f32x4 (&e)[2][2][2]) {
      const f32x4 c0 = *(LAS const f32x4*)(cs + part * 64 + 4 * g), c1 = *(LAS const f32x4*)(cs + part * 64 + 16 + 4 * g);
      f32x4 pp[2][2];
      float sc[2];
#pragma unroll
      for (int t = 0; t < 2; ++t) {
        const f32x2 ms = rst[t * 16 + r16];
        sc[t] = ms[1] * 1.4426950408889634f;
        f32x4 y0, y1;
#pragma unroll
        for (int e_ = 0; e_ < 4; ++e_) { y0[e_] = __builtin_fmaf(-ms[0], c0[e_], x[t][0][e_]); y1[e_] = __builtin_fmaf(-ms[0], c1[e_], x[t][1][e_]); }
        const bf16x8 yb = pack_frag(y0, y1);
        pp[t][0] = MFMA16(wf[0], yb, ((f32x4){0.f, 0.f, 0.f, 0.f}));
        pp[t][1] = MFMA16(wf[1], yb, ((f32x4){0.f, 0.f, 0.f, 0.f}));
      }
      dual_softmax(pp, sc, e);
    };
    if (wid == 0) STAMP(4, 5);
    {
      const float cv0 = cs[128 + r16], cv1 = cs[144 + r16];
#pragma unroll
      for (int t = 0; t < 2; ++t) {
        const f32x4 s01 = *(LAS const f32x4*)(rst + t * 16 + 4 * g), s23 = *(LAS const f32x4*)(rst + t * 16 + 4 * g + 2);
        const float mv[4] = {s01[0], s01[2], s23[0], s23[2]}, rv[4] = {s01[1], s01[3], s23[1], s23[3]};
#pragma unroll
        for (int e_ = 0; e_ < 4; ++e_) {
          fv[t][0][e_] = __builtin_fmaf(-mv[e_], cv0, fv[t][0][e_]) * rv[e_];
          fv[t][1][e_] = __builtin_fmaf(-mv[e_], cv1, fv[t][1][e_]) * rv[e_];
        }
      }
      LAS char* vd = smem + (loader ? L_XV + lw * 4096 : L_VF + lw * 2048) + lane * 16;
      *(LAS bf16x8*)vd = pack_frag(fv[0][0], fv[1][0]);
      *(LAS bf16x8*)(vd + 1024) = pack_frag(fv[0][1], fv[1][1]);
      f32x4 e[2][2][2];
      feature(fk, 1, e);
      LAS char* wr = smem + L_PHI + hh * 17408 + (rbase + r16) * 136 + g * 8;
#pragma unroll
      for (int w = 0; w < 2; ++w)
#pragma unroll
        for (int t = 0; t < 2; ++t)
#pragma unroll
          for (int u = 0; u < 2; ++u)
            *(LAS u32x2*)(wr + t * 16 * 136 + w * 64 + u * 32) = (u32x2){pk2(e[w][t][u][0], e[w][t][u][1]), pk2(e[w][t][u][2], e[w][t][u][3])};
    }
    if (wid == 0) STAMP(4, 7);
    lbar();
    __builtin_amdgcn_s_setprio(0);
    if (wid == 0) STAMP(4, 8);
    bf16x8 pf[2][2];
    auto qpath = [&]() {
      f32x4 e[2][2][2];
      feature(fq, 0, e);
#pragma unroll
      for (int t = 0; t < 2; ++t)
#pragma unroll
        for (int w = 0; w < 2; ++w) { pf[t][w] = pack_frag(e[w][t][0], e[w][t][1]); asm volatile("" : "+v"(pf[t][w])); }
    };
    if (loader) {
      ld4(smem + L_XQ + lw * 4096 + lane * 16, fq);
      qpath();
      load_wtile<224, 4, 7>(smem + L_WT, mkrsrc(W1, D_ * FF_ * 2), tm * 64 * (FF_ * 2) + gp * 448, FF_ * 2, 128, lw, lane);
    } else {
      f32x4 k00 = {0.f, 0.f, 0.f, 0.f}, k01 = k00, k10 = k00, k11 = k00, z0 = k00, z1 = k00;
      const s16x8 one_ = {0x3f80, 0x3f80, 0x3f80, 0x3f80, 0x3f80, 0x3f80, 0x3f80, 0x3f80};
      LAS const char* rd = smem + L_PHI + hh * 17408 + (4 * g + q) * 136 + wm * 64 + p * 8;
#pragma unroll
      for (int u = 0; u < 4; ++u) {
        const bf16x8 pa0 = ld_tr2(rd + 32 * u * 136, rd + (32 * u + 16) * 136), pa1 = ld_tr2(rd + 32 * u * 136 + 32, rd + (32 * u + 16) * 136 + 32);
        LAS const char* vp = smem + ((u & 1) ? L_XV + ((u >> 1) * 2 + hh) * 4096 : L_VF + ((u >> 1) * 2 + hh) * 2048) + lane * 16;
        const bf16x8 v0 = ld_b128(vp), v1 = ld_b128(vp + 1024);
        k00 = MFMA16(pa0, v0, k00); k10 = MFMA16(pa1, v0, k10);
        k01 = MFMA16(pa0, v1, k01); k11 = MFMA16(pa1, v1, k11);
        z0 = MFMA16(pa0, __builtin_bit_cast(bf16x8, one_), z0); z1 = MFMA16(pa1, __builtin_bit_cast(bf16x8, one_), z1);
      }
      const int kb = kvp_grp + tm * KVP_BYTES;
      st128(pack_xy(k00, k10), rws, kb + hh * 4096 + wm * 2048 + lane * 16);
      st128(pack_xy(k01, k11), rws, kb + hh * 4096 + wm * 2048 + 1024 + lane * 16);
      st128(__builtin_bit_cast(u32x4, r16 == 0 ? z0 : z1), rws, r16 < 2 ? kb + 8192 + hh * 256 + wm * 128 + g * 32 + r16 * 16 : (int)0x80000000);
      if (wid == 0) STAMP(0, 3);
      wait_vm<0>();
    }
    lbar();
    if (tid == 0) set_flag(ctl, C_KV + gp, tm, tag);
    if (wid == 0) STAMP(0, 4);
    if (loader) {
      load_wo<0, WO1>(smem, mkrsrc(WO, D_ * D_ * 2), gp, lw, lane);
    } else {
      qpath();
    }
    FastPoll fpk = {0u, 0u, 0u, 0u};
    if (wid == 0) {
      STAMP(0, 5);
      fpk.run(ctl, C_KV + gp, 0, 16, tag, lane);
    }
    bar();
    if (wid == 0) STAMP(0, 6);
    {
      u32x4 tz[16];
      if (wid == 7) {
#pragma unroll
        for (int j = 0; j < 16; ++j) tz[j] = ld128(rws, lane < 32 ? kvp_grp + 8192 + lane * 16 : 0x80000000u, j * KVP_BYTES);
      }
      u32x4 t[16];
#pragma unroll
      for (int j = 0; j < 16; ++j) t[j] = ld128(rws, kvp_grp + tid * 16, j * KVP_BYTES);
      if (wid == 7) {
        f32x4 z = __builtin_bit_cast(f32x4, tz[0]);
#pragma unroll
        for (int j = 1; j < 16; ++j) z += __builtin_bit_cast(f32x4, tz[j]);
        if (lane < 32) *(LAS u32x2*)(smem + L_KVT + 8192 + lane * 8) = (u32x2){pk2(z[0], z[1]), pk2(z[2], z[3])};
      }
      f32x4 x = {0.f, 0.f, 0.f, 0.f}, y = x;
#pragma unroll
      for (int j = 0; j < 16; ++j) add_bf8(x, y, t[j]);
      *(LAS u32x4*)(smem + L_KVT + tid * 16) = pack_xy(x, y);
    }
    fpk.keep();
    lbar();
    if (wid == 0) STAMP(0, 7);
    {
      bf16x8 ka[2][2], za[2];
#pragma unroll
      for (int w = 0; w < 2; ++w) {
        ka[w][0] = ld_b128(smem + L_KVT + hh * 4096 + w * 2048 + lane * 16);
        ka[w][1] = ld_b128(smem + L_KVT + hh * 4096 + w * 2048 + 1024 + lane * 16);
        za[w] = ld_b128(smem + L_KVT + 8192 + hh * 128 + w * 64 + g * 16);
      }
#pragma unroll
      for (int t = 0; t < 2; ++t) {
        f32x4 o0 = {0.f, 0.f, 0.f, 0.f}, o1 = o0, dn = o0;
#pragma unroll
        for (int w = 0; w < 2; ++w) { dn = MFMA16(za[w], pf[t][w], dn); o0 = MFMA16(ka[w][0], pf[t][w], o0); o1 = MFMA16(ka[w][1], pf[t][w], o1); }
        const float rd = __builtin_amdgcn_rcpf(dn[0] + 1e-6f);
#pragma unroll
        for (int e_ = 0; e_ < 4; ++e_) { o0[e_] *= rd; o1[e_] *= rd; }
        swap_pair(o0, o1);
        st128(pack_xy(o0, o1), rws, WS_O + (tm * 32 + head) * 8192 + (rbase + t * 16) * 64 + cswz);
      }
    }
    wait_vm<0>();
    lbar();
    if (tid == 0) set_flag(ctl, C_O + tm, gp, tag);
    if (wid == 0) STAMP(0, 8);
  }

  relane();
  {
    FastPoll fp = {0u, 0u, 0u, 0u};
    if (loader) {
      load_wo<WO1, 16>(smem, mkrsrc(WO, D_ * D_ * 2), gp, lw, lane);
      colsum_tile<224>(smem + L_WT, rws, WS_CS3 + (gp * 16 + tm) * 896, lw, lane);
    }
    if (wid == 0) {
      fp.run(ctl, C_O + tm, 0, 16, tag, lane);
      STAMP(1, 0);
      if (bid == 0 && lane == 0) __hip_atomic_store(ctl + C_EPOCH * 64, tag, __ATOMIC_RELAXED, __HIP_MEMORY_SCOPE_AGENT);
    }
    if (loader) {
      wait_vm<0>();
      bar();
      if (lane == 0) set_flag(ctl, C_CS3 + gp, tm * 4 + lw, tag);
    } else {
      const int row = tm * 128 + wid * 32 + r16, col = gp * 64 + cpair;
      u32x4 res[2][2];
#pragma unroll
      for (int i = 0; i < 2; ++i)
#pragma unroll
        for (int j = 0; j < 2; ++j) res[i][j] = *(const u32x4*)(X + (size_t)(row + i * 16) * D_ + col + j * 32);
      f32x4 acc[2][4];
#pragma unroll
      for (int j = 0; j < 8; ++j) { acc[j >> 2][j & 3] = (f32x4){0.f, 0.f, 0.f, 0.f}; asm volatile("" : "+v"(acc[j >> 2][j & 3])); }
      constexpr int PF = PFD, BD = BDEPTH;
      const int q = r16 >> 2, p = lane & 3;
      int bo[4];
#pragma unroll
      for (int j = 0; j < 4; ++j) bo[j] = (8 * g + q) * 128 + (((2 * j + (p >> 1)) ^ bswz<64>(8 * g + q)) * 16) + 8 * (p & 1);
      const int ao = WS_O + tm * 262144 + wid * 2048 + lane * 16;
      u32x4 af[PF][2];
      s16x4 bf[BD][4][2];
      auto ldb = [&](int sl, int k) {
        bf[sl % BD][k >> 1][k & 1] = __builtin_amdgcn_ds_read_tr16_b64_v4i16((LAS s16x4*)(smem + wo_slot(sl >> 1) + bo[k >> 1] + (32 * (sl & 1) + 4 * (k & 1)) * 128));
      };
      bar();
      if (wid == 0) STAMP(1, 1);
#pragma unroll
      for (int sl = 0; sl < PF; ++sl) { af[sl][0] = ld128(rws, ao, sl * 8192); af[sl][1] = ld128(rws, ao + 1024, sl * 8192); }
#pragma unroll
      for (int k = 0; k < 8 * (BD - 1); ++k) ldb(k >> 3, k & 7);
      __builtin_amdgcn_sched_barrier(0);
#pragma unroll
      for (int sl = 0; sl < 32; ++sl) {
#pragma unroll
        for (int m = 0; m < 8; ++m) {
          const int i = m >> 2, j = m & 3;
          mfma16(acc[i][j], __builtin_bit_cast(bf16x8, __builtin_shufflevector(bf[sl % BD][j][0], bf[sl % BD][j][1], 0, 1, 2, 3, 4, 5, 6, 7)),
                 __builtin_bit_cast(bf16x8, af[sl % PF][i]));
          if (sl + BD - 1 < 32) ldb(sl + BD - 1, m);
          if (j == 3 && sl + PF < 32) af[sl % PF][i] = ld128(rws, ao + i * 1024, (sl + PF) * 8192);
          __builtin_amdgcn_sched_barrier(0);
        }
        if (sl == 30) lbar();
      }
      fp.keep();
      mfma_drain();
#pragma unroll
      for (int j = 0; j < 8; ++j) asm volatile("" : "+v"(acc[j >> 2][j & 3]));
      if (wid == 0) STAMP(1, 2);
#pragma unroll
      for (int i = 0; i < 2; ++i) {
        float s = 0.f, qq = 0.f;
#pragma unroll
        for (int j = 0; j < 2; ++j) {
          f32x4 x = acc[i][2 * j], y = acc[i][2 * j + 1];
          swap_pair(x, y);
          add_bf8(x, y, res[i][j]);
          const u32x4 o = pack_xy(x, y);
          st128(o, rws, WS_H + (tm * 32 + gp * 2 + j) * 8192 + (wid * 32 + i * 16) * 64 + cswz);
          *(LAS u32x4*)(smem + j * 8192 + (wid * 32 + i * 16) * 64 + cswz) = o;
#pragma unroll
          for (int e = 0; e < 4; ++e) { const float lo = bflo(o[e]), hi = bfhi(o[e]); s += lo + hi; qq = __builtin_fmaf(lo, lo, __builtin_fmaf(hi, hi, qq)); }
        }
        red2(s, qq, [](float x, float y) { return x + y; });
        __builtin_amdgcn_raw_buffer_store_b64(__builtin_bit_cast(u32x2, (f32x2){s, qq}), rws,
                                              g == 0 ? WS_HST + ((tm * 16 + gp) * 128 + wid * 32 + i * 16 + r16) * 8 : 0x80000000u, 0, SC1);
      }
      if (wid == 0) STAMP(1, 3);
    }
  }

  const int kh = gp >> 3;
  if (loader) {
    __builtin_amdgcn_s_setprio(3);
    relane();
    const rsrc_t r2 = mkrsrc(W2, FF_ * D_ * 2);
    FastPoll fp;
    {
      Loader<224, 3, 16, false> L;
      L.init(smem, rws, mkrsrc(W1, D_ * FF_ * 2), WS_H + tm * 128 * D_ * 2, 0, gp * 448, FF_ * 2, 128, lw, lane, gp);
      __builtin_amdgcn_s_sleep(B3DELAY);
      L.issueB(0);
      bar();
      bar();
      if (wid == 4 && lane == 0) set_flag(ctl, C_H + tm, gp, tag);
      wait_vm<0>(); bar();
      L.issueB(1); L.issueB(2);
      poll4(fp, smem, 2, lw, ctl, C_H + tm, 0, 16, tag, lane);
      L.issueA(1); L.issueA(2);
      u32x4 gt[4], hs[4];
#pragma unroll
      for (int j = 0; j < 4; ++j) hs[j] = ld128(rws, WS_HST + tm * 16384 + lw * 4096 + lane * 16, j * 1024);
      const unsigned seen = peek(ctl, C_CS3 + gp, lane);
      L.tn = 3;
      L.step<4 + 5>();
      fp.keep();
      L.step<5 + 11>();
      confirm(seen, ctl, C_CS3 + gp, tag, lane);
#pragma unroll
      for (int j = 0; j < 4; ++j) gt[j] = ld128(rws, WS_CS3 + (gp * 16 + lw * 4) * 896 + lane * 16, j * 896);
      *(LAS f32x4*)(smem + L_CSS + 4096 + lw * 1024 + lane * 16) = sum4(hs);
      L.step();
      if (lw == 1) {
        const f32x4 sum = lds_sum4(smem + L_CSS + 4096 + lane * 16);
        const float m0 = sum[0] * (1.0f / D_), m1 = sum[2] * (1.0f / D_);
        const float v0 = __builtin_fmaxf(sum[1] * (1.0f / D_) - m0 * m0, 0.f), v1 = __builtin_fmaxf(sum[3] * (1.0f / D_) - m1 * m1, 0.f);
        *(LAS f32x4*)(smem + L_RST + lane * 16) = (f32x4){m0, __builtin_amdgcn_rsqf(v0 + LN_EPS), m1, __builtin_amdgcn_rsqf(v1 + LN_EPS)};
      }
      L.step();
      *(LAS f32x4*)(smem + L_CSS + lw * 1024 + lane * 16) = sum4(gt);
      L.step();
      if (lw == 0) *(LAS f32x4*)(smem + L_CS + lane * 16) = lds_sum4(smem + L_CSS + lane * 16);
      const int stag = (loc & 7) < 6 ? (loc & 7) : 6;
      L.steps(stag);
      touch_lines(smem, r2, bid * 28672, lane < 56 ? lw * 56 + lane : 4096, 224);
      L.step();
      L.step<12>();
      L.steps(6 - stag);
    }
    {
      relane();
      Loader<128, 4, 31, false, 1> L;
      L.kh = kh;
      L.init(smem, rws, r2, WS_H1 + tm * 112 * 8192, 0, gp * 128, D_ * 2, kh ? -1024 : 1024, lw, lane, gp & 7);
      L.issueB(0);
      wait_vm<11 + 4>(); lbar(); wait_vm<4>(); bar();
      bar();
      L.issueB(1); L.issueB(2); L.issueB(3);
      wait_vm<12>(); bar();
      if (wid == 4 && lane == 0) set_flag(ctl, C_H1 + tm, gp, tag);
      wait_vm<8>(); bar();
      wait_vm<4>(); bar();
      L.issueB(4);
      poll4(fp, smem, 3, lw, ctl, C_H1 + tm, kh * 8, 8, tag, lane);
      L.issueA(3); L.issueA(4);
      L.tn = 5;
      L.step<4>();
      fp.keep();
      L.steps(3);
      if (wid == 4 && lane == 0) set_flag(ctl, C_H1B + tm, gp, tag);
      L.steps(5);
      const unsigned seen = peek(ctl, C_H1B + tm, lane, 16);
      L.steps(3);
      confirm(seen, ctl, C_H1B + tm, tag, lane, 16);
      L.steps(8);
      L.init_half(gp * 128, D_ * 2, lw, lane);
      wait_vm<8>(); bar(); L.issueH(25);
      wait_vm<6>(); bar(); L.issueH(26);
#pragma unroll 1
      for (int t = 27; t <= 30; ++t) { wait_vm<6>(); bar(); L.issueH(t); }
      wait_vm<6>(); bar();
      wait_vm<0>(); bar();
      bar();
    }
  } else {
    const int wm = wid >> 1, wn = wid & 1;
    relane();
    wait_vm<0>();
    bar();
    {
      const int par = gp & 1;
      auto slab3 = [&](int a) { return wn * 4 + (a + par == 3 ? 0 : a + par); };
      auto ntile3 = [&](int j) { return j < 6 ? slab3(j >> 1) * 2 + (j & 1) : 6 + wn; };
      f32x4 acc[4][7];
      mma_loop<224, 3, 16, 4, 7, false, true>(smem, acc, wm * 64, lane, ntile3, []() {}, [](int) {});
      if (wid == 0) STAMP(2, 2);
      relane();
      f32x4 cs[7];
#pragma unroll
      for (int j = 0; j < 7; ++j) cs[j] = *(LAS const f32x4*)(smem + L_CS + (ntile3(j) * 16 + g * 4) * 4);
      float fa[4], fb[4];
#pragma unroll
      for (int i = 0; i < 4; ++i) {
        const f32x2 ms = *(LAS const f32x2*)(smem + L_RST + (wm * 64 + i * 16 + r16) * 8);
        fa[i] = ms[1]; fb[i] = -ms[0] * ms[1];
      }
      auto act = [&](int i, int j) {
        f32x4 y;
#pragma unroll
        for (int e = 0; e < 4; ++e) y[e] = __builtin_fmaxf(__builtin_fmaf(acc[i][j][e], fa[i], fb[i] * cs[j][e]), 0.f);
        return y;
      };
      const int hb = WS_H1 + (tm * 112 + gp * 7) * 8192;
#pragma unroll
      for (int a = 0; a < 3; ++a) {
        const int sl = slab3(a);
#pragma unroll
        for (int i = 0; i < 4; ++i) {
          f32x4 x = act(i, 2 * a), y = act(i, 2 * a + 1);
          swap_pair(x, y);
          const u32x4 o = pack_xy(x, y);
          const int ro = (wm * 64 + i * 16) * 64 + cswz;
          st128(o, rws, hb + sl * 8192 + ro);
          if (a < 2 || (unsigned)(sl - par) < 6u) *(LAS u32x4*)(smem + (sl - par) * 8192 + ro) = o;
        }
        asm volatile("" ::: "memory");
      }
#pragma unroll
      for (int i = 1; i < 4; i += 2) {
        f32x4 x = act(i - 1, 6), y = act(i, 6);
        swap_pair(x, y);
        const u32x4 o = pack_xy(x, y);
        const int r3 = (wm * 64 + (i - 1 + (g & 1)) * 16) * 64 + (2 * wn + (g >> 1)) * 256 + r16 * 16;
        st128(o, rws, hb + 3 * 8192 + r3);
        *(LAS u32x4*)(smem + (3 - par) * 8192 + r3) = o;
      }
      if (wid == 0) STAMP(2, 5);
      wait_vm<6>();
      if (wid == 0) STAMP(2, 3);
    }
    relane();
    {
      const int row = tm * 128 + wm * 64 + r16, col = gp * 64 + wn * 32 + cpair;
      u32x4 res[4], t[4];
      unsigned seen = 0;
      f32x4 acc[4][4];
      mma_loop<128, 4, 31, 4, 4, true, true>(smem, acc, wm * 64, lane, [&](int j) { return (j >> 1) * 4 + wn * 2 + (j & 1); }, [&]() {
        wait_vm<0>();
        if (wid == 0) STAMP(2, 4);
#pragma unroll
        for (int i = 0; i < 4; ++i) res[i] = ld128(rws, WS_H + (tm * 32 + gp * 2 + wn) * 8192 + (wm * 64 + i * 16) * 64 + cswz, 0);
      }, [&](int what) {
        if (what == 0) {
          const int pb = WS_P4 + (tm * 16 + gp) * 16384 + wid * 4096 + lane * 16;
#pragma unroll
          for (int i = 0; i < 4; ++i) st128(pack_xy(acc[i][2], acc[i][3]), rws, pb + i * 1024);
        } else if (what == 1) {
          wait_vm<0>();
          if (lane == 0) set_flag(ctl, C_P4 + tm, gp * 4 + wid, tag);
          if (wid == 0) STAMP(3, 3);
        } else if (what == 2) {
          seen = __hip_atomic_load(ctl + (C_P4 + tm) * 64 + (gp ^ 8) * 4 + wid, __ATOMIC_RELAXED, __HIP_MEMORY_SCOPE_AGENT);
        } else {
          asm volatile("" : "+v"(seen));
          if (!wave_all(seen == tag)) poll(ctl, C_P4 + tm, (gp ^ 8) * 4 + wid, 1, tag, lane);
          acquire();
          if (wid == 0) STAMP(3, 4);
          const int qb = WS_P4 + (tm * 16 + (gp ^ 8)) * 16384 + wid * 4096 + lane * 16;
#pragma unroll
          for (int i = 0; i < 4; ++i) t[i] = ld128(rws, qb + i * 1024, 0);
        }
      });
      if (wid == 0) STAMP(3, 2);
#pragma unroll
      for (int i = 0; i < 4; ++i) {
        f32x4 x = acc[i][0], y = acc[i][1];
        add_bf8(x, y, t[i]);
        swap_pair(x, y);
        add_bf8(x, y, res[i]);
        *(u32x4*)(OUT + (size_t)(row + i * 16) * D_ + col) = pack_xy(x, y);
      }
      if (wid == 0) {
        STAMP(3, 5);
        const unsigned long long t0 = __builtin_amdgcn_s_memrealtime(), dt = tseg_b - tseg_a;
        while (__builtin_amdgcn_s_memrealtime() - t0 < dt && dt < 20000) __builtin_amdgcn_s_sleep(1);
      }
    }
  }
#endif
}
#if defined(__HIP_DEVICE_COMPILE__)
#pragma clang attribute pop
#endif

extern "C" void kernel_launch(void* const* d_in, const int* in_sizes, int n_in, void* d_out, int out_size, void* d_ws, size_t ws_size,
                              hipStream_t stream) {
  (void)hipFuncSetAttribute((const void*)block_kernel, hipFuncAttributeMaxDynamicSharedMemorySize, LDS_BYTES);
  block_kernel<<<256, 512, LDS_BYTES, stream>>>((const u16*)d_in[0], (const u16*)d_in[1], (const u16*)d_in[2], (const u16*)d_in[3],
                                                (const u16*)d_in[5], (const u16*)d_in[6], (u16*)d_out, (char*)d_ws);
}
```

```cpp
#include <hip/hip_runtime.h>
#include <hip/hip_bf16.h>
#include <type_traits>

#define LAS __attribute__((address_space(3)))
#define GAS __attribute__((address_space(1)))
#define DI __device__ __forceinline__
typedef __bf16 bf16x8 __attribute__((ext_vector_type(8)));
typedef __bf16 bf16x2 __attribute__((ext_vector_type(2)));
typedef short s16x4 __attribute__((ext_vector_type(4)));
typedef short s16x8 __attribute__((ext_vector_type(8)));
typedef float f32x2 __attribute__((ext_vector_type(2)));
typedef float f32x4 __attribute__((ext_vector_type(4)));
typedef unsigned u32x2 __attribute__((ext_vector_type(2)));
typedef unsigned u32x4 __attribute__((ext_vector_type(4)));
typedef unsigned short u16;

constexpr int S_ = 2048, D_ = 1024, FF_ = 3584;
constexpr float LN_EPS = 1e-5f;

constexpr int C_EPOCH = 0, C_TMO = 1, C_ST = 2, C_CS1 = 18, C_CS3 = 34, C_KV = 50, C_O = 66, C_H = 82, C_H1 = 98, C_P4 = 114, C_H1B = 130, C_LINES = 146;
constexpr unsigned WS_XST = 40960;
constexpr unsigned WS_CS1 = WS_XST + S_ * 8;
constexpr unsigned WS_CS3 = WS_CS1 + 16 * 16 * 192 * 4;
constexpr unsigned WS_HST = WS_CS3 + 16 * 16 * 224 * 4;
constexpr unsigned KVP_BYTES = 8192 + 512;
constexpr unsigned WS_KVP = WS_HST + S_ * 16 * 8;
constexpr unsigned WS_O = WS_KVP + 256 * KVP_BYTES;
constexpr unsigned WS_H = WS_O + S_ * D_ * 2;
constexpr unsigned WS_H1 = WS_H + S_ * D_ * 2;
constexpr unsigned WS_P4 = WS_O, WS_END = WS_H1 + S_ * FF_ * 2;
static_assert(C_LINES * 256 <= (int)WS_XST && WS_O % 256 == 0 && WS_KVP % 256 == 0, "");

constexpr int L_WT = 120 * 1024;
constexpr int L_TAB = 149 * 1024;
constexpr int L_DUMMY = L_TAB;
constexpr int L_RST = L_TAB + 256;
constexpr int L_CS = L_RST + 1024;
constexpr int L_CSS = L_CS + 1024;
constexpr int L_GO = L_CSS + 8192;
constexpr int LDS_BYTES = L_GO + 16;
static_assert(LDS_BYTES <= 160 * 1024, "");

#if defined(__HIP_DEVICE_COMPILE__)
#pragma clang attribute push(__attribute__((target("no-packed-fp32-ops"))), apply_to = function)
typedef __amdgpu_buffer_rsrc_t rsrc_t;
DI float u2f(unsigned u) { return __builtin_bit_cast(float, u); }
DI unsigned f2u(float f) { return __builtin_bit_cast(unsigned, f); }
DI bool wave_all(bool p) { return __builtin_amdgcn_ballot_w64(p) == ~0ull; }
#define CODE_AHEAD 52224
#define PSLEEP "2"
#define WO1 9
#define BDEPTH 3
#define PFD 8
#define B3DELAY 48
#define CPW 0
constexpr int SC1 = 16;
template <int N> DI void wait_vm() { asm volatile("s_waitcnt vmcnt(%0)" ::"n"(N) : "memory"); }
DI void wait_lgkm0() { asm volatile("" ::: "memory"); __builtin_amdgcn_s_waitcnt(0xC07F); asm volatile("" ::: "memory"); }
DI void bar() { asm volatile("" ::: "memory"); __builtin_amdgcn_s_barrier(); asm volatile("" ::: "memory"); }
DI void lbar() { wait_lgkm0(); bar(); }
DI unsigned pk2(float a, float b) { f32x2 v = {a, b}; return __builtin_bit_cast(unsigned, __builtin_convertvector(v, bf16x2)); }
DI float bflo(unsigned u) { return u2f(u << 16); }
DI float bfhi(unsigned u) { return u2f(u & 0xffff0000u); }
DI bf16x8 ld_b128(LAS const char* p) { return *(LAS const bf16x8*)p; }
DI bf16x8 ld_tr2(LAS const char* p0, LAS const char* p1) {
  s16x4 a = __builtin_amdgcn_ds_read_tr16_b64_v4i16((LAS s16x4*)p0);
  s16x4 b = __builtin_amdgcn_ds_read_tr16_b64_v4i16((LAS s16x4*)p1);
  return __builtin_bit_cast(bf16x8, __builtin_shufflevector(a, b, 0, 1, 2, 3, 4, 5, 6, 7));
}
DI float x32_sum(float v) { auto r = __builtin_amdgcn_permlane32_swap(f2u(v), f2u(v), false, false); return u2f(r[0]) + u2f(r[1]); }
DI float x16_sum(float v) { auto r = __builtin_amdgcn_permlane16_swap(f2u(v), f2u(v), false, false); return u2f(r[0]) + u2f(r[1]); }
template <int CTRL> DI float dpp_add(float v) { return v + u2f(__builtin_amdgcn_update_dpp(0, (int)f2u(v), CTRL, 0xf, 0xf, true)); }
DI float half_sum(float v) {
  v = dpp_add<0xB1>(v);
  v = dpp_add<0x4E>(v);
  v = dpp_add<0x141>(v);
  v = dpp_add<0x140>(v);
  return x16_sum(v);
}
#define MFMA16(a, b, c) __builtin_amdgcn_mfma_f32_16x16x32_bf16((a), (b), (c), 0, 0, 0)
DI rsrc_t mkrsrc(const void* p, unsigned bytes) { return __builtin_amdgcn_make_buffer_rsrc((void*)p, 0, bytes, 0x00020000); }
DI void st128(u32x4 v, rsrc_t r, int off) { __builtin_amdgcn_raw_buffer_store_b128(v, r, off, 0, SC1); }
DI u32x4 ld128(rsrc_t r, int off, int soff) { return __builtin_amdgcn_raw_buffer_load_b128(r, off, soff, SC1); }

DI void set_flag(GAS unsigned* ctl, int line, int word, unsigned tag) { __hip_atomic_store(ctl + line * 64 + word, tag, __ATOMIC_RELAXED, __HIP_MEMORY_SCOPE_AGENT); }
DI void acquire() {
  asm volatile("" ::: "memory");
}
DI void poll(GAS unsigned* ctl, int line, int lo, int n, unsigned tag, int lane) {
  GAS unsigned* p = ctl + line * 64 + lo + (lane < n ? lane : 0);
  unsigned spins = 0;
  while (!wave_all(__hip_atomic_load(p, __ATOMIC_RELAXED, __HIP_MEMORY_SCOPE_AGENT) == tag)) {
    __builtin_amdgcn_s_sleep(1);
    if (++spins > (1u << 17)) { if (lane == 0) __hip_atomic_store(ctl + C_TMO * 64, (unsigned)line, __ATOMIC_RELAXED, __HIP_MEMORY_SCOPE_AGENT); break; }
  }
  acquire();
}
struct FastPoll {
  unsigned a, b, c, d;
  DI void run(GAS unsigned* ctl, int line, int lo, int n, unsigned tag, int lane) {
    GAS unsigned* p = ctl + line * 64 + lo + (lane < n ? lane : 0);
    unsigned cnt = 1u << 17;
#define FP_LD(r) "global_load_dword %" #r ", %5, off sc1\n\ts_sleep " PSLEEP "\n\t"
#define FP_CK(r) "s_waitcnt vmcnt(3)\n\tv_cmp_eq_u32 vcc, %6, %" #r "\n\ts_cmp_eq_u64 vcc, -1\n\ts_cbranch_scc1 2f\n\t" FP_LD(r)
    asm volatile(FP_LD(0) FP_LD(1) FP_LD(2) FP_LD(3) "1:\n\t" FP_CK(0) FP_CK(1) FP_CK(2) FP_CK(3)
                 "s_sub_u32 %4, %4, 1\n\ts_cbranch_scc0 1b\n\t2:"
                 : "=&v"(a), "=&v"(b), "=&v"(c), "=&v"(d), "+s"(cnt) : "v"(p), "s"(tag) : "vcc", "scc", "memory");
#undef FP_LD
#undef FP_CK
  }
  DI void keep() const { asm volatile("" :: "v"(a), "v"(b), "v"(c), "v"(d)); }
};
DI void poll4(FastPoll& fp, LAS char* smem, unsigned go, int lw, GAS unsigned* ctl, int line, int lo, int n, unsigned tag, int lane) {
  LAS volatile unsigned* w = (LAS volatile unsigned*)(smem + L_GO);
  if (lw == 0) {
    fp.run(ctl, line, lo, n, tag, lane);
    *w = go;
  } else {
    fp.a = fp.b = fp.c = fp.d = 0u;
    unsigned spins = 0;
    while (__builtin_amdgcn_readfirstlane(*w) != go && ++spins < (1u << 24)) __builtin_amdgcn_s_sleep(1);
  }
  asm volatile("" ::: "memory");
}

template <int BN> DI constexpr int bswz(int k) {
  return BN == 224 ? (((k >> 3) & 1) << 1)
       : BN == 128 ? (((k & 3) | (((k >> 3) & 1) << 2)) << 1)
                   : ((((k >> 1) & 1) | (((k >> 3) & 1) << 1)) << 1);
}

template <int BN, int NS> DI int bslot(int s) {
  if (BN == 224) return ((0x4c3078 >> (8 * s)) & 255) << 10;
  if (BN == 128) return ((0x78504068 >> (8 * s)) & 255) << 10;
  return NS * 16384 + s * BN * 128;
}
DI constexpr int wo_slot(int t) { return t < 5 ? 81920 + t * 8192 : t < 15 ? (t - 5) * 8192 : L_CSS; }

template <int BN, int NS, int NT, bool AROW, int LAG = 0>
struct Loader {
  static constexpr int PA = 4, PB = BN / 32, PW = PA + PB, BST = BN * 128, CPR = BN / 8;
  static_assert(NS * (16384 + BST) <= L_TAB, "");
  LAS char* dst0; rsrc_t ra, rb; int va[PA], vb[PB], vh[2]; int ldb64, own0, tn;
  static constexpr int NOWN = BN == 128 ? 3 : BN == 224 ? 1 : 0;
  int kh;
  DI int ktile(int t) const {
    if (AROW) return (t + own0) & (NT - 1);
    if (BN != 128) return t < NOWN ? own0 + t : (t - NOWN < own0 ? t - NOWN : t);
    const int pi = t < 17 ? (t - 3) >> 1 : t - 17, p = t < 3 ? own0 : pi + (pi >= own0 ? 1 : 0);
    const int k = t >= 24 ? 7 * ((t >= 28 ? t - 3 : t) - 24) + 3 : ((7 * p + 1) >> 1) + (t < 3 ? t : t < 17 ? 2 * ((t - 3) & 1) : 1);
    return k + ((t >= 28) != (kh != 0) ? 28 : 0);
  }
  DI void issueA(int t) {
    const int kk = ktile(t), slot = t % NS;
#pragma unroll
    for (int i = 0; i < PA; ++i)
      __builtin_amdgcn_raw_ptr_buffer_load_lds(ra, dst0 + slot * 16384 + i * 4096, 16, va[i], kk * (AROW ? 128 : 16384), 0, AROW ? 0 : SC1);
  }
  DI void issueB(int t) {
    const int kk = ktile(t), slot = t % NS;
#pragma unroll
    for (int i = 0; i < PB; ++i) __builtin_amdgcn_raw_ptr_buffer_load_lds(rb, dst0 + bslot<BN, NS>(slot) + i * 4096, 16, vb[i], kk * ldb64, 0, 0);
  }
  DI void issueH(int t) {
    issueA(t);
    const int kk = ktile(t), slot = t % NS;
#pragma unroll
    for (int i = 0; i < 2; ++i) __builtin_amdgcn_raw_ptr_buffer_load_lds(rb, dst0 + bslot<BN, NS>(slot) + i * 4096, 16, vh[i], kk * ldb64, 0, 0);
  }
  DI void init(LAS char* smem, rsrc_t ra_, rsrc_t rb_, int a_off, int lda_b, int b_off, int ldb_b, int segb, int lw, int lane, int own) {
    dst0 = smem + lw * 1024; ra = ra_; rb = rb_; ldb64 = 64 * ldb_b; own0 = own; tn = NS - 1;
#pragma unroll
    for (int i = 0; i < PA; ++i) {
      const int ins = lw + i * 4, r = ins * 8 + (lane >> 3), c = (lane & 7) ^ ((r >> 1) & 7);
      va[i] = AROW ? a_off + r * lda_b + c * 16 : a_off + ins * 1024 + lane * 16;
    }
#pragma unroll
    for (int i = 0; i < PB; ++i) {
      const int ch = (lw + i * 4) * 64 + lane, k = ch / CPR, c = (ch % CPR) ^ bswz<BN>(k);
      vb[i] = b_off + k * ldb_b + (c >> 3) * segb + (c & 7) * 16;
    }
  }
  DI void init_half(int b_off, int ldb_b, int lw, int lane) {
#pragma unroll
    for (int i = 0; i < 2; ++i) { const int k = (lw + i * 4) * 8 + (lane >> 3); vh[i] = b_off + k * ldb_b + (((lane & 7) ^ bswz<64>(k)) * 16); }
  }
  template <int ALLOW = (NS - 2 - LAG) * PW> DI void step() { wait_vm<ALLOW>(); lbar(); issueA(tn); issueB(tn); ++tn; }
  DI void steps(int n) {
#pragma unroll 1
    for (int t = 0; t < n; ++t) step();
  }
  DI void finish() {
    if constexpr (NS - LAG >= 5) { wait_vm<3 * PW>(); lbar(); }
    if constexpr (NS - LAG >= 4) { wait_vm<2 * PW>(); lbar(); }
    if constexpr (NS - LAG >= 3) { wait_vm<1 * PW>(); lbar(); }
    wait_vm<0>(); lbar();
  }
};
template <int BN, int I0, int I1> DI void load_wtile(LAS char* dst, rsrc_t r, int off, int ldb_b, int segb, int lw, int lane) {
#pragma unroll
  for (int i = I0; i < I1; ++i) {
    const int ch = (lw + i * 4) * 64 + lane, k = ch / (BN / 8), c = (ch % (BN / 8)) ^ bswz<BN>(k);
    __builtin_amdgcn_raw_ptr_buffer_load_lds(r, dst + (lw + i * 4) * 1024, 16, off + k * ldb_b + (c >> 3) * segb + (c & 7) * 16, 0, 0, 0);
  }
}
template <int T0, int T1> DI void load_wo(LAS char* smem, rsrc_t ro, int gp, int lw, int lane) {
  int vo[2];
#pragma unroll
  for (int i = 0; i < 2; ++i) { const int k = (lw + i * 4) * 8 + (lane >> 3); vo[i] = gp * 128 + k * (D_ * 2) + (((lane & 7) ^ bswz<64>(k)) * 16); }
#pragma unroll
  for (int t = T0; t < T1; ++t)
#pragma unroll
    for (int i = 0; i < 2; ++i) __builtin_amdgcn_raw_ptr_buffer_load_lds(ro, smem + wo_slot(t) + (lw + i * 4) * 1024, 16, vo[i], t * 64 * (D_ * 2), 0, 0);
}
DI int opaque(int v) { asm volatile("" : "+v"(v)); return v; }

DI void touch_lines(LAS char* smem, rsrc_t r, int base, int line, int nlines) {
  __builtin_amdgcn_raw_ptr_buffer_load_lds(r, smem + L_DUMMY, 4, line < nlines ? base + line * 128 : (int)0x80000000, 0, 0, 0);
}

template <int BN> DI void colsum_tile(LAS char* img, rsrc_t rws, unsigned off, int lw, int lane) {
  constexpr int RS = BN * 2;
  const int r16 = lane & 15, g = lane >> 4, q = (lane & 15) >> 2, p = lane & 3;
  const s16x8 one_ = {0x3f80, 0x3f80, 0x3f80, 0x3f80, 0x3f80, 0x3f80, 0x3f80, 0x3f80};
  LAS const char* rd[4];
#pragma unroll
  for (int i = 0; i < 4; ++i) {
    const int nt = lw + 4 * i < BN / 16 ? lw + 4 * i : lw;
    rd[i] = img + (8 * g + q) * RS + (((2 * nt + (p >> 1)) ^ bswz<BN>(8 * g + q)) * 16) + 8 * (p & 1);
  }
  s16x4 v[4][4];
#define TR4(a, b, c, d, r) "ds_read_b64_tr_b16 %" #a ", %" #r "\n\tds_read_b64_tr_b16 %" #b ", %" #r " offset:%c20\n\t" \
                           "ds_read_b64_tr_b16 %" #c ", %" #r " offset:%c21\n\tds_read_b64_tr_b16 %" #d ", %" #r " offset:%c22\n\t"
  asm volatile(TR4(0, 1, 2, 3, 16) TR4(4, 5, 6, 7, 17) TR4(8, 9, 10, 11, 18) TR4(12, 13, 14, 15, 19) "s_waitcnt lgkmcnt(0)"
               : "=&v"(v[0][0]), "=&v"(v[0][1]), "=&v"(v[0][2]), "=&v"(v[0][3]), "=&v"(v[1][0]), "=&v"(v[1][1]), "=&v"(v[1][2]), "=&v"(v[1][3]),
                 "=&v"(v[2][0]), "=&v"(v[2][1]), "=&v"(v[2][2]), "=&v"(v[2][3]), "=&v"(v[3][0]), "=&v"(v[3][1]), "=&v"(v[3][2]), "=&v"(v[3][3])
               : "v"(rd[0]), "v"(rd[1]), "v"(rd[2]), "v"(rd[3]), "i"(4 * RS), "i"(32 * RS), "i"(36 * RS) : "memory");
#undef TR4
  f32x4 c[4];
#pragma unroll
  for (int i = 0; i < 4; ++i) {
    c[i] = (f32x4){0.f, 0.f, 0.f, 0.f};
    c[i] = MFMA16(__builtin_bit_cast(bf16x8, __builtin_shufflevector(v[i][0], v[i][1], 0, 1, 2, 3, 4, 5, 6, 7)), __builtin_bit_cast(bf16x8, one_), c[i]);
    c[i] = MFMA16(__builtin_bit_cast(bf16x8, __builtin_shufflevector(v[i][2], v[i][3], 0, 1, 2, 3, 4, 5, 6, 7)), __builtin_bit_cast(bf16x8, one_), c[i]);
  }
  const f32x4 out = r16 == 0 ? c[0] : r16 == 1 ? c[1] : r16 == 2 ? c[2] : c[3];
  st128(__builtin_bit_cast(u32x4, out), rws, lw + 4 * r16 < BN / 16 ? off + (lw + 4 * r16) * 64 + g * 16 : 0x80000000u);
}
DI f32x4 sum4(u32x4 (&t)[4]) {
  asm volatile("" : "+v"(t[0]), "+v"(t[1]), "+v"(t[2]), "+v"(t[3]));
  return (__builtin_bit_cast(f32x4, t[0]) + __builtin_bit_cast(f32x4, t[1])) + (__builtin_bit_cast(f32x4, t[2]) + __builtin_bit_cast(f32x4, t[3]));
}
DI f32x4 lds_sum4(LAS const char* p) {
  return (*(LAS const f32x4*)p + *(LAS const f32x4*)(p + 1024)) + (*(LAS const f32x4*)(p + 2048) + *(LAS const f32x4*)(p + 3072));
}
DI unsigned peek(GAS unsigned* ctl, int line, int lane, int n = 64) { return __hip_atomic_load(ctl + line * 64 + (lane & (n - 1)), __ATOMIC_RELAXED, __HIP_MEMORY_SCOPE_AGENT); }
DI void confirm(unsigned seen, GAS unsigned* ctl, int line, unsigned tag, int lane, int n = 64) {
  asm volatile("" : "+v"(seen));
  if (!wave_all(seen == tag)) poll(ctl, line, 0, n, tag, lane);
  else acquire();
}

DI void mfma16(f32x4& c, const bf16x8& a, const bf16x8& b) { asm volatile("v_mfma_f32_16x16x32_bf16 %0, %1, %2, %0" : "+v"(c) : "v"(a), "v"(b)); }
DI void mfma_drain() { asm volatile("s_nop 7\n\ts_nop 7" ::: "memory"); }
template <int TM, int TN> struct Frag { s16x4 b[TN][2]; bf16x8 a[TM]; };
template <int BN, int NS, int NT, int TM, int TN, bool DEEP, bool OWN, class NMap, class Mid, class Send>
DI void mma_loop(LAS char* smem, f32x4 (&acc)[TM][TN], int row0, int lane, NMap&& nt, Mid&& mid, Send&& send) {
  constexpr int RS = BN * 2, BST = BN * 128, NL = TM + 2 * TN, NM = TM * TN;
  typedef Frag<TM, TN> F;
  const int r16 = lane & 15, g = lane >> 4, q = (lane & 15) >> 2, p = lane & 3;
  constexpr bool AROW = BN == 192;
  const int aoff = AROW ? (row0 + r16) * 128 + ((g ^ (r16 >> 1)) * 16) : row0 * 64 + lane * 16;
  const int aoff1 = AROW ? aoff ^ 64 : aoff + 8192;
  int boff[TN];
#pragma unroll
  for (int j = 0; j < TN; ++j) boff[j] = (8 * g + q) * RS + (((2 * nt(j) + (p >> 1)) ^ bswz<BN>(8 * g + q)) * 16) + 8 * (p & 1);
#pragma unroll
  for (int i = 0; i < TM; ++i)
#pragma unroll
    for (int j = 0; j < TN; ++j) { acc[i][j] = (f32x4){0.f, 0.f, 0.f, 0.f}; asm volatile("" : "+v"(acc[i][j])); }
  int boffh[2];
  auto ld1 = [&](F& f, int slot, int s, int k, int tn = TN) {
    if (k == 0 || k > 2 * tn) { const int i = k == 0 ? 0 : k - 2 * tn; f.a[i] = ld_b128(smem + slot * 16384 + (s ? aoff1 : aoff) + i * (AROW ? 2048 : 1024)); }
    else {
      const int j = (k - 1) >> 1, u = (k - 1) & 1;
      f.b[j][u] = __builtin_amdgcn_ds_read_tr16_b64_v4i16((LAS s16x4*)(smem + bslot<BN, NS>(slot) + (tn == TN ? boff[j] + (32 * s + 4 * u) * RS : boffh[j & 1] + (32 * s + 4 * u) * 128)));
    }
  };
  auto mm1 = [&](const F& f, int m, int tn = TN) {
    const int i = m / tn, j = m % tn;
    const bf16x8 b = __builtin_bit_cast(bf16x8, __builtin_shufflevector(f.b[j][0], f.b[j][1], 0, 1, 2, 3, 4, 5, 6, 7));
    if (AROW && j >= 4) mfma16(acc[i][j], f.a[i], b); else mfma16(acc[i][j], b, f.a[i]);
  };
  auto ldall = [&](F& f, int slot, int s) {
#pragma unroll
    for (int k = 0; k < NL; ++k) ld1(f, slot, s, k);
  };
  auto mmall = [&](const F& f) {
#pragma unroll
    for (int m = 0; m < NM; ++m) mm1(f, m);
  };
  auto sub = [&](const F& x, F& u, int slot, int s) {
    constexpr int SP = DEEP ? NM : (NL < NM ? NL : NM - 2);
#pragma unroll
    for (int m = 0; m < NM; ++m) {
      mm1(x, m);
#pragma unroll
      for (int k = (m * NL + SP - 1) / SP; k < NL && k < ((m + 1) * NL + SP - 1) / SP; ++k) ld1(u, slot, s, k);
      __builtin_amdgcn_sched_barrier(0);
    }
  };
  auto subh = [&](const F& x, F& u, int slot, int s, auto tx, auto tu) {
    constexpr int TX = decltype(tx)::value, TU = decltype(tu)::value, M = TM * TX, LN = TM + 2 * TU;
#pragma unroll
    for (int m = 0; m < M; ++m) {
      mm1(x, m, TX);
#pragma unroll
      for (int k = (m * LN + M - 1) / M; k < LN && k < ((m + 1) * LN + M - 1) / M; ++k) ld1(u, slot, s, k, TU);
      __builtin_amdgcn_sched_barrier(0);
    }
  };
  auto nxt = [](int s) { return s == NS - 1 ? 0 : s + 1; };
  lbar();
  int slot = 0;
  if constexpr (!DEEP) {
    constexpr int NB = AROW ? 2 : TN, NA = AROW ? 2 : 1;
    LAS char* ca[NA];
    LAS char* cb[NB];
    ca[0] = smem + aoff;
    if constexpr (AROW) ca[NA - 1] = smem + aoff1;
#pragma unroll
    for (int j = 0; j < NB; ++j) cb[j] = smem + bslot<BN, NS>(0) + boff[j];
    auto ldc = [&](F& f, int s, int k) {
      if (k == 0 || k > 2 * TN) { const int i = k == 0 ? 0 : k - 2 * TN; f.a[i] = ld_b128(AROW ? ca[s] + i * 2048 : ca[0] + s * 8192 + i * 1024); }
      else {
        const int j = (k - 1) >> 1, u = (k - 1) & 1;
        f.b[j][u] = __builtin_amdgcn_ds_read_tr16_b64_v4i16((LAS s16x4*)((AROW ? cb[j & 1] + (j >> 1) * 128 : cb[j]) + (32 * s + 4 * u) * RS));
      }
    };
    auto subc = [&](const F& x, F& u, int s, bool adv) {
      const int ns = nxt(slot), da = (ns - slot) * 16384, db = bslot<BN, NS>(ns) - bslot<BN, NS>(slot);
#pragma unroll
      for (int m = 0; m < NM; ++m) {
        mm1(x, m);
        if (m < NL) ldc(u, s, m);
        else if (adv && m - NL < NA) asm volatile("v_add_u32 %0, %1, %0" : "+v"(ca[m - NL < NA ? m - NL : 0]) : "s"(da));
        else if (adv && m - NL - NA < NB) asm volatile("v_add_u32 %0, %1, %0" : "+v"(cb[m - NL - NA < NB ? m - NL - NA : 0]) : "s"(db));
        __builtin_amdgcn_sched_barrier(0);
      }
      if (adv) slot = ns;
    };
    static_assert(NL + NA + NB <= NM, "");
    F f0, f1;
#pragma unroll
    for (int k = 0; k < NL; ++k) ldc(f0, 0, k);
    if constexpr (OWN) {
      subc(f0, f1, 1, true);
      mmall(f1);
      __builtin_amdgcn_sched_barrier(0);
      bar();
#pragma unroll
      for (int k = 0; k < NL; ++k) ldc(f0, 0, k);
    }
#pragma unroll 1
    for (int t = OWN ? 1 : 0; t < NT - 1; ++t) {
      subc(f0, f1, 1, true);
      lbar();
      subc(f1, f0, 0, false);
    }
    subc(f0, f1, 1, false);
    lbar();
    mmall(f1);
  } else {
    F f0, f1, g0, g1;
    auto tile = [&](const F& x0, const F& x1, F& u0, F& u1, int t) {
      bar();
      slot = nxt(slot);
      sub(x0, u0, slot, 0);
      sub(x1, u1, slot, 1);
    };
    ldall(f0, 0, 0); ldall(f1, 0, 1);
    int t0 = 0;
    if constexpr (OWN) {
      tile(f0, f1, g0, g1, 0);
      tile(g0, g1, f0, f1, 1);
      mmall(f0); mmall(f1);
      __builtin_amdgcn_sched_barrier(0);
      lbar();
      slot = 3;
      ldall(g0, 3, 0); ldall(g1, 3, 1);
      tile(g0, g1, f0, f1, 3);
      t0 = 4;
    }
    if constexpr (!OWN) {
#pragma unroll 1
      for (int t = t0; t < NT - 2; t += 2) {
        tile(f0, f1, g0, g1, t);
        tile(g0, g1, f0, f1, t + 1);
      }
      tile(f0, f1, g0, g1, NT - 2);
      lbar();
      mmall(g0); mmall(g1);
    } else {
#pragma unroll 1
      for (int t = t0; t < 24; t += 2) {
        tile(f0, f1, g0, g1, t);
        if (t == 4) mid();
        tile(g0, g1, f0, f1, t + 1);
      }
      typedef std::integral_constant<int, TN> Full;
      typedef std::integral_constant<int, TN / 2> Half;
      auto tileh = [&](const F& x0, const F& x1, F& u0, F& u1, auto tx) {
        bar();
        slot = nxt(slot);
        subh(x0, u0, slot, 0, tx, Half{});
        subh(x1, u1, slot, 1, tx, Half{});
      };
#pragma unroll
      for (int j = 0; j < 2; ++j) {
        const int l2 = opaque(lane), k = 8 * (l2 >> 4) + ((l2 & 15) >> 2);
        boffh[j] = k * 128 + (((2 * nt(j) + ((l2 & 3) >> 1)) ^ bswz<64>(k)) * 16) + 8 * (l2 & 1);
      }
      tileh(f0, f1, g0, g1, Full{});
      mfma_drain();
#pragma unroll
      for (int i = 0; i < TM; ++i)
#pragma unroll
        for (int j = TN / 2; j < TN; ++j) asm volatile("" : "+v"(acc[i][j]));
      send(0);
      __builtin_amdgcn_sched_barrier(0);
      tileh(g0, g1, f0, f1, Half{});
      tileh(f0, f1, g0, g1, Half{});
      send(1);
      tileh(g0, g1, f0, f1, Half{});
      tileh(f0, f1, g0, g1, Half{});
      send(2);
      tileh(g0, g1, f0, f1, Half{});
      send(3);
      lbar();
#pragma unroll
      for (int m = 0; m < TM * TN / 2; ++m) mm1(f0, m, TN / 2);
#pragma unroll
      for (int m = 0; m < TM * TN / 2; ++m) mm1(f1, m, TN / 2);
    }
  }
  mfma_drain();
#pragma unroll
  for (int i = 0; i < TM; ++i)
#pragma unroll
    for (int j = 0; j < TN; ++j) asm volatile("" : "+v"(acc[i][j]));
}
DI void swap_pair(f32x4& x, f32x4& y) {
#pragma unroll
  for (int e = 0; e < 4; ++e) {
    auto r = __builtin_amdgcn_permlane16_swap(f2u(x[e]), f2u(y[e]), false, false);
    x[e] = u2f(r[0]); y[e] = u2f(r[1]);
  }
}
DI void add_bf8(f32x4& x, f32x4& y, const u32x4& rv) {
  x[0] += bflo(rv[0]); x[1] += bfhi(rv[0]); x[2] += bflo(rv[1]); x[3] += bfhi(rv[1]);
  y[0] += bflo(rv[2]); y[1] += bfhi(rv[2]); y[2] += bflo(rv[3]); y[3] += bfhi(rv[3]);
}
DI u32x4 pack_xy(const f32x4& x, const f32x4& y) { return (u32x4){pk2(x[0], x[1]), pk2(x[2], x[3]), pk2(y[0], y[1]), pk2(y[2], y[3])}; }

template <class Op> DI void red2(float& a, float& b, Op&& op) {
  auto r = __builtin_amdgcn_permlane16_swap(f2u(a), f2u(b), false, false);
  const float c = op(u2f(r[0]), u2f(r[1]));
  auto t = __builtin_amdgcn_permlane32_swap(f2u(c), f2u(c), false, false);
  const float d = op(u2f(t[0]), u2f(t[1]));
  auto u = __builtin_amdgcn_permlane16_swap(f2u(d), f2u(d), false, false);
  a = u2f(u[0]); b = u2f(u[1]);
}
DI void dual_softmax(const f32x4 (&p)[2][2], const float (&sc)[2], f32x4 (&e)[2][2][2]) {
  float mx[2], mn[2], s1[2], s2[2];
#pragma unroll
  for (int t = 0; t < 2; ++t) {
    mx[t] = p[t][0][0]; mn[t] = p[t][0][0];
#pragma unroll
    for (int r = 1; r < 8; ++r) { mx[t] = __builtin_fmaxf(mx[t], p[t][r >> 2][r & 3]); mn[t] = __builtin_fminf(mn[t], p[t][r >> 2][r & 3]); }
  }
  red2(mx[0], mx[1], [](float x, float y) { return __builtin_fmaxf(x, y); });
  red2(mn[0], mn[1], [](float x, float y) { return __builtin_fminf(x, y); });
#pragma unroll
  for (int t = 0; t < 2; ++t) {
    const float a = -mx[t] * sc[t], b = mn[t] * sc[t];
    s1[t] = 0.f; s2[t] = 0.f;
#pragma unroll
    for (int r = 0; r < 8; ++r) {
      const float x = __builtin_amdgcn_exp2f(__builtin_fmaf(p[t][r >> 2][r & 3], sc[t], a));
      const float y = __builtin_amdgcn_exp2f(__builtin_fmaf(p[t][r >> 2][r & 3], -sc[t], b));
      e[0][t][r >> 2][r & 3] = x; e[1][t][r >> 2][r & 3] = y;
      s1[t] += x; s2[t] += y;
    }
  }
  red2(s1[0], s1[1], [](float x, float y) { return x + y; });
  red2(s2[0], s2[1], [](float x, float y) { return x + y; });
#pragma unroll
  for (int t = 0; t < 2; ++t) {
    const float r1 = __builtin_amdgcn_rcpf(s1[t]), r2 = __builtin_amdgcn_rcpf(s2[t]);
#pragma unroll
    for (int r = 0; r < 8; ++r) { e[0][t][r >> 2][r & 3] *= r1; e[1][t][r >> 2][r & 3] *= r2; }
  }
}
DI bf16x8 pack_frag(const f32x4& x, const f32x4& y) { return __builtin_bit_cast(bf16x8, pack_xy(x, y)); }
DI void st4(LAS char* p, const f32x4& a, const f32x4& b, const f32x4& c, const f32x4& d) {
  *(LAS f32x4*)p = a; *(LAS f32x4*)(p + 1024) = b; *(LAS f32x4*)(p + 2048) = c; *(LAS f32x4*)(p + 3072) = d;
}
DI void ld4(LAS const char* p, f32x4 (&v)[2][2]) {
  v[0][0] = *(LAS const f32x4*)p; v[0][1] = *(LAS const f32x4*)(p + 1024); v[1][0] = *(LAS const f32x4*)(p + 2048); v[1][1] = *(LAS const f32x4*)(p + 3072);
}
#endif

__global__ __launch_bounds__(512, 2) void block_kernel(const u16* __restrict__ X, const u16* __restrict__ WQKV, const u16* __restrict__ WPHI,
                                                        const u16* __restrict__ WO, const u16* __restrict__ W1, const u16* __restrict__ W2,
                                                        u16* __restrict__ OUT, char* WS) {
#if defined(__HIP_DEVICE_COMPILE__)
  extern __shared__ __attribute__((aligned(16))) char smem_[];
  LAS char* smem = (LAS char*)smem_;
  const int tid = threadIdx.x, wid = __builtin_amdgcn_readfirstlane(tid >> 6);
  int lane = tid & 63;
  const int bid = blockIdx.x, xcd = bid & 7, loc = bid >> 3;
  const int tm = (xcd & 1) * 8 + (loc & 7), gp = (xcd >> 1) * 4 + (loc >> 3);
  const bool loader = wid >= 4;
  const int lw = wid & 3;
  GAS unsigned* ctl = (GAS unsigned*)WS;
  const rsrc_t rws = mkrsrc(WS, WS_END);
  if (wid == CPW) {
    unsigned long long pc;
    asm volatile("s_getpc_b64 %0" : "=s"(pc));
    touch_lines(smem, mkrsrc((const void*)(pc & ~127ull), CODE_AHEAD), loc * 2048, lane < 16 ? lane : 4096, 16);
  }
  unsigned epoch, tag;
  auto load_tag = [&]() { epoch = __hip_atomic_load(ctl + C_EPOCH * 64, __ATOMIC_RELAXED, __HIP_MEMORY_SCOPE_AGENT); };
  auto read_tag = [&]() { asm volatile("" : "+v"(epoch)); tag = __builtin_amdgcn_readfirstlane(epoch) + 1u; };

  int r16, g, cpair, cswz;
  auto relane = [&]() {
    lane = opaque(lane);
    r16 = lane & 15; g = lane >> 4;
    cpair = (g & 1) * 16 + (g >> 1) * 8;
    cswz = (cpair >> 3) * 256 + r16 * 16;
  };
  {
    constexpr int NT = 16, NS = 3;
    constexpr int L_PHI = 0;
    constexpr int L_XV = 2 * 17408;
    constexpr int L_VF = L_XV + 16384;
    constexpr int L_XQ = L_VF + 8192;
    constexpr int L_KVT = L_XQ;
    constexpr int L_WF = L_XQ + 16384;
    static_assert(L_WF + 4096 <= 5 * 16384, "phase 2's B ring is filled during this epilogue");
    relane();
    const int wm = (wid >> 1) & 1, hh = wid & 1, mi = wid >> 2;
    const int head = gp * 2 + hh;
    const unsigned kvp_grp = WS_KVP + gp * 16 * KVP_BYTES;
    LAS char* const xk = smem + L_PHI + hh * 17408 + (wm * 64 + 32) * 136 + lane * 16;
    bf16x8 wf[2];
    f32x4 fq[2][2], fk[2][2], fv[2][2];
    if (loader) {
      const rsrc_t rb = mkrsrc(WQKV, D_ * 3 * D_ * 2), ro = mkrsrc(WO, D_ * D_ * 2);
      Loader<192, NS, NT, true> L;
      const rsrc_t rx = mkrsrc(X, S_ * D_ * 2);
      L.init(smem, rx, rb, tm * 128 * D_ * 2, D_ * 2, gp * 128, 3 * D_ * 2, 2048, lw, lane, (tm & 3) * 4);
      L.issueA(0); L.issueB(0); L.issueA(1); L.issueB(1);
      if (lw == 0) *(LAS volatile unsigned*)(smem + L_GO) = 0u;
      load_tag();
      __builtin_amdgcn_s_setprio(3);
      auto cs1 = [&](int t) { if ((tm >> 2) == t) colsum_tile<192>(smem + NS * 16384 + (t % NS) * 24576, rws, WS_CS1 + (gp * 16 + tm) * 768, lw, lane); };
      L.step();
      cs1(0);
      const int xrow = tm * 128 + gp * 8 + lw * 2 + (lane >> 5);
      u32x4 xv[4];
#pragma unroll
      for (int j = 0; j < 4; ++j) xv[j] = __builtin_amdgcn_raw_buffer_load_b128(rx, xrow * (D_ * 2) + (lane & 31) * 16, j * 512, 0);
      L.step<14>();
      cs1(1);
      L.step<14>();
      cs1(2);
      L.step();
      cs1(3);
      L.step();
      read_tag();
      asm volatile("" : "+v"(xv[0]), "+v"(xv[1]), "+v"(xv[2]), "+v"(xv[3]));
      float sum = 0.f, qq = 0.f;
#pragma unroll
      for (int i = 0; i < 16; ++i) sum += bflo(xv[i >> 2][i & 3]) + bfhi(xv[i >> 2][i & 3]);
      float mean = half_sum(sum) * (1.0f / D_);
      asm volatile("" : "+v"(mean));
      L.step();
      asm volatile("" : "+v"(xv[0]), "+v"(xv[1]), "+v"(xv[2]), "+v"(xv[3]));
#pragma unroll
      for (int i = 0; i < 16; ++i) {
        const float d0 = bflo(xv[i >> 2][i & 3]) - mean, d1 = bfhi(xv[i >> 2][i & 3]) - mean;
        qq = __builtin_fmaf(d0, d0, __builtin_fmaf(d1, d1, qq));
      }
      const float rstd = __builtin_amdgcn_rsqf(half_sum(qq) * (1.0f / D_) + LN_EPS);
      __builtin_amdgcn_raw_buffer_store_b64(__builtin_bit_cast(u32x2, (f32x2){mean, rstd}), rws, (lane & 31) == 0 ? WS_XST + xrow * 8 : 0x80000000u, 0, SC1);
      L.step();
      L.step<0>();
      if (lane == 0) { set_flag(ctl, C_CS1 + gp, tm * 4 + lw, tag); set_flag(ctl, C_ST + tm, gp * 4 + lw, tag); }
      touch_lines(smem, ro, bid * 8192, lane < 16 ? lw * 16 + lane : 4096, 64);
      L.step();
      L.step<13>();
      L.step();
      const unsigned seen = peek(ctl, C_CS1 + gp, lane), seen2 = peek(ctl, C_ST + tm, lane);
      L.steps(2);
      confirm(seen, ctl, C_CS1 + gp, tag, lane);
      confirm(seen2, ctl, C_ST + tm, tag, lane);
      u32x4 gt[4];
#pragma unroll
      for (int j = 0; j < 4; ++j) gt[j] = ld128(rws, WS_CS1 + (gp * 16 + lw * 4) * 768 + lane * 16, j * 768);
      u32x4 xs = ld128(rws, WS_XST + tm * 1024 + lane * 16, 0);
      L.step();
      wait_vm<10>(); lbar();
      *(LAS f32x4*)(smem + L_CSS + lw * 1024 + lane * 16) = sum4(gt);
      asm volatile("" : "+v"(xs));
      if (lw == 1) *(LAS u32x4*)(smem + L_RST + lane * 16) = xs;
      wait_vm<0>(); lbar();
      if (lw == 0) *(LAS f32x4*)(smem + L_CS + lane * 16) = lds_sum4(smem + L_CSS + lane * 16);
      const rsrc_t r1 = mkrsrc(W1, D_ * FF_ * 2);
      load_wtile<224, 0, 4>(smem + L_WT, r1, tm * 64 * (FF_ * 2) + gp * 448, FF_ * 2, 128, lw, lane);
      lbar();
      bar();
      __builtin_amdgcn_s_setprio(1);
      ld4(xk, fk);
      wf[0] = ld_b128(smem + L_WF + hh * 2048 + lane * 16);
      wf[1] = ld_b128(smem + L_WF + hh * 2048 + 1024 + lane * 16);
      ld4(smem + L_XV + lw * 4096 + lane * 16, fv);
    } else {
      {
#pragma unroll
        for (int t = 0; t < 2; ++t) {
          s16x8 w;
#pragma unroll
          for (int j = 0; j < 8; ++j) w[j] = (short)WPHI[head * 1024 + (16 * (j >> 2) + 4 * g + (j & 3)) * 32 + 16 * t + r16];
          wf[t] = __builtin_bit_cast(bf16x8, w);
        }
        load_tag();
      }
      f32x4 acc[4][6];
      mma_loop<192, NS, NT, 4, 6, false, false>(smem, acc, wm * 64, lane, [&](int j) { return (j >> 1) * 4 + hh * 2 + (j & 1); }, []() {}, [](int) {});
      read_tag();
      if (wm == 0) {
        *(LAS bf16x8*)(smem + L_WF + hh * 2048 + lane * 16) = wf[0];
        *(LAS bf16x8*)(smem + L_WF + hh * 2048 + 1024 + lane * 16) = wf[1];
      }
      st4(xk, acc[2][2], acc[2][3], acc[3][2], acc[3][3]);
      st4(smem + L_XV + wid * 4096 + lane * 16, acc[2][4], acc[2][5], acc[3][4], acc[3][5]);
      lbar();
      st4(smem + L_XQ + wid * 4096 + lane * 16, acc[2][0], acc[2][1], acc[3][0], acc[3][1]);
#pragma unroll
      for (int t = 0; t < 2; ++t)
#pragma unroll
        for (int u = 0; u < 2; ++u) { fq[t][u] = acc[t][u]; fk[t][u] = acc[t][2 + u]; fv[t][u] = acc[t][4 + u]; }
    }
    const int rbase = wm * 64 + mi * 32, q = r16 >> 2, p = lane & 3;
    LAS const float* cs = (LAS const float*)(smem + L_CS) + hh * 32;
    LAS const f32x2* rst = (LAS const f32x2*)(smem + L_RST) + rbase;
    auto feature = [&](const f32x4 (&x)[2][2], int part, f32x4 (&e)[2][2][2]) {
      const f32x4 c0 = *(LAS const f32x4*)(cs + part * 64 + 4 * g), c1 = *(LAS const f32x4*)(cs + part * 64 + 16 + 4 * g);
      f32x4 pp[2][2];
      float sc[2];
#pragma unroll
      for (int t = 0; t < 2; ++t) {
        const f32x2 ms = rst[t * 16 + r16];
        sc[t] = ms[1] * 1.4426950408889634f;
        f32x4 y0, y1;
#pragma unroll
        for (int e_ = 0; e_ < 4; ++e_) { y0[e_] = __builtin_fmaf(-ms[0], c0[e_], x[t][0][e_]); y1[e_] = __builtin_fmaf(-ms[0], c1[e_], x[t][1][e_]); }
        const bf16x8 yb = pack_frag(y0, y1);
        pp[t][0] = MFMA16(wf[0], yb, ((f32x4){0.f, 0.f, 0.f, 0.f}));
        pp[t][1] = MFMA16(wf[1], yb, ((f32x4){0.f, 0.f, 0.f, 0.f}));
      }
      dual_softmax(pp, sc, e);
    };
    {
      const float cv0 = cs[128 + r16], cv1 = cs[144 + r16];
#pragma unroll
      for (int t = 0; t < 2; ++t) {
        const f32x4 s01 = *(LAS const f32x4*)(rst + t * 16 + 4 * g), s23 = *(LAS const f32x4*)(rst + t * 16 + 4 * g + 2);
        const float mv[4] = {s01[0], s01[2], s23[0], s23[2]}, rv[4] = {s01[1], s01[3], s23[1], s23[3]};
#pragma unroll
        for (int e_ = 0; e_ < 4; ++e_) {
          fv[t][0][e_] = __builtin_fmaf(-mv[e_], cv0, fv[t][0][e_]) * rv[e_];
          fv[t][1][e_] = __builtin_fmaf(-mv[e_], cv1, fv[t][1][e_]) * rv[e_];
        }
      }
      LAS char* vd = smem + (loader ? L_XV + lw * 4096 : L_VF + lw * 2048) + lane * 16;
      *(LAS bf16x8*)vd = pack_frag(fv[0][0], fv[1][0]);
      *(LAS bf16x8*)(vd + 1024) = pack_frag(fv[0][1], fv[1][1]);
      f32x4 e[2][2][2];
      feature(fk, 1, e);
      LAS char* wr = smem + L_PHI + hh * 17408 + (rbase + r16) * 136 + g * 8;
#pragma unroll
      for (int w = 0; w < 2; ++w)
#pragma unroll
        for (int t = 0; t < 2; ++t)
#pragma unroll
          for (int u = 0; u < 2; ++u)
            *(LAS u32x2*)(wr + t * 16 * 136 + w * 64 + u * 32) = (u32x2){pk2(e[w][t][u][0], e[w][t][u][1]), pk2(e[w][t][u][2], e[w][t][u][3])};
    }
    lbar();
    __builtin_amdgcn_s_setprio(0);
    bf16x8 pf[2][2];
    auto qpath = [&]() {
      f32x4 e[2][2][2];
      feature(fq, 0, e);
#pragma unroll
      for (int t = 0; t < 2; ++t)
#pragma unroll
        for (int w = 0; w < 2; ++w) { pf[t][w] = pack_frag(e[w][t][0], e[w][t][1]); asm volatile("" : "+v"(pf[t][w])); }
    };
    if (loader) {
      ld4(smem + L_XQ + lw * 4096 + lane * 16, fq);
      qpath();
      load_wtile<224, 4, 7>(smem + L_WT, mkrsrc(W1, D_ * FF_ * 2), tm * 64 * (FF_ * 2) + gp * 448, FF_ * 2, 128, lw, lane);
    } else {
      f32x4 k00 = {0.f, 0.f, 0.f, 0.f}, k01 = k00, k10 = k00, k11 = k00, z0 = k00, z1 = k00;
      const s16x8 one_ = {0x3f80, 0x3f80, 0x3f80, 0x3f80, 0x3f80, 0x3f80, 0x3f80, 0x3f80};
      LAS const char* rd = smem + L_PHI + hh * 17408 + (4 * g + q) * 136 + wm * 64 + p * 8;
#pragma unroll
      for (int u = 0; u < 4; ++u) {
        const bf16x8 pa0 = ld_tr2(rd + 32 * u * 136, rd + (32 * u + 16) * 136), pa1 = ld_tr2(rd + 32 * u * 136 + 32, rd + (32 * u + 16) * 136 + 32);
        LAS const char* vp = smem + ((u & 1) ? L_XV + ((u >> 1) * 2 + hh) * 4096 : L_VF + ((u >> 1) * 2 + hh) * 2048) + lane * 16;
        const bf16x8 v0 = ld_b128(vp), v1 = ld_b128(vp + 1024);
        k00 = MFMA16(pa0, v0, k00); k10 = MFMA16(pa1, v0, k10);
        k01 = MFMA16(pa0, v1, k01); k11 = MFMA16(pa1, v1, k11);
        z0 = MFMA16(pa0, __builtin_bit_cast(bf16x8, one_), z0); z1 = MFMA16(pa1, __builtin_bit_cast(bf16x8, one_), z1);
      }
      const int kb = kvp_grp + tm * KVP_BYTES;
      st128(pack_xy(k00, k10), rws, kb + hh * 4096 + wm * 2048 + lane * 16);
      st128(pack_xy(k01, k11), rws, kb + hh * 4096 + wm * 2048 + 1024 + lane * 16);
      st128(__builtin_bit_cast(u32x4, r16 == 0 ? z0 : z1), rws, r16 < 2 ? kb + 8192 + hh * 256 + wm * 128 + g * 32 + r16 * 16 : (int)0x80000000);
      wait_vm<0>();
    }
    lbar();
    if (tid == 0) set_flag(ctl, C_KV + gp, tm, tag);
    if (loader) {
      load_wo<0, WO1>(smem, mkrsrc(WO, D_ * D_ * 2), gp, lw, lane);
    } else {
      qpath();
    }
    FastPoll fpk = {0u, 0u, 0u, 0u};
    if (wid == 0) {
      fpk.run(ctl, C_KV + gp, 0, 16, tag, lane);
    }
    bar();
    {
      u32x4 tz[16];
      if (wid == 7) {
#pragma unroll
        for (int j = 0; j < 16; ++j) tz[j] = ld128(rws, lane < 32 ? kvp_grp + 8192 + lane * 16 : 0x80000000u, j * KVP_BYTES);
      }
      u32x4 t[16];
#pragma unroll
      for (int j = 0; j < 16; ++j) t[j] = ld128(rws, kvp_grp + tid * 16, j * KVP_BYTES);
      if (wid == 7) {
        f32x4 z = __builtin_bit_cast(f32x4, tz[0]);
#pragma unroll
        for (int j = 1; j < 16; ++j) z += __builtin_bit_cast(f32x4, tz[j]);
        if (lane < 32) *(LAS u32x2*)(smem + L_KVT + 8192 + lane * 8) = (u32x2){pk2(z[0], z[1]), pk2(z[2], z[3])};
      }
      f32x4 x = {0.f, 0.f, 0.f, 0.f}, y = x;
#pragma unroll
      for (int j = 0; j < 16; ++j) add_bf8(x, y, t[j]);
      *(LAS u32x4*)(smem + L_KVT + tid * 16) = pack_xy(x, y);
    }
    fpk.keep();
    lbar();
    {
      bf16x8 ka[2][2], za[2];
#pragma unroll
      for (int w = 0; w < 2; ++w) {
        ka[w][0] = ld_b128(smem + L_KVT + hh * 4096 + w * 2048 + lane * 16);
        ka[w][1] = ld_b128(smem + L_KVT + hh * 4096 + w * 2048 + 1024 + lane * 16);
        za[w] = ld_b128(smem + L_KVT + 8192 + hh * 128 + w * 64 + g * 16);
      }
#pragma unroll
      for (int t = 0; t < 2; ++t) {
        f32x4 o0 = {0.f, 0.f, 0.f, 0.f}, o1 = o0, dn = o0;
#pragma unroll
        for (int w = 0; w < 2; ++w) { dn = MFMA16(za[w], pf[t][w], dn); o0 = MFMA16(ka[w][0], pf[t][w], o0); o1 = MFMA16(ka[w][1], pf[t][w], o1); }
        const float rd = __builtin_amdgcn_rcpf(dn[0] + 1e-6f);
#pragma unroll
        for (int e_ = 0; e_ < 4; ++e_) { o0[e_] *= rd; o1[e_] *= rd; }
        swap_pair(o0, o1);
        st128(pack_xy(o0, o1), rws, WS_O + (tm * 32 + head) * 8192 + (rbase + t * 16) * 64 + cswz);
      }
    }
    wait_vm<0>();
    lbar();
    if (tid == 0) set_flag(ctl, C_O + tm, gp, tag);
  }

  relane();
  {
    FastPoll fp = {0u, 0u, 0u, 0u};
    if (loader) {
      load_wo<WO1, 16>(smem, mkrsrc(WO, D_ * D_ * 2), gp, lw, lane);
      colsum_tile<224>(smem + L_WT, rws, WS_CS3 + (gp * 16 + tm) * 896, lw, lane);
    }
    if (wid == 0) {
      fp.run(ctl, C_O + tm, 0, 16, tag, lane);
      if (bid == 0 && lane == 0) __hip_atomic_store(ctl + C_EPOCH * 64, tag, __ATOMIC_RELAXED, __HIP_MEMORY_SCOPE_AGENT);
    }
    if (loader) {
      wait_vm<0>();
      bar();
      if (lane == 0) set_flag(ctl, C_CS3 + gp, tm * 4 + lw, tag);
    } else {
      const int row = tm * 128 + wid * 32 + r16, col = gp * 64 + cpair;
      u32x4 res[2][2];
#pragma unroll
      for (int i = 0; i < 2; ++i)
#pragma unroll
        for (int j = 0; j < 2; ++j) res[i][j] = *(const u32x4*)(X + (size_t)(row + i * 16) * D_ + col + j * 32);
      f32x4 acc[2][4];
#pragma unroll
      for (int j = 0; j < 8; ++j) { acc[j >> 2][j & 3] = (f32x4){0.f, 0.f, 0.f, 0.f}; asm volatile("" : "+v"(acc[j >> 2][j & 3])); }
      constexpr int PF = PFD, BD = BDEPTH;
      const int q = r16 >> 2, p = lane & 3;
      int bo[4];
#pragma unroll
      for (int j = 0; j < 4; ++j) bo[j] = (8 * g + q) * 128 + (((2 * j + (p >> 1)) ^ bswz<64>(8 * g + q)) * 16) + 8 * (p & 1);
      const int ao = WS_O + tm * 262144 + wid * 2048 + lane * 16;
      u32x4 af[PF][2];
      s16x4 bf[BD][4][2];
      auto ldb = [&](int sl, int k) {
        bf[sl % BD][k >> 1][k & 1] = __builtin_amdgcn_ds_read_tr16_b64_v4i16((LAS s16x4*)(smem + wo_slot(sl >> 1) + bo[k >> 1] + (32 * (sl & 1) + 4 * (k & 1)) * 128));
      };
      bar();
#pragma unroll
      for (int sl = 0; sl < PF; ++sl) { af[sl][0] = ld128(rws, ao, sl * 8192); af[sl][1] = ld128(rws, ao + 1024, sl * 8192); }
#pragma unroll
      for (int k = 0; k < 8 * (BD - 1); ++k) ldb(k >> 3, k & 7);
      __builtin_amdgcn_sched_barrier(0);
#pragma unroll
      for (int sl = 0; sl < 32; ++sl) {
#pragma unroll
        for (int m = 0; m < 8; ++m) {
          const int i = m >> 2, j = m & 3;
          mfma16(acc[i][j], __builtin_bit_cast(bf16x8, __builtin_shufflevector(bf[sl % BD][j][0], bf[sl % BD][j][1], 0, 1, 2, 3, 4, 5, 6, 7)),
                 __builtin_bit_cast(bf16x8, af[sl % PF][i]));
          if (sl + BD - 1 < 32) ldb(sl + BD - 1, m);
          if (j == 3 && sl + PF < 32) af[sl % PF][i] = ld128(rws, ao + i * 1024, (sl + PF) * 8192);
          __builtin_amdgcn_sched_barrier(0);
        }
        if (sl == 30) lbar();
      }
      fp.keep();
      mfma_drain();
#pragma unroll
      for (int j = 0; j < 8; ++j) asm volatile("" : "+v"(acc[j >> 2][j & 3]));
#pragma unroll
      for (int i = 0; i < 2; ++i) {
        float s = 0.f, qq = 0.f;
#pragma unroll
        for (int j = 0; j < 2; ++j) {
          f32x4 x = acc[i][2 * j], y = acc[i][2 * j + 1];
          swap_pair(x, y);
          add_bf8(x, y, res[i][j]);
          const u32x4 o = pack_xy(x, y);
          st128(o, rws, WS_H + (tm * 32 + gp * 2 + j) * 8192 + (wid * 32 + i * 16) * 64 + cswz);
          *(LAS u32x4*)(smem + j * 8192 + (wid * 32 + i * 16) * 64 + cswz) = o;
#pragma unroll
          for (int e = 0; e < 4; ++e) { const float lo = bflo(o[e]), hi = bfhi(o[e]); s += lo + hi; qq = __builtin_fmaf(lo, lo, __builtin_fmaf(hi, hi, qq)); }
        }
        red2(s, qq, [](float x, float y) { return x + y; });
        __builtin_amdgcn_raw_buffer_store_b64(__builtin_bit_cast(u32x2, (f32x2){s, qq}), rws,
                                              g == 0 ? WS_HST + ((tm * 16 + gp) * 128 + wid * 32 + i * 16 + r16) * 8 : 0x80000000u, 0, SC1);
      }
    }
  }

  const int kh = gp >> 3;
  if (loader) {
    __builtin_amdgcn_s_setprio(3);
    relane();
    const rsrc_t r2 = mkrsrc(W2, FF_ * D_ * 2);
    FastPoll fp;
    {
      Loader<224, 3, 16, false> L;
      L.init(smem, rws, mkrsrc(W1, D_ * FF_ * 2), WS_H + tm * 128 * D_ * 2, 0, gp * 448, FF_ * 2, 128, lw, lane, gp);
      __builtin_amdgcn_s_sleep(B3DELAY);
      L.issueB(0);
      bar();
      bar();
      if (wid == 4 && lane == 0) set_flag(ctl, C_H + tm, gp, tag);
      wait_vm<0>(); bar();
      L.issueB(1); L.issueB(2);
      poll4(fp, smem, 2, lw, ctl, C_H + tm, 0, 16, tag, lane);
      L.issueA(1); L.issueA(2);
      u32x4 gt[4], hs[4];
#pragma unroll
      for (int j = 0; j < 4; ++j) hs[j] = ld128(rws, WS_HST + tm * 16384 + lw * 4096 + lane * 16, j * 1024);
      const unsigned seen = peek(ctl, C_CS3 + gp, lane);
      L.tn = 3;
      L.step<4 + 5>();
      fp.keep();
      L.step<5 + 11>();
      confirm(seen, ctl, C_CS3 + gp, tag, lane);
#pragma unroll
      for (int j = 0; j < 4; ++j) gt[j] = ld128(rws, WS_CS3 + (gp * 16 + lw * 4) * 896 + lane * 16, j * 896);
      *(LAS f32x4*)(smem + L_CSS + 4096 + lw * 1024 + lane * 16) = sum4(hs);
      L.step();
      if (lw == 1) {
        const f32x4 sum = lds_sum4(smem + L_CSS + 4096 + lane * 16);
        const float m0 = sum[0] * (1.0f / D_), m1 = sum[2] * (1.0f / D_);
        const float v0 = __builtin_fmaxf(sum[1] * (1.0f / D_) - m0 * m0, 0.f), v1 = __builtin_fmaxf(sum[3] * (1.0f / D_) - m1 * m1, 0.f);
        *(LAS f32x4*)(smem + L_RST + lane * 16) = (f32x4){m0, __builtin_amdgcn_rsqf(v0 + LN_EPS), m1, __builtin_amdgcn_rsqf(v1 + LN_EPS)};
      }
      L.step();
      *(LAS f32x4*)(smem + L_CSS + lw * 1024 + lane * 16) = sum4(gt);
      L.step();
      if (lw == 0) *(LAS f32x4*)(smem + L_CS + lane * 16) = lds_sum4(smem + L_CSS + lane * 16);
      const int stag = (loc & 7) < 6 ? (loc & 7) : 6;
      L.steps(stag);
      touch_lines(smem, r2, bid * 28672, lane < 56 ? lw * 56 + lane : 4096, 224);
      L.step();
      L.step<12>();
      L.steps(6 - stag);
    }
    {
      relane();
      Loader<128, 4, 31, false, 1> L;
      L.kh = kh;
      L.init(smem, rws, r2, WS_H1 + tm * 112 * 8192, 0, gp * 128, D_ * 2, kh ? -1024 : 1024, lw, lane, gp & 7);
      L.issueB(0);
      wait_vm<11 + 4>(); lbar(); wait_vm<4>(); bar();
      bar();
      L.issueB(1); L.issueB(2); L.issueB(3);
      wait_vm<12>(); bar();
      if (wid == 4 && lane == 0) set_flag(ctl, C_H1 + tm, gp, tag);
      wait_vm<8>(); bar();
      wait_vm<4>(); bar();
      L.issueB(4);
      poll4(fp, smem, 3, lw, ctl, C_H1 + tm, kh * 8, 8, tag, lane);
      L.issueA(3); L.issueA(4);
      L.tn = 5;
      L.step<4>();
      fp.keep();
      L.steps(3);
      if (wid == 4 && lane == 0) set_flag(ctl, C_H1B + tm, gp, tag);
      L.steps(5);
      const unsigned seen = peek(ctl, C_H1B + tm, lane, 16);
      L.steps(3);
      confirm(seen, ctl, C_H1B + tm, tag, lane, 16);
      L.steps(8);
      L.init_half(gp * 128, D_ * 2, lw, lane);
      wait_vm<8>(); bar(); L.issueH(25);
      wait_vm<6>(); bar(); L.issueH(26);
#pragma unroll 1
      for (int t = 27; t <= 30; ++t) { wait_vm<6>(); bar(); L.issueH(t); }
      wait_vm<6>(); bar();
      wait_vm<0>(); bar();
      bar();
    }
  } else {
    const int wm = wid >> 1, wn = wid & 1;
    relane();
    wait_vm<0>();
    bar();
    {
      const int par = gp & 1;
      auto slab3 = [&](int a) { return wn * 4 + (a + par == 3 ? 0 : a + par); };
      auto ntile3 = [&](int j) { return j < 6 ? slab3(j >> 1) * 2 + (j & 1) : 6 + wn; };
      f32x4 acc[4][7];
      mma_loop<224, 3, 16, 4, 7, false, true>(smem, acc, wm * 64, lane, ntile3, []() {}, [](int) {});
      relane();
      f32x4 cs[7];
#pragma unroll
      for (int j = 0; j < 7; ++j) cs[j] = *(LAS const f32x4*)(smem + L_CS + (ntile3(j) * 16 + g * 4) * 4);
      float fa[4], fb[4];
#pragma unroll
      for (int i = 0; i < 4; ++i) {
        const f32x2 ms = *(LAS const f32x2*)(smem + L_RST + (wm * 64 + i * 16 + r16) * 8);
        fa[i] = ms[1]; fb[i] = -ms[0] * ms[1];
      }
      auto act = [&](int i, int j) {
        f32x4 y;
#pragma unroll
        for (int e = 0; e < 4; ++e) y[e] = __builtin_fmaxf(__builtin_fmaf(acc[i][j][e], fa[i], fb[i] * cs[j][e]), 0.f);
        return y;
      };
      const int hb = WS_H1 + (tm * 112 + gp * 7) * 8192;
#pragma unroll
      for (int a = 0; a < 3; ++a) {
        const int sl = slab3(a);
#pragma unroll
        for (int i = 0; i < 4; ++i) {
          f32x4 x = act(i, 2 * a), y = act(i, 2 * a + 1);
          swap_pair(x, y);
          const u32x4 o = pack_xy(x, y);
          const int ro = (wm * 64 + i * 16) * 64 + cswz;
          st128(o, rws, hb + sl * 8192 + ro);
          if (a < 2 || (unsigned)(sl - par) < 6u) *(LAS u32x4*)(smem + (sl - par) * 8192 + ro) = o;
        }
        asm volatile("" ::: "memory");
      }
#pragma unroll
      for (int i = 1; i < 4; i += 2) {
        f32x4 x = act(i - 1, 6), y = act(i, 6);
        swap_pair(x, y);
        const u32x4 o = pack_xy(x, y);
        const int r3 = (wm * 64 + (i - 1 + (g & 1)) * 16) * 64 + (2 * wn + (g >> 1)) * 256 + r16 * 16;
        st128(o, rws, hb + 3 * 8192 + r3);
        *(LAS u32x4*)(smem + (3 - par) * 8192 + r3) = o;
      }
      wait_vm<6>();
    }
    relane();
    {
      const int row = tm * 128 + wm * 64 + r16, col = gp * 64 + wn * 32 + cpair;
      u32x4 res[4], t[4];
      unsigned seen = 0;
      f32x4 acc[4][4];
      mma_loop<128, 4, 31, 4, 4, true, true>(smem, acc, wm * 64, lane, [&](int j) { return (j >> 1) * 4 + wn * 2 + (j & 1); }, [&]() {
        wait_vm<0>();
#pragma unroll
        for (int i = 0; i < 4; ++i) res[i] = ld128(rws, WS_H + (tm * 32 + gp * 2 + wn) * 8192 + (wm * 64 + i * 16) * 64 + cswz, 0);
      }, [&](int what) {
        if (what == 0) {
          const int pb = WS_P4 + (tm * 16 + gp) * 16384 + wid * 4096 + lane * 16;
#pragma unroll
          for (int i = 0; i < 4; ++i) st128(pack_xy(acc[i][2], acc[i][3]), rws, pb + i * 1024);
        } else if (what == 1) {
          wait_vm<0>();
          if (lane == 0) set_flag(ctl, C_P4 + tm, gp * 4 + wid, tag);
        } else if (what == 2) {
          seen = __hip_atomic_load(ctl + (C_P4 + tm) * 64 + (gp ^ 8) * 4 + wid, __ATOMIC_RELAXED, __HIP_MEMORY_SCOPE_AGENT);
        } else {
          asm volatile("" : "+v"(seen));
          if (!wave_all(seen == tag)) poll(ctl, C_P4 + tm, (gp ^ 8) * 4 + wid, 1, tag, lane);
          acquire();
          const int qb = WS_P4 + (tm * 16 + (gp ^ 8)) * 16384 + wid * 4096 + lane * 16;
#pragma unroll
          for (int i = 0; i < 4; ++i) t[i] = ld128(rws, qb + i * 1024, 0);
        }
      });
#pragma unroll
      for (int i = 0; i < 4; ++i) {
        f32x4 x = acc[i][0], y = acc[i][1];
        add_bf8(x, y, t[i]);
        swap_pair(x, y);
        add_bf8(x, y, res[i]);
        __builtin_amdgcn_raw_buffer_store_b128(pack_xy(x, y), mkrsrc(OUT, S_ * D_ * 2), ((row + i * 16) * D_ + col) * 2, 0, 18);
      }
    }
  }
#endif
}
#if defined(__HIP_DEVICE_COMPILE__)
#pragma clang attribute pop
#endif

extern "C" void kernel_launch(void* const* d_in, const int* in_sizes, int n_in, void* d_out, int out_size, void* d_ws, size_t ws_size,
                              hipStream_t stream) {
  (void)hipFuncSetAttribute((const void*)block_kernel, hipFuncAttributeMaxDynamicSharedMemorySize, LDS_BYTES);
  block_kernel<<<256, 512, LDS_BYTES, stream>>>((const u16*)d_in[0], (const u16*)d_in[1], (const u16*)d_in[2], (const u16*)d_in[3],
                                                (const u16*)d_in[5], (const u16*)d_in[6], (u16*)d_out, (char*)d_ws);
}
```

```cpp
#include <hip/hip_runtime.h>
#include <hip/hip_bf16.h>
#include <type_traits>

#define LAS __attribute__((address_space(3)))
#define GAS __attribute__((address_space(1)))
#define DI __device__ __forceinline__
typedef __bf16 bf16x8 __attribute__((ext_vector_type(8)));
typedef __bf16 bf16x2 __attribute__((ext_vector_type(2)));
typedef short s16x4 __attribute__((ext_vector_type(4)));
typedef short s16x8 __attribute__((ext_vector_type(8)));
typedef float f32x2 __attribute__((ext_vector_type(2)));
typedef float f32x4 __attribute__((ext_vector_type(4)));
typedef unsigned u32x2 __attribute__((ext_vector_type(2)));
typedef unsigned u32x4 __attribute__((ext_vector_type(4)));
typedef unsigned short u16;

constexpr int S_ = 2048, D_ = 1024, FF_ = 3584;
constexpr float LN_EPS = 1e-5f;

constexpr int C_EPOCH = 0, C_TMO = 1, C_ST = 2, C_CS1 = 18, C_CS3 = 34, C_KV = 50, C_O = 66, C_H = 82, C_H1 = 98, C_P4 = 114, C_H1B = 130, C_LINES = 146;
constexpr unsigned WS_XST = 40960;
constexpr unsigned WS_CS1 = WS_XST + S_ * 8;
constexpr unsigned WS_CS3 = WS_CS1 + 16 * 16 * 192 * 4;
constexpr unsigned WS_HST = WS_CS3 + 16 * 16 * 224 * 4;
constexpr unsigned KVP_BYTES = 8192 + 512;
constexpr unsigned WS_KVP = WS_HST + S_ * 16 * 8;
constexpr unsigned WS_O = WS_KVP + 256 * KVP_BYTES;
constexpr unsigned WS_H = WS_O + S_ * D_ * 2;
constexpr unsigned WS_H1 = WS_H + S_ * D_ * 2;
constexpr unsigned WS_P4 = WS_O, WS_END = WS_H1 + S_ * FF_ * 2;
static_assert(C_LINES * 256 <= (int)WS_XST && WS_O % 256 == 0 && WS_KVP % 256 == 0, "");

constexpr int L_WT = 120 * 1024;
constexpr int L_TAB = 149 * 1024;
constexpr int L_DUMMY = L_TAB;
constexpr int L_RST = L_TAB + 256;
constexpr int L_CS = L_RST + 1024;
constexpr int L_CSS = L_CS + 1024;
constexpr int L_GO = L_CSS + 8192;
constexpr int LDS_BYTES = L_GO + 16;
static_assert(LDS_BYTES <= 160 * 1024, "");

#if defined(__HIP_DEVICE_COMPILE__)
#pragma clang attribute push(__attribute__((target("no-packed-fp32-ops"))), apply_to = function)
typedef __amdgpu_buffer_rsrc_t rsrc_t;
DI float u2f(unsigned u) { return __builtin_bit_cast(float, u); }
DI unsigned f2u(float f) { return __builtin_bit_cast(unsigned, f); }
DI bool wave_all(bool p) { return __builtin_amdgcn_ballot_w64(p) == ~0ull; }
#define CODE_AHEAD 52224
#define PSLEEP "2"
#define WO1 9
#define BDEPTH 3
#define PFD 8
#define B3DELAY 48
#define CPW 0
constexpr int SC1 = 16;
template <int N> DI void wait_vm() { asm volatile("s_waitcnt vmcnt(%0)" ::"n"(N) : "memory"); }
DI void wait_lgkm0() { asm volatile("" ::: "memory"); __builtin_amdgcn_s_waitcnt(0xC07F); asm volatile("" ::: "memory"); }
DI void bar() { asm volatile("" ::: "memory"); __builtin_amdgcn_s_barrier(); asm volatile("" ::: "memory"); }
DI void lbar() { wait_lgkm0(); bar(); }
DI unsigned pk2(float a, float b) { f32x2 v = {a, b}; return __builtin_bit_cast(unsigned, __builtin_convertvector(v, bf16x2)); }
DI float bflo(unsigned u) { return u2f(u << 16); }
DI float bfhi(unsigned u) { return u2f(u & 0xffff0000u); }
DI bf16x8 ld_b128(LAS const char* p) { return *(LAS const bf16x8*)p; }
DI bf16x8 ld_tr2(LAS const char* p0, LAS const char* p1) {
  s16x4 a = __builtin_amdgcn_ds_read_tr16_b64_v4i16((LAS s16x4*)p0);
  s16x4 b = __builtin_amdgcn_ds_read_tr16_b64_v4i16((LAS s16x4*)p1);
  return __builtin_bit_cast(bf16x8, __builtin_shufflevector(a, b, 0, 1, 2, 3, 4, 5, 6, 7));
}
DI float x32_sum(float v) { auto r = __builtin_amdgcn_permlane32_swap(f2u(v), f2u(v), false, false); return u2f(r[0]) + u2f(r[1]); }
DI float x16_sum(float v) { auto r = __builtin_amdgcn_permlane16_swap(f2u(v), f2u(v), false, false); return u2f(r[0]) + u2f(r[1]); }
template <int CTRL> DI float dpp_add(float v) { return v + u2f(__builtin_amdgcn_update_dpp(0, (int)f2u(v), CTRL, 0xf, 0xf, true)); }
DI float half_sum(float v) {
  v = dpp_add<0xB1>(v);
  v = dpp_add<0x4E>(v);
  v = dpp_add<0x141>(v);
  v = dpp_add<0x140>(v);
  return x16_sum(v);
}
#define MFMA16(a, b, c) __builtin_amdgcn_mfma_f32_16x16x32_bf16((a), (b), (c), 0, 0, 0)
DI rsrc_t mkrsrc(const void* p, unsigned bytes) { return __builtin_amdgcn_make_buffer_rsrc((void*)p, 0, bytes, 0x00020000); }
DI void st128(u32x4 v, rsrc_t r, int off) { __builtin_amdgcn_raw_buffer_store_b128(v, r, off, 0, SC1); }
DI u32x4 ld128(rsrc_t r, int off, int soff) { return __builtin_amdgcn_raw_buffer_load_b128(r, off, soff, SC1); }

DI void set_flag(GAS unsigned* ctl, int line, int word, unsigned tag) { __hip_atomic_store(ctl + line * 64 + word, tag, __ATOMIC_RELAXED, __HIP_MEMORY_SCOPE_AGENT); }
DI void acquire() {
  asm volatile("" ::: "memory");
}
DI void poll(GAS unsigned* ctl, int line, int lo, int n, unsigned tag, int lane) {
  GAS unsigned* p = ctl + line * 64 + lo + (lane < n ? lane : 0);
  unsigned spins = 0;
  while (!wave_all(__hip_atomic_load(p, __ATOMIC_RELAXED, __HIP_MEMORY_SCOPE_AGENT) == tag)) {
    __builtin_amdgcn_s_sleep(1);
    if (++spins > (1u << 17)) { if (lane == 0) __hip_atomic_store(ctl + C_TMO * 64, (unsigned)line, __ATOMIC_RELAXED, __HIP_MEMORY_SCOPE_AGENT); break; }
  }
  acquire();
}
struct FastPoll {
  unsigned a, b, c, d;
  DI void run(GAS unsigned* ctl, int line, int lo, int n, unsigned tag, int lane) {
    GAS unsigned* p = ctl + line * 64 + lo + (lane < n ? lane : 0);
    unsigned cnt = 1u << 17;
#define FP_LD(r) "global_load_dword %" #r ", %5, off sc1\n\ts_sleep " PSLEEP "\n\t"
#define FP_CK(r) "s_waitcnt vmcnt(3)\n\tv_cmp_eq_u32 vcc, %6, %" #r "\n\ts_cmp_eq_u64 vcc, -1\n\ts_cbranch_scc1 2f\n\t" FP_LD(r)
    asm volatile(FP_LD(0) FP_LD(1) FP_LD(2) FP_LD(3) "1:\n\t" FP_CK(0) FP_CK(1) FP_CK(2) FP_CK(3)
                 "s_sub_u32 %4, %4, 1\n\ts_cbranch_scc0 1b\n\t2:"
                 : "=&v"(a), "=&v"(b), "=&v"(c), "=&v"(d), "+s"(cnt) : "v"(p), "s"(tag) : "vcc", "scc", "memory");
#undef FP_LD
#undef FP_CK
  }
  DI void keep() const { asm volatile("" :: "v"(a), "v"(b), "v"(c), "v"(d)); }
};
DI void poll4(FastPoll& fp, LAS char* smem, unsigned go, int lw, GAS unsigned* ctl, int line, int lo, int n, unsigned tag, int lane) {
  LAS volatile unsigned* w = (LAS volatile unsigned*)(smem + L_GO);
  if (lw == 0) {
    fp.run(ctl, line, lo, n, tag, lane);
    *w = go;
  } else {
    fp.a = fp.b = fp.c = fp.d = 0u;
    unsigned spins = 0;
    while (__builtin_amdgcn_readfirstlane(*w) != go && ++spins < (1u << 24)) __builtin_amdgcn_s_sleep(1);
  }
  asm volatile("" ::: "memory");
}

template <int BN> DI constexpr int bswz(int k) {
  return BN == 224 ? (((k >> 3) & 1) << 1)
       : BN == 128 ? (((k & 3) | (((k >> 3) & 1) << 2)) << 1)
                   : ((((k >> 1) & 1) | (((k >> 3) & 1) << 1)) << 1);
}

template <int BN, int NS> DI int bslot(int s) {
  if (BN == 224) return ((0x4c3078 >> (8 * s)) & 255) << 10;
  if (BN == 128) return ((0x78504068 >> (8 * s)) & 255) << 10;
  return NS * 16384 + s * BN * 128;
}
DI constexpr int wo_slot(int t) { return t < 5 ? 81920 + t * 8192 : t < 15 ? (t - 5) * 8192 : L_CSS; }

template <int BN, int NS, int NT, bool AROW, int LAG = 0>
struct Loader {
  static constexpr int PA = 4, PB = BN / 32, PW = PA + PB, BST = BN * 128, CPR = BN / 8;
  static_assert(NS * (16384 + BST) <= L_TAB, "");
  LAS char* dst0; rsrc_t ra, rb; int va[PA], vb[PB], vh[2]; int ldb64, own0, tn;
  static constexpr int NOWN = BN == 128 ? 3 : BN == 224 ? 1 : 0;
  int kh;
  DI int ktile(int t) const {
    if (AROW) return (t + own0) & (NT - 1);
    if (BN != 128) return t < NOWN ? own0 + t : (t - NOWN < own0 ? t - NOWN : t);
    const int pi = t < 17 ? (t - 3) >> 1 : t - 17, p = t < 3 ? own0 : pi + (pi >= own0 ? 1 : 0);
    const int k = t >= 24 ? 7 * ((t >= 28 ? t - 3 : t) - 24) + 3 : ((7 * p + 1) >> 1) + (t < 3 ? t : t < 17 ? 2 * ((t - 3) & 1) : 1);
    return k + ((t >= 28) != (kh != 0) ? 28 : 0);
  }
  DI void issueA(int t) {
    const int kk = ktile(t), slot = t % NS;
#pragma unroll
    for (int i = 0; i < PA; ++i)
      __builtin_amdgcn_raw_ptr_buffer_load_lds(ra, dst0 + slot * 16384 + i * 4096, 16, va[i], kk * (AROW ? 128 : 16384), 0, AROW ? 0 : SC1);
  }
  DI void issueB(int t) {
    const int kk = ktile(t), slot = t % NS;
#pragma unroll
    for (int i = 0; i < PB; ++i) __builtin_amdgcn_raw_ptr_buffer_load_lds(rb, dst0 + bslot<BN, NS>(slot) + i * 4096, 16, vb[i], kk * ldb64, 0, 0);
  }
  DI void issueH(int t) {
    issueA(t);
    const int kk = ktile(t), slot = t % NS;
#pragma unroll
    for (int i = 0; i < 2; ++i) __builtin_amdgcn_raw_ptr_buffer_load_lds(rb, dst0 + bslot<BN, NS>(slot) + i * 4096, 16, vh[i], kk * ldb64, 0, 0);
  }
  DI void init(LAS char* smem, rsrc_t ra_, rsrc_t rb_, int a_off, int lda_b, int b_off, int ldb_b, int segb, int lw, int lane, int own) {
    dst0 = smem + lw * 1024; ra = ra_; rb = rb_; ldb64 = 64 * ldb_b; own0 = own; tn = NS - 1;
#pragma unroll
    for (int i = 0; i < PA; ++i) {
      const int ins = lw + i * 4, r = ins * 8 + (lane >> 3), c = (lane & 7) ^ ((r >> 1) & 7);
      va[i] = AROW ? a_off + r * lda_b + c * 16 : a_off + ins * 1024 + lane * 16;
    }
#pragma unroll
    for (int i = 0; i < PB; ++i) {
      const int ch = (lw + i * 4) * 64 + lane, k = ch / CPR, c = (ch % CPR) ^ bswz<BN>(k);
      vb[i] = b_off + k * ldb_b + (c >> 3) * segb + (c & 7) * 16;
    }
  }
  DI void init_half(int b_off, int ldb_b, int lw, int lane) {
#pragma unroll
    for (int i = 0; i < 2; ++i) { const int k = (lw + i * 4) * 8 + (lane >> 3); vh[i] = b_off + k * ldb_b + (((lane & 7) ^ bswz<64>(k)) * 16); }
  }
  template <int ALLOW = (NS - 2 - LAG) * PW> DI void step() { wait_vm<ALLOW>(); lbar(); issueA(tn); issueB(tn); ++tn; }
  DI void steps(int n) {
#pragma unroll 1
    for (int t = 0; t < n; ++t) step();
  }
  DI void finish() {
    if constexpr (NS - LAG >= 5) { wait_vm<3 * PW>(); lbar(); }
    if constexpr (NS - LAG >= 4) { wait_vm<2 * PW>(); lbar(); }
    if constexpr (NS - LAG >= 3) { wait_vm<1 * PW>(); lbar(); }
    wait_vm<0>(); lbar();
  }
};
template <int BN, int I0, int I1> DI void load_wtile(LAS char* dst, rsrc_t r, int off, int ldb_b, int segb, int lw, int lane) {
#pragma unroll
  for (int i = I0; i < I1; ++i) {
    const int ch = (lw + i * 4) * 64 + lane, k = ch / (BN / 8), c = (ch % (BN / 8)) ^ bswz<BN>(k);
    __builtin_amdgcn_raw_ptr_buffer_load_lds(r, dst + (lw + i * 4) * 1024, 16, off + k * ldb_b + (c >> 3) * segb + (c & 7) * 16, 0, 0, 0);
  }
}
template <int T0, int T1> DI void load_wo(LAS char* smem, rsrc_t ro, int gp, int lw, int lane) {
  int vo[2];
#pragma unroll
  for (int i = 0; i < 2; ++i) { const int k = (lw + i * 4) * 8 + (lane >> 3); vo[i] = gp * 128 + k * (D_ * 2) + (((lane & 7) ^ bswz<64>(k)) * 16); }
#pragma unroll
  for (int t = T0; t < T1; ++t)
#pragma unroll
    for (int i = 0; i < 2; ++i) __builtin_amdgcn_raw_ptr_buffer_load_lds(ro, smem + wo_slot(t) + (lw + i * 4) * 1024, 16, vo[i], t * 64 * (D_ * 2), 0, 0);
}
DI int opaque(int v) { asm volatile("" : "+v"(v)); return v; }

DI void touch_lines(LAS char* smem, rsrc_t r, int base, int line, int nlines) {
  __builtin_amdgcn_raw_ptr_buffer_load_lds(r, smem + L_DUMMY, 4, line < nlines ? base + line * 128 : (int)0x80000000, 0, 0, 0);
}

template <int BN> DI void colsum_tile(LAS char* img, rsrc_t rws, unsigned off, int lw, int lane) {
  constexpr int RS = BN * 2;
  const int r16 = lane & 15, g = lane >> 4, q = (lane & 15) >> 2, p = lane & 3;
  const s16x8 one_ = {0x3f80, 0x3f80, 0x3f80, 0x3f80, 0x3f80, 0x3f80, 0x3f80, 0x3f80};
  LAS const char* rd[4];
#pragma unroll
  for (int i = 0; i < 4; ++i) {
    const int nt = lw + 4 * i < BN / 16 ? lw + 4 * i : lw;
    rd[i] = img + (8 * g + q) * RS + (((2 * nt + (p >> 1)) ^ bswz<BN>(8 * g + q)) * 16) + 8 * (p & 1);
  }
  s16x4 v[4][4];
#define TR4(a, b, c, d, r) "ds_read_b64_tr_b16 %" #a ", %" #r "\n\tds_read_b64_tr_b16 %" #b ", %" #r " offset:%c20\n\t" \
                           "ds_read_b64_tr_b16 %" #c ", %" #r " offset:%c21\n\tds_read_b64_tr_b16 %" #d ", %" #r " offset:%c22\n\t"
  asm volatile(TR4(0, 1, 2, 3, 16) TR4(4, 5, 6, 7, 17) TR4(8, 9, 10, 11, 18) TR4(12, 13, 14, 15, 19) "s_waitcnt lgkmcnt(0)"
               : "=&v"(v[0][0]), "=&v"(v[0][1]), "=&v"(v[0][2]), "=&v"(v[0][3]), "=&v"(v[1][0]), "=&v"(v[1][1]), "=&v"(v[1][2]), "=&v"(v[1][3]),
                 "=&v"(v[2][0]), "=&v"(v[2][1]), "=&v"(v[2][2]), "=&v"(v[2][3]), "=&v"(v[3][0]), "=&v"(v[3][1]), "=&v"(v[3][2]), "=&v"(v[3][3])
               : "v"(rd[0]), "v"(rd[1]), "v"(rd[2]), "v"(rd[3]), "i"(4 * RS), "i"(32 * RS), "i"(36 * RS) : "memory");
#undef TR4
  f32x4 c[4];
#pragma unroll
  for (int i = 0; i < 4; ++i) {
    c[i] = (f32x4){0.f, 0.f, 0.f, 0.f};
    c[i] = MFMA16(__builtin_bit_cast(bf16x8, __builtin_shufflevector(v[i][0], v[i][1], 0, 1, 2, 3, 4, 5, 6, 7)), __builtin_bit_cast(bf16x8, one_), c[i]);
    c[i] = MFMA16(__builtin_bit_cast(bf16x8, __builtin_shufflevector(v[i][2], v[i][3], 0, 1, 2, 3, 4, 5, 6, 7)), __builtin_bit_cast(bf16x8, one_), c[i]);
  }
  const f32x4 out = r16 == 0 ? c[0] : r16 == 1 ? c[1] : r16 == 2 ? c[2] : c[3];
  st128(__builtin_bit_cast(u32x4, out), rws, lw + 4 * r16 < BN / 16 ? off + (lw + 4 * r16) * 64 + g * 16 : 0x80000000u);
}
DI f32x4 sum4(u32x4 (&t)[4]) {
  asm volatile("" : "+v"(t[0]), "+v"(t[1]), "+v"(t[2]), "+v"(t[3]));
  return (__builtin_bit_cast(f32x4, t[0]) + __builtin_bit_cast(f32x4, t[1])) + (__builtin_bit_cast(f32x4, t[2]) + __builtin_bit_cast(f32x4, t[3]));
}
DI f32x4 lds_sum4(LAS const char* p) {
  return (*(LAS const f32x4*)p + *(LAS const f32x4*)(p + 1024)) + (*(LAS const f32x4*)(p + 2048) + *(LAS const f32x4*)(p + 3072));
}
DI unsigned peek(GAS unsigned* ctl, int line, int lane, int n = 64) { return __hip_atomic_load(ctl + line * 64 + (lane & (n - 1)), __ATOMIC_RELAXED, __HIP_MEMORY_SCOPE_AGENT); }
DI void confirm(unsigned seen, GAS unsigned* ctl, int line, unsigned tag, int lane, int n = 64) {
  asm volatile("" : "+v"(seen));
  if (!wave_all(seen == tag)) poll(ctl, line, 0, n, tag, lane);
  else acquire();
}

DI void mfma16(f32x4& c, const bf16x8& a, const bf16x8& b) { asm volatile("v_mfma_f32_16x16x32_bf16 %0, %1, %2, %0" : "+v"(c) : "v"(a), "v"(b)); }
DI void mfma_drain() { asm volatile("s_nop 7\n\ts_nop 7" ::: "memory"); }
template <int TM, int TN> struct Frag { s16x4 b[TN][2]; bf16x8 a[TM]; };
template <int BN, int NS, int NT, int TM, int TN, bool DEEP, bool OWN, class NMap, class Mid, class Send>
DI void mma_loop(LAS char* smem, f32x4 (&acc)[TM][TN], int row0, int lane, NMap&& nt, Mid&& mid, Send&& send) {
  constexpr int RS = BN * 2, BST = BN * 128, NL = TM + 2 * TN, NM = TM * TN;
  typedef Frag<TM, TN> F;
  const int r16 = lane & 15, g = lane >> 4, q = (lane & 15) >> 2, p = lane & 3;
  constexpr bool AROW = BN == 192;
  const int aoff = AROW ? (row0 + r16) * 128 + ((g ^ (r16 >> 1)) * 16) : row0 * 64 + lane * 16;
  const int aoff1 = AROW ? aoff ^ 64 : aoff + 8192;
  int boff[TN];
#pragma unroll
  for (int j = 0; j < TN; ++j) boff[j] = (8 * g + q) * RS + (((2 * nt(j) + (p >> 1)) ^ bswz<BN>(8 * g + q)) * 16) + 8 * (p & 1);
#pragma unroll
  for (int i = 0; i < TM; ++i)
#pragma unroll
    for (int j = 0; j < TN; ++j) { acc[i][j] = (f32x4){0.f, 0.f, 0.f, 0.f}; asm volatile("" : "+v"(acc[i][j])); }
  int boffh[2];
  auto ld1 = [&](F& f, int slot, int s, int k, int tn = TN) {
    if (k == 0 || k > 2 * tn) { const int i = k == 0 ? 0 : k - 2 * tn; f.a[i] = ld_b128(smem + slot * 16384 + (s ? aoff1 : aoff) + i * (AROW ? 2048 : 1024)); }
    else {
      const int j = (k - 1) >> 1, u = (k - 1) & 1;
      f.b[j][u] = __builtin_amdgcn_ds_read_tr16_b64_v4i16((LAS s16x4*)(smem + bslot<BN, NS>(slot) + (tn == TN ? boff[j] + (32 * s + 4 * u) * RS : boffh[j & 1] + (32 * s + 4 * u) * 128)));
    }
  };
  auto mm1 = [&](const F& f, int m, int tn = TN) {
    const int i = m / tn, j = m % tn;
    const bf16x8 b = __builtin_bit_cast(bf16x8, __builtin_shufflevector(f.b[j][0], f.b[j][1], 0, 1, 2, 3, 4, 5, 6, 7));
    if (AROW && j >= 4) mfma16(acc[i][j], f.a[i], b); else mfma16(acc[i][j], b, f.a[i]);
  };
  auto ldall = [&](F& f, int slot, int s) {
#pragma unroll
    for (int k = 0; k < NL; ++k) ld1(f, slot, s, k);
  };
  auto mmall = [&](const F& f) {
#pragma unroll
    for (int m = 0; m < NM; ++m) mm1(f, m);
  };
  auto sub = [&](const F& x, F& u, int slot, int s) {
    constexpr int SP = DEEP ? NM : (NL < NM ? NL : NM - 2);
#pragma unroll
    for (int m = 0; m < NM; ++m) {
      mm1(x, m);
#pragma unroll
      for (int k = (m * NL + SP - 1) / SP; k < NL && k < ((m + 1) * NL + SP - 1) / SP; ++k) ld1(u, slot, s, k);
      __builtin_amdgcn_sched_barrier(0);
    }
  };
  auto subh = [&](const F& x, F& u, int slot, int s, auto tx, auto tu) {
    constexpr int TX = decltype(tx)::value, TU = decltype(tu)::value, M = TM * TX, LN = TM + 2 * TU;
#pragma unroll
    for (int m = 0; m < M; ++m) {
      mm1(x, m, TX);
#pragma unroll
      for (int k = (m * LN + M - 1) / M; k < LN && k < ((m + 1) * LN + M - 1) / M; ++k) ld1(u, slot, s, k, TU);
      __builtin_amdgcn_sched_barrier(0);
    }
  };
  auto nxt = [](int s) { return s == NS - 1 ? 0 : s + 1; };
  lbar();
  int slot = 0;
  if constexpr (!DEEP) {
    constexpr int NB = AROW ? 2 : TN, NA = AROW ? 2 : 1;
    LAS char* ca[NA];
    LAS char* cb[NB];
    ca[0] = smem + aoff;
    if constexpr (AROW) ca[NA - 1] = smem + aoff1;
#pragma unroll
    for (int j = 0; j < NB; ++j) cb[j] = smem + bslot<BN, NS>(0) + boff[j];
    auto ldc = [&](F& f, int s, int k) {
      if (k == 0 || k > 2 * TN) { const int i = k == 0 ? 0 : k - 2 * TN; f.a[i] = ld_b128(AROW ? ca[s] + i * 2048 : ca[0] + s * 8192 + i * 1024); }
      else {
        const int j = (k - 1) >> 1, u = (k - 1) & 1;
        f.b[j][u] = __builtin_amdgcn_ds_read_tr16_b64_v4i16((LAS s16x4*)((AROW ? cb[j & 1] + (j >> 1) * 128 : cb[j]) + (32 * s + 4 * u) * RS));
      }
    };
    auto subc = [&](const F& x, F& u, int s, bool adv) {
      const int ns = nxt(slot), da = (ns - slot) * 16384, db = bslot<BN, NS>(ns) - bslot<BN, NS>(slot);
#pragma unroll
      for (int m = 0; m < NM; ++m) {
        mm1(x, m);
        if (m < NL) ldc(u, s, m);
        else if (adv && m - NL < NA) asm volatile("v_add_u32 %0, %1, %0" : "+v"(ca[m - NL < NA ? m - NL : 0]) : "s"(da));
        else if (adv && m - NL - NA < NB) asm volatile("v_add_u32 %0, %1, %0" : "+v"(cb[m - NL - NA < NB ? m - NL - NA : 0]) : "s"(db));
        __builtin_amdgcn_sched_barrier(0);
      }
      if (adv) slot = ns;
    };
    static_assert(NL + NA + NB <= NM, "");
    F f0, f1;
#pragma unroll
    for (int k = 0; k < NL; ++k) ldc(f0, 0, k);
    if constexpr (OWN) {
      subc(f0, f1, 1, true);
      mmall(f1);
      __builtin_amdgcn_sched_barrier(0);
      bar();
#pragma unroll
      for (int k = 0; k < NL; ++k) ldc(f0, 0, k);
    }
#pragma unroll 1
    for (int t = OWN ? 1 : 0; t < NT - 1; ++t) {
      subc(f0, f1, 1, true);
      lbar();
      subc(f1, f0, 0, false);
    }
    subc(f0, f1, 1, false);
    lbar();
    mmall(f1);
  } else {
    F f0, f1, g0, g1;
    auto tile = [&](const F& x0, const F& x1, F& u0, F& u1, int t) {
      bar();
      slot = nxt(slot);
      sub(x0, u0, slot, 0);
      sub(x1, u1, slot, 1);
    };
    ldall(f0, 0, 0); ldall(f1, 0, 1);
    int t0 = 0;
    if constexpr (OWN) {
      tile(f0, f1, g0, g1, 0);
      tile(g0, g1, f0, f1, 1);
      mmall(f0); mmall(f1);
      __builtin_amdgcn_sched_barrier(0);
      lbar();
      slot = 3;
      ldall(g0, 3, 0); ldall(g1, 3, 1);
      tile(g0, g1, f0, f1, 3);
      t0 = 4;
    }
    if constexpr (!OWN) {
#pragma unroll 1
      for (int t = t0; t < NT - 2; t += 2) {
        tile(f0, f1, g0, g1, t);
        tile(g0, g1, f0, f1, t + 1);
      }
      tile(f0, f1, g0, g1, NT - 2);
      lbar();
      mmall(g0); mmall(g1);
    } else {
#pragma unroll 1
      for (int t = t0; t < 24; t += 2) {
        tile(f0, f1, g0, g1, t);
        if (t == 4) mid();
        tile(g0, g1, f0, f1, t + 1);
      }
      typedef std::integral_constant<int, TN> Full;
      typedef std::integral_constant<int, TN / 2> Half;
      auto tileh = [&](const F& x0, const F& x1, F& u0, F& u1, auto tx) {
        bar();
        slot = nxt(slot);
        subh(x0, u0, slot, 0, tx, Half{});
        subh(x1, u1, slot, 1, tx, Half{});
      };
#pragma unroll
      for (int j = 0; j < 2; ++j) {
        const int l2 = opaque(lane), k = 8 * (l2 >> 4) + ((l2 & 15) >> 2);
        boffh[j] = k * 128 + (((2 * nt(j) + ((l2 & 3) >> 1)) ^ bswz<64>(k)) * 16) + 8 * (l2 & 1);
      }
      tileh(f0, f1, g0, g1, Full{});
      mfma_drain();
#pragma unroll
      for (int i = 0; i < TM; ++i)
#pragma unroll
        for (int j = TN / 2; j < TN; ++j) asm volatile("" : "+v"(acc[i][j]));
      send(0);
      __builtin_amdgcn_sched_barrier(0);
      tileh(g0, g1, f0, f1, Half{});
      tileh(f0, f1, g0, g1, Half{});
      send(1);
      tileh(g0, g1, f0, f1, Half{});
      tileh(f0, f1, g0, g1, Half{});
      send(2);
      tileh(g0, g1, f0, f1, Half{});
      send(3);
      lbar();
#pragma unroll
      for (int m = 0; m < TM * TN / 2; ++m) mm1(f0, m, TN / 2);
#pragma unroll
      for (int m = 0; m < TM * TN / 2; ++m) mm1(f1, m, TN / 2);
    }
  }
  mfma_drain();
#pragma unroll
  for (int i = 0; i < TM; ++i)
#pragma unroll
    for (int j = 0; j < TN; ++j) asm volatile("" : "+v"(acc[i][j]));
}
DI void swap_pair(f32x4& x, f32x4& y) {
#pragma unroll
  for (int e = 0; e < 4; ++e) {
    auto r = __builtin_amdgcn_permlane16_swap(f2u(x[e]), f2u(y[e]), false, false);
    x[e] = u2f(r[0]); y[e] = u2f(r[1]);
  }
}
DI void add_bf8(f32x4& x, f32x4& y, const u32x4& rv) {
  x[0] += bflo(rv[0]); x[1] += bfhi(rv[0]); x[2] += bflo(rv[1]); x[3] += bfhi(rv[1]);
  y[0] += bflo(rv[2]); y[1] += bfhi(rv[2]); y[2] += bflo(rv[3]); y[3] += bfhi(rv[3]);
}
DI u32x4 pack_xy(const f32x4& x, const f32x4& y) { return (u32x4){pk2(x[0], x[1]), pk2(x[2], x[3]), pk2(y[0], y[1]), pk2(y[2], y[3])}; }

template <class Op> DI void red2(float& a, float& b, Op&& op) {
  auto r = __builtin_amdgcn_permlane16_swap(f2u(a), f2u(b), false, false);
  const float c = op(u2f(r[0]), u2f(r[1]));
  auto t = __builtin_amdgcn_permlane32_swap(f2u(c), f2u(c), false, false);
  const float d = op(u2f(t[0]), u2f(t[1]));
  auto u = __builtin_amdgcn_permlane16_swap(f2u(d), f2u(d), false, false);
  a = u2f(u[0]); b = u2f(u[1]);
}
DI void dual_softmax(const f32x4 (&p)[2][2], const float (&sc)[2], f32x4 (&e)[2][2][2]) {
  float mx[2], mn[2], s1[2], s2[2];
#pragma unroll
  for (int t = 0; t < 2; ++t) {
    mx[t] = p[t][0][0]; mn[t] = p[t][0][0];
#pragma unroll
    for (int r = 1; r < 8; ++r) { mx[t] = __builtin_fmaxf(mx[t], p[t][r >> 2][r & 3]); mn[t] = __builtin_fminf(mn[t], p[t][r >> 2][r & 3]); }
  }
  red2(mx[0], mx[1], [](float x, float y) { return __builtin_fmaxf(x, y); });
  red2(mn[0], mn[1], [](float x, float y) { return __builtin_fminf(x, y); });
#pragma unroll
  for (int t = 0; t < 2; ++t) {
    const float a = -mx[t] * sc[t], b = mn[t] * sc[t];
    s1[t] = 0.f; s2[t] = 0.f;
#pragma unroll
    for (int r = 0; r < 8; ++r) {
      const float x = __builtin_amdgcn_exp2f(__builtin_fmaf(p[t][r >> 2][r & 3], sc[t], a));
      const float y = __builtin_amdgcn_exp2f(__builtin_fmaf(p[t][r >> 2][r & 3], -sc[t], b));
      e[0][t][r >> 2][r & 3] = x; e[1][t][r >> 2][r & 3] = y;
      s1[t] += x; s2[t] += y;
    }
  }
  red2(s1[0], s1[1], [](float x, float y) { return x + y; });
  red2(s2[0], s2[1], [](float x, float y) { return x + y; });
#pragma unroll
  for (int t = 0; t < 2; ++t) {
    const float r1 = __builtin_amdgcn_rcpf(s1[t]), r2 = __builtin_amdgcn_rcpf(s2[t]);
#pragma unroll
    for (int r = 0; r < 8; ++r) { e[0][t][r >> 2][r & 3] *= r1; e[1][t][r >> 2][r & 3] *= r2; }
  }
}
DI bf16x8 pack_frag(const f32x4& x, const f32x4& y) { return __builtin_bit_cast(bf16x8, pack_xy(x, y)); }
DI void st4(LAS char* p, const f32x4& a, const f32x4& b, const f32x4& c, const f32x4& d) {
  *(LAS f32x4*)p = a; *(LAS f32x4*)(p + 1024) = b; *(LAS f32x4*)(p + 2048) = c; *(LAS f32x4*)(p + 3072) = d;
}
DI void ld4(LAS const char* p, f32x4 (&v)[2][2]) {
  v[0][0] = *(LAS const f32x4*)p; v[0][1] = *(LAS const f32x4*)(p + 1024); v[1][0] = *(LAS const f32x4*)(p + 2048); v[1][1] = *(LAS const f32x4*)(p + 3072);
}
#endif

__global__ __launch_bounds__(512, 2) void block_kernel(const u16* __restrict__ X, const u16* __restrict__ WQKV, const u16* __restrict__ WPHI,
                                                        const u16* __restrict__ WO, const u16* __restrict__ W1, const u16* __restrict__ W2,
                                                        u16* __restrict__ OUT, char* WS) {
#if defined(__HIP_DEVICE_COMPILE__)
  extern __shared__ __attribute__((aligned(16))) char smem_[];
  LAS char* smem = (LAS char*)smem_;
  const int tid = threadIdx.x, wid = __builtin_amdgcn_readfirstlane(tid >> 6);
  int lane = tid & 63;
  const int bid = blockIdx.x, xcd = bid & 7, loc = bid >> 3;
  const int tm = (xcd & 1) * 8 + (loc & 7), gp = (xcd >> 1) * 4 + (loc >> 3);
  const bool loader = wid >= 4;
  const int lw = wid & 3;
  GAS unsigned* ctl = (GAS unsigned*)WS;
  const rsrc_t rws = mkrsrc(WS, WS_END);
  if (wid == CPW) {
    unsigned long long pc;
    asm volatile("s_getpc_b64 %0" : "=s"(pc));
    touch_lines(smem, mkrsrc((const void*)(pc & ~127ull), CODE_AHEAD), loc * 2048, lane < 16 ? lane : 4096, 16);
  }
  unsigned epoch, tag;
  auto load_tag = [&]() { epoch = __hip_atomic_load(ctl + C_EPOCH * 64, __ATOMIC_RELAXED, __HIP_MEMORY_SCOPE_AGENT); };
  auto read_tag = [&]() { asm volatile("" : "+v"(epoch)); tag = __builtin_amdgcn_readfirstlane(epoch) + 1u; };

  int r16, g, cpair, cswz;
  auto relane = [&]() {
    lane = opaque(lane);
    r16 = lane & 15; g = lane >> 4;
    cpair = (g & 1) * 16 + (g >> 1) * 8;
    cswz = (cpair >> 3) * 256 + r16 * 16;
  };
  {
    constexpr int NT = 16, NS = 3;
    constexpr int L_PHI = 0;
    constexpr int L_XV = 2 * 17408;
    constexpr int L_VF = L_XV + 16384;
    constexpr int L_XQ = L_VF + 8192;
    constexpr int L_KVT = L_XQ;
    constexpr int L_WF = L_XQ + 16384;
    static_assert(L_WF + 4096 <= 5 * 16384, "phase 2's B ring is filled during this epilogue");
    relane();
    const int wm = (wid >> 1) & 1, hh = wid & 1, mi = wid >> 2;
    const int head = gp * 2 + hh;
    const unsigned kvp_grp = WS_KVP + gp * 16 * KVP_BYTES;
    LAS char* const xk = smem + L_PHI + hh * 17408 + (wm * 64 + 32) * 136 + lane * 16;
    bf16x8 wf[2];
    f32x4 fq[2][2], fk[2][2], fv[2][2];
    if (loader) {
      const rsrc_t rb = mkrsrc(WQKV, D_ * 3 * D_ * 2), ro = mkrsrc(WO, D_ * D_ * 2);
      Loader<192, NS, NT, true> L;
      const rsrc_t rx = mkrsrc(X, S_ * D_ * 2);
      L.init(smem, rx, rb, tm * 128 * D_ * 2, D_ * 2, gp * 128, 3 * D_ * 2, 2048, lw, lane, (tm & 3) * 4);
      L.issueA(0); L.issueB(0); L.issueA(1); L.issueB(1);
      if (lw == 0) *(LAS volatile unsigned*)(smem + L_GO) = 0u;
      load_tag();
      __builtin_amdgcn_s_setprio(3);
      auto cs1 = [&](int t) { if ((tm >> 2) == t) colsum_tile<192>(smem + NS * 16384 + (t % NS) * 24576, rws, WS_CS1 + (gp * 16 + tm) * 768, lw, lane); };
      L.step();
      cs1(0);
      const int xrow = tm * 128 + gp * 8 + lw * 2 + (lane >> 5);
      u32x4 xv[4];
#pragma unroll
      for (int j = 0; j < 4; ++j) xv[j] = __builtin_amdgcn_raw_buffer_load_b128(rx, xrow * (D_ * 2) + (lane & 31) * 16, j * 512, 0);
      L.step<14>();
      cs1(1);
      L.step<14>();
      cs1(2);
      L.step();
      cs1(3);
      L.step();
      read_tag();
      asm volatile("" : "+v"(xv[0]), "+v"(xv[1]), "+v"(xv[2]), "+v"(xv[3]));
      float sum = 0.f, qq = 0.f;
#pragma unroll
      for (int i = 0; i < 16; ++i) sum += bflo(xv[i >> 2][i & 3]) + bfhi(xv[i >> 2][i & 3]);
      float mean = half_sum(sum) * (1.0f / D_);
      asm volatile("" : "+v"(mean));
      L.step();
      asm volatile("" : "+v"(xv[0]), "+v"(xv[1]), "+v"(xv[2]), "+v"(xv[3]));
#pragma unroll
      for (int i = 0; i < 16; ++i) {
        const float d0 = bflo(xv[i >> 2][i & 3]) - mean, d1 = bfhi(xv[i >> 2][i & 3]) - mean;
        qq = __builtin_fmaf(d0, d0, __builtin_fmaf(d1, d1, qq));
      }
      const float rstd = __builtin_amdgcn_rsqf(half_sum(qq) * (1.0f / D_) + LN_EPS);
      __builtin_amdgcn_raw_buffer_store_b64(__builtin_bit_cast(u32x2, (f32x2){mean, rstd}), rws, (lane & 31) == 0 ? WS_XST + xrow * 8 : 0x80000000u, 0, SC1);
      L.step();
      L.step<0>();
      if (lane == 0) { set_flag(ctl, C_CS1 + gp, tm * 4 + lw, tag); set_flag(ctl, C_ST + tm, gp * 4 + lw, tag); }
      touch_lines(smem, ro, bid * 8192, lane < 16 ? lw * 16 + lane : 4096, 64);
      L.step();
      L.step<13>();
      L.step();
      const unsigned seen = peek(ctl, C_CS1 + gp, lane), seen2 = peek(ctl, C_ST + tm, lane);
      L.steps(2);
      confirm(seen, ctl, C_CS1 + gp, tag, lane);
      confirm(seen2, ctl, C_ST + tm, tag, lane);
      u32x4 gt[4];
#pragma unroll
      for (int j = 0; j < 4; ++j) gt[j] = ld128(rws, WS_CS1 + (gp * 16 + lw * 4) * 768 + lane * 16, j * 768);
      u32x4 xs = ld128(rws, WS_XST + tm * 1024 + lane * 16, 0);
      L.step();
      wait_vm<10>(); lbar();
      *(LAS f32x4*)(smem + L_CSS + lw * 1024 + lane * 16) = sum4(gt);
      asm volatile("" : "+v"(xs));
      if (lw == 1) *(LAS u32x4*)(smem + L_RST + lane * 16) = xs;
      wait_vm<0>(); lbar();
      if (lw == 0) *(LAS f32x4*)(smem + L_CS + lane * 16) = lds_sum4(smem + L_CSS + lane * 16);
      const rsrc_t r1 = mkrsrc(W1, D_ * FF_ * 2);
      load_wtile<224, 0, 4>(smem + L_WT, r1, tm * 64 * (FF_ * 2) + gp * 448, FF_ * 2, 128, lw, lane);
      lbar();
      bar();
      __builtin_amdgcn_s_setprio(1);
      ld4(xk, fk);
      wf[0] = ld_b128(smem + L_WF + hh * 2048 + lane * 16);
      wf[1] = ld_b128(smem + L_WF + hh * 2048 + 1024 + lane * 16);
      ld4(smem + L_XV + lw * 4096 + lane * 16, fv);
    } else {
      {
#pragma unroll
        for (int t = 0; t < 2; ++t) {
          s16x8 w;
#pragma unroll
          for (int j = 0; j < 8; ++j) w[j] = (short)WPHI[head * 1024 + (16 * (j >> 2) + 4 * g + (j & 3)) * 32 + 16 * t + r16];
          wf[t] = __builtin_bit_cast(bf16x8, w);
        }
        load_tag();
        asm volatile("" : "+v"(wf[0]), "+v"(wf[1]));
      }
      f32x4 acc[4][6];
      mma_loop<192, NS, NT, 4, 6, false, false>(smem, acc, wm * 64, lane, [&](int j) { return (j >> 1) * 4 + hh * 2 + (j & 1); }, []() {}, [](int) {});
      read_tag();
      if (wm == 0) {
        *(LAS bf16x8*)(smem + L_WF + hh * 2048 + lane * 16) = wf[0];
        *(LAS bf16x8*)(smem + L_WF + hh * 2048 + 1024 + lane * 16) = wf[1];
      }
      st4(xk, acc[2][2], acc[2][3], acc[3][2], acc[3][3]);
      st4(smem + L_XV + wid * 4096 + lane * 16, acc[2][4], acc[2][5], acc[3][4], acc[3][5]);
      lbar();
      st4(smem + L_XQ + wid * 4096 + lane * 16, acc[2][0], acc[2][1], acc[3][0], acc[3][1]);
#pragma unroll
      for (int t = 0; t < 2; ++t)
#pragma unroll
        for (int u = 0; u < 2; ++u) { fq[t][u] = acc[t][u]; fk[t][u] = acc[t][2 + u]; fv[t][u] = acc[t][4 + u]; }
    }
    const int rbase = wm * 64 + mi * 32, q = r16 >> 2, p = lane & 3;
    LAS const float* cs = (LAS const float*)(smem + L_CS) + hh * 32;
    LAS const f32x2* rst = (LAS const f32x2*)(smem + L_RST) + rbase;
    auto feature = [&](const f32x4 (&x)[2][2], int part, f32x4 (&e)[2][2][2]) {
      const f32x4 c0 = *(LAS const f32x4*)(cs + part * 64 + 4 * g), c1 = *(LAS const f32x4*)(cs + part * 64 + 16 + 4 * g);
      f32x4 pp[2][2];
      float sc[2];
#pragma unroll
      for (int t = 0; t < 2; ++t) {
        const f32x2 ms = rst[t * 16 + r16];
        sc[t] = ms[1] * 1.4426950408889634f;
        f32x4 y0, y1;
#pragma unroll
        for (int e_ = 0; e_ < 4; ++e_) { y0[e_] = __builtin_fmaf(-ms[0], c0[e_], x[t][0][e_]); y1[e_] = __builtin_fmaf(-ms[0], c1[e_], x[t][1][e_]); }
        const bf16x8 yb = pack_frag(y0, y1);
        pp[t][0] = MFMA16(wf[0], yb, ((f32x4){0.f, 0.f, 0.f, 0.f}));
        pp[t][1] = MFMA16(wf[1], yb, ((f32x4){0.f, 0.f, 0.f, 0.f}));
      }
      dual_softmax(pp, sc, e);
    };
    {
      const float cv0 = cs[128 + r16], cv1 = cs[144 + r16];
#pragma unroll
      for (int t = 0; t < 2; ++t) {
        const f32x4 s01 = *(LAS const f32x4*)(rst + t * 16 + 4 * g), s23 = *(LAS const f32x4*)(rst + t * 16 + 4 * g + 2);
        const float mv[4] = {s01[0], s01[2], s23[0], s23[2]}, rv[4] = {s01[1], s01[3], s23[1], s23[3]};
#pragma unroll
        for (int e_ = 0; e_ < 4; ++e_) {
          fv[t][0][e_] = __builtin_fmaf(-mv[e_], cv0, fv[t][0][e_]) * rv[e_];
          fv[t][1][e_] = __builtin_fmaf(-mv[e_], cv1, fv[t][1][e_]) * rv[e_];
        }
      }
      LAS char* vd = smem + (loader ? L_XV + lw * 4096 : L_VF + lw * 2048) + lane * 16;
      *(LAS bf16x8*)vd = pack_frag(fv[0][0], fv[1][0]);
      *(LAS bf16x8*)(vd + 1024) = pack_frag(fv[0][1], fv[1][1]);
      f32x4 e[2][2][2];
      feature(fk, 1, e);
      LAS char* wr = smem + L_PHI + hh * 17408 + (rbase + r16) * 136 + g * 8;
#pragma unroll
      for (int w = 0; w < 2; ++w)
#pragma unroll
        for (int t = 0; t < 2; ++t)
#pragma unroll
          for (int u = 0; u < 2; ++u)
            *(LAS u32x2*)(wr + t * 16 * 136 + w * 64 + u * 32) = (u32x2){pk2(e[w][t][u][0], e[w][t][u][1]), pk2(e[w][t][u][2], e[w][t][u][3])};
    }
    lbar();
    __builtin_amdgcn_s_setprio(0);
    bf16x8 pf[2][2];
    auto qpath = [&]() {
      f32x4 e[2][2][2];
      feature(fq, 0, e);
#pragma unroll
      for (int t = 0; t < 2; ++t)
#pragma unroll
        for (int w = 0; w < 2; ++w) { pf[t][w] = pack_frag(e[w][t][0], e[w][t][1]); asm volatile("" : "+v"(pf[t][w])); }
    };
    if (loader) {
      ld4(smem + L_XQ + lw * 4096 + lane * 16, fq);
      qpath();
      load_wtile<224, 4, 7>(smem + L_WT, mkrsrc(W1, D_ * FF_ * 2), tm * 64 * (FF_ * 2) + gp * 448, FF_ * 2, 128, lw, lane);
    } else {
      f32x4 k00 = {0.f, 0.f, 0.f, 0.f}, k01 = k00, k10 = k00, k11 = k00, z0 = k00, z1 = k00;
      const s16x8 one_ = {0x3f80, 0x3f80, 0x3f80, 0x3f80, 0x3f80, 0x3f80, 0x3f80, 0x3f80};
      LAS const char* rd = smem + L_PHI + hh * 17408 + (4 * g + q) * 136 + wm * 64 + p * 8;
#pragma unroll
      for (int u = 0; u < 4; ++u) {
        const bf16x8 pa0 = ld_tr2(rd + 32 * u * 136, rd + (32 * u + 16) * 136), pa1 = ld_tr2(rd + 32 * u * 136 + 32, rd + (32 * u + 16) * 136 + 32);
        LAS const char* vp = smem + ((u & 1) ? L_XV + ((u >> 1) * 2 + hh) * 4096 : L_VF + ((u >> 1) * 2 + hh) * 2048) + lane * 16;
        const bf16x8 v0 = ld_b128(vp), v1 = ld_b128(vp + 1024);
        k00 = MFMA16(pa0, v0, k00); k10 = MFMA16(pa1, v0, k10);
        k01 = MFMA16(pa0, v1, k01); k11 = MFMA16(pa1, v1, k11);
        z0 = MFMA16(pa0, __builtin_bit_cast(bf16x8, one_), z0); z1 = MFMA16(pa1, __builtin_bit_cast(bf16x8, one_), z1);
      }
      const int kb = kvp_grp + tm * KVP_BYTES;
      st128(pack_xy(k00, k10), rws, kb + hh * 4096 + wm * 2048 + lane * 16);
      st128(pack_xy(k01, k11), rws, kb + hh * 4096 + wm * 2048 + 1024 + lane * 16);
      st128(__builtin_bit_cast(u32x4, r16 == 0 ? z0 : z1), rws, r16 < 2 ? kb + 8192 + hh * 256 + wm * 128 + g * 32 + r16 * 16 : (int)0x80000000);
      wait_vm<0>();
    }
    lbar();
    if (tid == 0) set_flag(ctl, C_KV + gp, tm, tag);
    if (loader) {
      load_wo<0, WO1>(smem, mkrsrc(WO, D_ * D_ * 2), gp, lw, lane);
    } else {
      qpath();
    }
    FastPoll fpk = {0u, 0u, 0u, 0u};
    if (wid == 0) {
      fpk.run(ctl, C_KV + gp, 0, 16, tag, lane);
    }
    bar();
    {
      u32x4 tz[16];
      if (wid == 7) {
#pragma unroll
        for (int j = 0; j < 16; ++j) tz[j] = ld128(rws, lane < 32 ? kvp_grp + 8192 + lane * 16 : 0x80000000u, j * KVP_BYTES);
      }
      u32x4 t[16];
#pragma unroll
      for (int j = 0; j < 16; ++j) t[j] = ld128(rws, kvp_grp + tid * 16, j * KVP_BYTES);
      if (wid == 7) {
        f32x4 z = __builtin_bit_cast(f32x4, tz[0]);
#pragma unroll
        for (int j = 1; j < 16; ++j) z += __builtin_bit_cast(f32x4, tz[j]);
        if (lane < 32) *(LAS u32x2*)(smem + L_KVT + 8192 + lane * 8) = (u32x2){pk2(z[0], z[1]), pk2(z[2], z[3])};
      }
      f32x4 x = {0.f, 0.f, 0.f, 0.f}, y = x;
#pragma unroll
      for (int j = 0; j < 16; ++j) add_bf8(x, y, t[j]);
      *(LAS u32x4*)(smem + L_KVT + tid * 16) = pack_xy(x, y);
    }
    fpk.keep();
    lbar();
    {
      bf16x8 ka[2][2], za[2];
#pragma unroll
      for (int w = 0; w < 2; ++w) {
        ka[w][0] = ld_b128(smem + L_KVT + hh * 4096 + w * 2048 + lane * 16);
        ka[w][1] = ld_b128(smem + L_KVT + hh * 4096 + w * 2048 + 1024 + lane * 16);
        za[w] = ld_b128(smem + L_KVT + 8192 + hh * 128 + w * 64 + g * 16);
      }
#pragma unroll
      for (int t = 0; t < 2; ++t) {
        f32x4 o0 = {0.f, 0.f, 0.f, 0.f}, o1 = o0, dn = o0;
#pragma unroll
        for (int w = 0; w < 2; ++w) { dn = MFMA16(za[w], pf[t][w], dn); o0 = MFMA16(ka[w][0], pf[t][w], o0); o1 = MFMA16(ka[w][1], pf[t][w], o1); }
        const float rd = __builtin_amdgcn_rcpf(dn[0] + 1e-6f);
#pragma unroll
        for (int e_ = 0; e_ < 4; ++e_) { o0[e_] *= rd; o1[e_] *= rd; }
        swap_pair(o0, o1);
        st128(pack_xy(o0, o1), rws, WS_O + (tm * 32 + head) * 8192 + (rbase + t * 16) * 64 + cswz);
      }
    }
    wait_vm<0>();
    lbar();
    if (tid == 0) set_flag(ctl, C_O + tm, gp, tag);
  }

  relane();
  {
    FastPoll fp = {0u, 0u, 0u, 0u};
    if (loader) {
      load_wo<WO1, 16>(smem, mkrsrc(WO, D_ * D_ * 2), gp, lw, lane);
      colsum_tile<224>(smem + L_WT, rws, WS_CS3 + (gp * 16 + tm) * 896, lw, lane);
    }
    if (loader) {
      wait_vm<0>();
      bar();
      if (lane == 0) set_flag(ctl, C_CS3 + gp, tm * 4 + lw, tag);
    } else {
      const int row = tm * 128 + wid * 32 + r16, col = gp * 64 + cpair;
      u32x4 res[2][2];
#pragma unroll
      for (int i = 0; i < 2; ++i)
#pragma unroll
        for (int j = 0; j < 2; ++j) res[i][j] = *(const u32x4*)(X + (size_t)(row + i * 16) * D_ + col + j * 32);
      f32x4 acc[2][4];
#pragma unroll
      for (int j = 0; j < 8; ++j) { acc[j >> 2][j & 3] = (f32x4){0.f, 0.f, 0.f, 0.f}; asm volatile("" : "+v"(acc[j >> 2][j & 3])); }
      constexpr int PF = PFD, BD = BDEPTH;
      const int q = r16 >> 2, p = lane & 3;
      int bo[4];
#pragma unroll
      for (int j = 0; j < 4; ++j) bo[j] = (8 * g + q) * 128 + (((2 * j + (p >> 1)) ^ bswz<64>(8 * g + q)) * 16) + 8 * (p & 1);
      int ao = WS_O + tm * 262144 + wid * 2048 + lane * 16;
      u32x4 af[PF][2];
      s16x4 bf[BD][4][2];
      auto ldb = [&](int sl, int k) {
        bf[sl % BD][k >> 1][k & 1] = __builtin_amdgcn_ds_read_tr16_b64_v4i16((LAS s16x4*)(smem + wo_slot(sl >> 1) + bo[k >> 1] + (32 * (sl & 1) + 4 * (k & 1)) * 128));
      };
      asm volatile("" : "+v"(bo[0]), "+v"(bo[1]), "+v"(bo[2]), "+v"(bo[3]), "+v"(ao));
      if (wid == 0) {
        fp.run(ctl, C_O + tm, 0, 16, tag, lane);
        if (bid == 0 && lane == 0) __hip_atomic_store(ctl + C_EPOCH * 64, tag, __ATOMIC_RELAXED, __HIP_MEMORY_SCOPE_AGENT);
      }
      bar();
#pragma unroll
      for (int sl = 0; sl < PF; ++sl) { af[sl][0] = ld128(rws, ao, sl * 8192); af[sl][1] = ld128(rws, ao + 1024, sl * 8192); }
#pragma unroll
      for (int k = 0; k < 8 * (BD - 1); ++k) ldb(k >> 3, k & 7);
      __builtin_amdgcn_sched_barrier(0);
#pragma unroll
      for (int sl = 0; sl < 32; ++sl) {
#pragma unroll
        for (int m = 0; m < 8; ++m) {
          const int i = m >> 2, j = m & 3;
          mfma16(acc[i][j], __builtin_bit_cast(bf16x8, __builtin_shufflevector(bf[sl % BD][j][0], bf[sl % BD][j][1], 0, 1, 2, 3, 4, 5, 6, 7)),
                 __builtin_bit_cast(bf16x8, af[sl % PF][i]));
          if (sl + BD - 1 < 32) ldb(sl + BD - 1, m);
          if (j == 3 && sl + PF < 32) af[sl % PF][i] = ld128(rws, ao + i * 1024, (sl + PF) * 8192);
          __builtin_amdgcn_sched_barrier(0);
        }
        if (sl == 30) lbar();
      }
      fp.keep();
      mfma_drain();
#pragma unroll
      for (int j = 0; j < 8; ++j) asm volatile("" : "+v"(acc[j >> 2][j & 3]));
#pragma unroll
      for (int i = 0; i < 2; ++i) {
        float s = 0.f, qq = 0.f;
#pragma unroll
        for (int j = 0; j < 2; ++j) {
          f32x4 x = acc[i][2 * j], y = acc[i][2 * j + 1];
          swap_pair(x, y);
          add_bf8(x, y, res[i][j]);
          const u32x4 o = pack_xy(x, y);
          st128(o, rws, WS_H + (tm * 32 + gp * 2 + j) * 8192 + (wid * 32 + i * 16) * 64 + cswz);
          *(LAS u32x4*)(smem + j * 8192 + (wid * 32 + i * 16) * 64 + cswz) = o;
#pragma unroll
          for (int e = 0; e < 4; ++e) { const float lo = bflo(o[e]), hi = bfhi(o[e]); s += lo + hi; qq = __builtin_fmaf(lo, lo, __builtin_fmaf(hi, hi, qq)); }
        }
        red2(s, qq, [](float x, float y) { return x + y; });
        __builtin_amdgcn_raw_buffer_store_b64(__builtin_bit_cast(u32x2, (f32x2){s, qq}), rws,
                                              g == 0 ? WS_HST + ((tm * 16 + gp) * 128 + wid * 32 + i * 16 + r16) * 8 : 0x80000000u, 0, SC1);
      }
    }
  }

  const int kh = gp >> 3;
  if (loader) {
    __builtin_amdgcn_s_setprio(3);
    relane();
    const rsrc_t r2 = mkrsrc(W2, FF_ * D_ * 2);
    FastPoll fp;
    {
      Loader<224, 3, 16, false> L;
      L.init(smem, rws, mkrsrc(W1, D_ * FF_ * 2), WS_H + tm * 128 * D_ * 2, 0, gp * 448, FF_ * 2, 128, lw, lane, gp);
      __builtin_amdgcn_s_sleep(B3DELAY);
      L.issueB(0);
      bar();
      bar();
      if (wid == 4 && lane == 0) set_flag(ctl, C_H + tm, gp, tag);
      wait_vm<0>(); bar();
      L.issueB(1); L.issueB(2);
      poll4(fp, smem, 2, lw, ctl, C_H + tm, 0, 16, tag, lane);
      L.issueA(1); L.issueA(2);
      u32x4 gt[4], hs[4];
#pragma unroll
      for (int j = 0; j < 4; ++j) hs[j] = ld128(rws, WS_HST + tm * 16384 + lw * 4096 + lane * 16, j * 1024);
      const unsigned seen = peek(ctl, C_CS3 + gp, lane);
      L.tn = 3;
      L.step<4 + 5>();
      fp.keep();
      L.step<5 + 11>();
      confirm(seen, ctl, C_CS3 + gp, tag, lane);
#pragma unroll
      for (int j = 0; j < 4; ++j) gt[j] = ld128(rws, WS_CS3 + (gp * 16 + lw * 4) * 896 + lane * 16, j * 896);
      *(LAS f32x4*)(smem + L_CSS + 4096 + lw * 1024 + lane * 16) = sum4(hs);
      L.step();
      if (lw == 1) {
        const f32x4 sum = lds_sum4(smem + L_CSS + 4096 + lane * 16);
        const float m0 = sum[0] * (1.0f / D_), m1 = sum[2] * (1.0f / D_);
        const float v0 = __builtin_fmaxf(sum[1] * (1.0f / D_) - m0 * m0, 0.f), v1 = __builtin_fmaxf(sum[3] * (1.0f / D_) - m1 * m1, 0.f);
        *(LAS f32x4*)(smem + L_RST + lane * 16) = (f32x4){m0, __builtin_amdgcn_rsqf(v0 + LN_EPS), m1, __builtin_amdgcn_rsqf(v1 + LN_EPS)};
      }
      L.step();
      *(LAS f32x4*)(smem + L_CSS + lw * 1024 + lane * 16) = sum4(gt);
      L.step();
      if (lw == 0) *(LAS f32x4*)(smem + L_CS + lane * 16) = lds_sum4(smem + L_CSS + lane * 16);
      const int stag = (loc & 7) < 6 ? (loc & 7) : 6;
      L.steps(stag);
      touch_lines(smem, r2, bid * 28672, lane < 56 ? lw * 56 + lane : 4096, 224);
      L.step();
      L.step<12>();
      L.steps(6 - stag);
    }
    {
      relane();
      Loader<128, 4, 31, false, 1> L;
      L.kh = kh;
      L.init(smem, rws, r2, WS_H1 + tm * 112 * 8192, 0, gp * 128, D_ * 2, kh ? -1024 : 1024, lw, lane, gp & 7);
      L.issueB(0);
      wait_vm<11 + 4>(); lbar(); wait_vm<4>(); bar();
      bar();
      L.issueB(1); L.issueB(2); L.issueB(3);
      wait_vm<12>(); bar();
      if (wid == 4 && lane == 0) set_flag(ctl, C_H1 + tm, gp, tag);
      wait_vm<8>(); bar();
      wait_vm<4>(); bar();
      L.issueB(4);
      poll4(fp, smem, 3, lw, ctl, C_H1 + tm, kh * 8, 8, tag, lane);
      L.issueA(3); L.issueA(4);
      L.tn = 5;
      L.step<4>();
      fp.keep();
      L.steps(3);
      if (wid == 4 && lane == 0) set_flag(ctl, C_H1B + tm, gp, tag);
      L.steps(5);
      const unsigned seen = peek(ctl, C_H1B + tm, lane, 16);
      L.steps(3);
      confirm(seen, ctl, C_H1B + tm, tag, lane, 16);
      L.steps(8);
      L.init_half(gp * 128, D_ * 2, lw, lane);
      wait_vm<8>(); bar(); L.issueH(25);
      wait_vm<6>(); bar(); L.issueH(26);
#pragma unroll 1
      for (int t = 27; t <= 30; ++t) { wait_vm<6>(); bar(); L.issueH(t); }
      wait_vm<6>(); bar();
      wait_vm<0>(); bar();
      bar();
    }
  } else {
    const int wm = wid >> 1, wn = wid & 1;
    relane();
    wait_vm<0>();
    bar();
    {
      const int par = gp & 1;
      auto slab3 = [&](int a) { return wn * 4 + (a + par == 3 ? 0 : a + par); };
      auto ntile3 = [&](int j) { return j < 6 ? slab3(j >> 1) * 2 + (j & 1) : 6 + wn; };
      f32x4 acc[4][7];
      mma_loop<224, 3, 16, 4, 7, false, true>(smem, acc, wm * 64, lane, ntile3, []() {}, [](int) {});
      relane();
      f32x4 cs[7];
#pragma unroll
      for (int j = 0; j < 7; ++j) cs[j] = *(LAS const f32x4*)(smem + L_CS + (ntile3(j) * 16 + g * 4) * 4);
      float fa[4], fb[4];
#pragma unroll
      for (int i = 0; i < 4; ++i) {
        const f32x2 ms = *(LAS const f32x2*)(smem + L_RST + (wm * 64 + i * 16 + r16) * 8);
        fa[i] = ms[1]; fb[i] = -ms[0] * ms[1];
      }
      auto act = [&](int i, int j) {
        f32x4 y;
#pragma unroll
        for (int e = 0; e < 4; ++e) y[e] = __builtin_fmaxf(__builtin_fmaf(acc[i][j][e], fa[i], fb[i] * cs[j][e]), 0.f);
        return y;
      };
      const int hb = WS_H1 + (tm * 112 + gp * 7) * 8192;
#pragma unroll
      for (int a = 0; a < 3; ++a) {
        const int sl = slab3(a);
#pragma unroll
        for (int i = 0; i < 4; ++i) {
          f32x4 x = act(i, 2 * a), y = act(i, 2 * a + 1);
          swap_pair(x, y);
          const u32x4 o = pack_xy(x, y);
          const int ro = (wm * 64 + i * 16) * 64 + cswz;
          st128(o, rws, hb + sl * 8192 + ro);
          if (a < 2 || (unsigned)(sl - par) < 6u) *(LAS u32x4*)(smem + (sl - par) * 8192 + ro) = o;
        }
        asm volatile("" ::: "memory");
      }
#pragma unroll
      for (int i = 1; i < 4; i += 2) {
        f32x4 x = act(i - 1, 6), y = act(i, 6);
        swap_pair(x, y);
        const u32x4 o = pack_xy(x, y);
        const int r3 = (wm * 64 + (i - 1 + (g & 1)) * 16) * 64 + (2 * wn + (g >> 1)) * 256 + r16 * 16;
        st128(o, rws, hb + 3 * 8192 + r3);
        *(LAS u32x4*)(smem + (3 - par) * 8192 + r3) = o;
      }
      wait_vm<6>();
    }
    relane();
    {
      const int row = tm * 128 + wm * 64 + r16, col = gp * 64 + wn * 32 + cpair;
      u32x4 res[4], t[4];
      unsigned seen = 0;
      f32x4 acc[4][4];
      mma_loop<128, 4, 31, 4, 4, true, true>(smem, acc, wm * 64, lane, [&](int j) { return (j >> 1) * 4 + wn * 2 + (j & 1); }, [&]() {
        wait_vm<0>();
#pragma unroll
        for (int i = 0; i < 4; ++i) res[i] = ld128(rws, WS_H + (tm * 32 + gp * 2 + wn) * 8192 + (wm * 64 + i * 16) * 64 + cswz, 0);
      }, [&](int what) {
        if (what == 0) {
          const int pb = WS_P4 + (tm * 16 + gp) * 16384 + wid * 4096 + lane * 16;
#pragma unroll
          for (int i = 0; i < 4; ++i) st128(pack_xy(acc[i][2], acc[i][3]), rws, pb + i * 1024);
        } else if (what == 1) {
          wait_vm<0>();
          if (lane == 0) set_flag(ctl, C_P4 + tm, gp * 4 + wid, tag);
        } else if (what == 2) {
          seen = __hip_atomic_load(ctl + (C_P4 + tm) * 64 + (gp ^ 8) * 4 + wid, __ATOMIC_RELAXED, __HIP_MEMORY_SCOPE_AGENT);
        } else {
          asm volatile("" : "+v"(seen));
          if (!wave_all(seen == tag)) poll(ctl, C_P4 + tm, (gp ^ 8) * 4 + wid, 1, tag, lane);
          acquire();
          const int qb = WS_P4 + (tm * 16 + (gp ^ 8)) * 16384 + wid * 4096 + lane * 16;
#pragma unroll
          for (int i = 0; i < 4; ++i) t[i] = ld128(rws, qb + i * 1024, 0);
        }
      });
#pragma unroll
      for (int i = 0; i < 4; ++i) {
        f32x4 x = acc[i][0], y = acc[i][1];
        add_bf8(x, y, t[i]);
        swap_pair(x, y);
        add_bf8(x, y, res[i]);
        __builtin_amdgcn_raw_buffer_store_b128(pack_xy(x, y), mkrsrc(OUT, S_ * D_ * 2), ((row + i * 16) * D_ + col) * 2, 0, SC1 | 2);
      }
    }
  }
#endif
}
#if defined(__HIP_DEVICE_COMPILE__)
#pragma clang attribute pop
#endif

extern "C" void kernel_launch(void* const* d_in, const int* in_sizes, int n_in, void* d_out, int out_size, void* d_ws, size_t ws_size,
                              hipStream_t stream) {
  (void)hipFuncSetAttribute((const void*)block_kernel, hipFuncAttributeMaxDynamicSharedMemorySize, LDS_BYTES);
  block_kernel<<<256, 512, LDS_BYTES, stream>>>((const u16*)d_in[0], (const u16*)d_in[1], (const u16*)d_in[2], (const u16*)d_in[3],
                                                (const u16*)d_in[5], (const u16*)d_in[6], (u16*)d_out, (char*)d_ws);
}
```
